```python
import torch

def _softmax(x, axis=-1):
    return torch.softmax(x, dim=axis)

def _gelu(x):
    return torch.nn.functional.gelu(x, approximate='tanh')

def _silu(x):
    return torch.nn.functional.silu(x)

def _sigmoid(x):
    return torch.sigmoid(x)

def _layernorm(x, axis=-1, eps=1e-05):
    m = x.mean(dim=axis, keepdim=True)
    v = x.var(dim=axis, keepdim=True, unbiased=False)
    return (x - m) / torch.sqrt(v + eps)

def _scatter_add(tgt, idx, src, axis=0):
    return tgt.clone().index_add_(axis, idx, src)

def _segment_sum(src, seg, n):
    out = torch.zeros((n,) + src.shape[1:], dtype=src.dtype)
    return out.index_add_(0, seg, src)

def _pad(x, pw):
    flat = tuple((v for pair in reversed(pw) for v in pair))
    return torch.nn.functional.pad(x, flat)
kernel_name = 'adaLN_cond__grouped_kv_mqa__glu_gated_reglu__prompt'

def setup_inputs():

    _w = lambda a: (lambda t: t.to(torch.bfloat16) if t.is_floating_point() else t)(torch.as_tensor(a))

    def _A_setup():
        B, S, D, H = (1, 2048, 1024, 32)
        Dh = D // H
        return {'x': _w((lambda *s: torch.randn(*s, dtype=torch.bfloat16) * 0.05)(B, S, D)), 'wq': _w((lambda *s: torch.randn(*s, dtype=torch.bfloat16) * 0.05)(D, H * Dh)), 'wk': _w((lambda *s: torch.randn(*s, dtype=torch.bfloat16) * 0.05)(D, Dh)), 'wv': _w((lambda *s: torch.randn(*s, dtype=torch.bfloat16) * 0.05)(D, Dh)), 'wo': _w((lambda *s: torch.randn(*s, dtype=torch.bfloat16) * 0.05)(H * Dh, D))}
    _A_in = _A_setup()

    def _B_setup():
        B, S, D, F = (1, 2048, 1024, 3584)
        return {'x': _w((lambda *s: torch.randn(*s, dtype=torch.bfloat16) * 0.05)(B, S, D)), 'W_gate': _w((lambda *s: torch.randn(*s, dtype=torch.bfloat16) * 0.8)(D, F) * (2.0 / D) ** 0.5), 'W_up': _w((lambda *s: torch.randn(*s, dtype=torch.bfloat16) * 0.8)(D, F) * (2.0 / D) ** 0.5), 'W_down': _w((lambda *s: torch.randn(*s, dtype=torch.bfloat16) * 0.8)(F, D) * (1.0 / F) ** 0.5)}
    _B_in = _B_setup()
    return {'A_x': _A_in['x'], 'A_wq': _A_in['wq'], 'A_wk': _A_in['wk'], 'A_wv': _A_in['wv'], 'A_wo': _A_in['wo'], 'B_x': _B_in['x'], 'B_W_gate': _B_in['W_gate'], 'B_W_up': _B_in['W_up'], 'B_W_down': _B_in['W_down'], 'c': _w((lambda *s: torch.randn(*s, dtype=torch.bfloat16) * 0.1)(1, 1024) * 8.0), 'w_mod': _w((lambda *s: torch.randn(*s, dtype=torch.bfloat16) * 0.1)(1024, 6 * 1024) * 1024 ** (-0.5) * 8.0)}

def reference(A_x, A_wq, A_wk, A_wv, A_wo, B_x, B_W_gate, B_W_up, B_W_down, c, w_mod):

    def _A_ref(x, wq, wk, wv, wo):
        B, S, D = x.shape
        H = 32
        Dh = D // H
        q = (lambda x, *d: x.permute(*d))((x @ wq).reshape(B, S, H, Dh), 0, 2, 1, 3)
        k = x @ wk
        v = x @ wv
        scores = torch.einsum('bhqd,bkd->bhqk', q, k) * Dh ** (-0.5)
        p = _softmax(scores, axis=-1)
        o = torch.einsum('bhqk,bkd->bhqd', p, v)
        return (lambda x, *d: x.permute(*d))(o, 0, 2, 1, 3).reshape(B, S, H * Dh) @ wo

    def _B_ref(x, W_gate, W_up, W_down):
        gate = torch.einsum('bsd,df->bsf', x, W_gate)
        up = torch.einsum('bsd,df->bsf', x, W_up)
        gate = gate * (lambda x, dt: x.to(dt))(gate > 0, gate.dtype)
        h = gate * up
        return torch.einsum('bsf,fd->bsd', h, W_down)
    x = A_x
    mod = (_silu(c) @ w_mod).reshape(c.shape[0], 6, 1, -1)
    sa, ca, ga, sb, cb, gb = (mod[:, 0].clone(), mod[:, 1].clone(), mod[:, 2].clone(), mod[:, 3].clone(), mod[:, 4].clone(), mod[:, 5].clone())
    h = _layernorm(x, axis=-1) * (1 + ca) + sa
    x = x + ga * _A_ref(h, A_wq, A_wk, A_wv, A_wo)
    h = _layernorm(x, axis=-1) * (1 + cb) + sb
    x = x + gb * _B_ref(h, B_W_gate, B_W_up, B_W_down)
    return x
```

```cpp
#include <hip/hip_runtime.h>
#include <hip/hip_bf16.h>

#define LAS __attribute__((address_space(3)))
#define GAS __attribute__((address_space(1)))
typedef unsigned short u16;
typedef short bf16x8 __attribute__((ext_vector_type(8)));
typedef short s16x4 __attribute__((ext_vector_type(4)));
typedef float f32x4 __attribute__((ext_vector_type(4)));
typedef float f32x16 __attribute__((ext_vector_type(16)));
typedef unsigned u32x4 __attribute__((ext_vector_type(4)));
typedef unsigned u32x2 __attribute__((ext_vector_type(2)));
typedef LAS char* lds_ptr;

constexpr int S = 2048, D = 1024, F = 3584;
constexpr size_t WS_FLAGS = 0;
constexpr size_t WS_MODP = 65536;
constexpr size_t WS_KN = WS_MODP + 4 * 6144 * 4;
constexpr size_t WS_VM = WS_KN + 8192;
constexpr size_t WS_KV = 262144;
constexpr size_t WS_H1 = 1u << 20;
constexpr size_t WS_O = WS_H1 + (4u << 20);
constexpr size_t WS_X1 = WS_O + (4u << 20);
constexpr size_t WS_Y = WS_X1 + (4u << 20);
constexpr size_t WS_HH = WS_Y + (4u << 20);
constexpr size_t WS_XCH = WS_HH + (16u << 20);


__device__ __forceinline__ float bf2f(u16 b) { return __uint_as_float(((unsigned)b) << 16); }
__device__ __forceinline__ float bflo(unsigned w) { return __uint_as_float(w << 16); }
__device__ __forceinline__ float bfhi(unsigned w) { return __uint_as_float(w & 0xffff0000u); }
typedef float f32x2 __attribute__((ext_vector_type(2)));
typedef __bf16 bf16x2v __attribute__((ext_vector_type(2)));
__device__ __forceinline__ unsigned cvtpk(float lo, float hi) { return __builtin_bit_cast(unsigned, __builtin_convertvector((f32x2){lo, hi}, bf16x2v)); }
__device__ __forceinline__ float rbf(float x) { return bflo(cvtpk(x, x)); }

__device__ __forceinline__ void dma16(const void* gbase, unsigned voff, unsigned ldsaddr) {
    asm volatile("s_mov_b32 m0, %0\n\ts_nop 0\n\tglobal_load_lds_dwordx4 %1, %2" ::"s"(ldsaddr), "v"(voff), "s"(gbase) : "memory");
}
#define WAIT_V(n) asm volatile("s_waitcnt vmcnt(" #n ")" ::: "memory")
#define BARRIER() asm volatile("s_barrier" ::: "memory")
#define WAITV_BAR(n) asm volatile("s_waitcnt vmcnt(" #n ")\n\ts_barrier" ::: "memory")
#define WAITVL_BAR(n) asm volatile("s_waitcnt vmcnt(" #n ") lgkmcnt(0)\n\ts_barrier" ::: "memory")
#define SBAR() __builtin_amdgcn_sched_barrier(0)
#define MFMA32(a, b, c) __builtin_amdgcn_mfma_f32_32x32x16_bf16(a, b, c, 0, 0, 0)
#define MFMA16(a, b, c) __builtin_amdgcn_mfma_f32_16x16x32_bf16(a, b, c, 0, 0, 0)
__device__ __forceinline__ bf16x8 ldsr128(lds_ptr p) { return *(const LAS bf16x8*)p; }
__device__ __forceinline__ s16x4 ldstr(lds_ptr p) { return __builtin_amdgcn_ds_read_tr16_b64_v4i16((LAS s16x4*)p); }
__device__ __forceinline__ bf16x8 ldstr2(lds_ptr p, int d) { s16x4 a = ldstr(p), b = ldstr(p + d); return (bf16x8){a[0], a[1], a[2], a[3], b[0], b[1], b[2], b[3]}; }
__device__ __forceinline__ unsigned uni(unsigned x) { return (unsigned)__builtin_amdgcn_readfirstlane((int)x); }

__global__ __launch_bounds__(256) void k_mod(const u16* __restrict__ c, const u16* __restrict__ wmod, char* __restrict__ ws) {
    __shared__ float red[4][64];
    const int tid = threadIdx.x, cs = blockIdx.x % 96, kq = blockIdx.x / 96;
    if (blockIdx.x < 64) ((unsigned*)(ws + WS_FLAGS))[blockIdx.x * 256 + tid] = 0u;
    const int r = tid >> 3, ch = tid & 7;
    u32x4 w[8];
#pragma unroll
    for (int p = 0; p < 8; ++p) w[p] = *(const u32x4*)(wmod + (size_t)(kq * 256 + p * 32 + r) * 6144 + cs * 64 + ch * 8);
    float acc[8] = {0.f, 0.f, 0.f, 0.f, 0.f, 0.f, 0.f, 0.f};
#pragma unroll
    for (int p = 0; p < 8; ++p) {
        const float cv = bf2f(c[kq * 256 + p * 32 + r]);
        const float s = rbf(cv / (1.f + __expf(-cv)));
#pragma unroll
        for (int j = 0; j < 4; ++j) { acc[2 * j] += s * bflo(w[p][j]); acc[2 * j + 1] += s * bfhi(w[p][j]); }
    }
#pragma unroll
    for (int j = 0; j < 8; ++j) { acc[j] += __shfl_xor(acc[j], 8); acc[j] += __shfl_xor(acc[j], 16); acc[j] += __shfl_xor(acc[j], 32); }
    if ((tid & 63) < 8) {
#pragma unroll
        for (int j = 0; j < 8; ++j) red[tid >> 6][ch * 8 + j] = acc[j];
    }
    __syncthreads();
    if (tid < 64) ((float*)(ws + WS_MODP))[kq * 6144 + cs * 64 + tid] = (red[0][tid] + red[1][tid]) + (red[2][tid] + red[3][tid]);
}

__device__ __forceinline__ f32x4 mod4(const char* ws, int col) {
    const float* p = (const float*)(ws + WS_MODP) + col;
    const f32x4 a = *(const f32x4*)p, b = *(const f32x4*)(p + 6144), c = *(const f32x4*)(p + 2 * 6144), d = *(const f32x4*)(p + 3 * 6144);
    const f32x4 s = (a + b) + (c + d);
    return (f32x4){rbf(s[0]), rbf(s[1]), rbf(s[2]), rbf(s[3])};
}
__device__ __forceinline__ float wsum(float v) {
#pragma unroll
    for (int o = 1; o < 64; o <<= 1) v += __shfl_xor(v, o);
    return v;
}

__device__ __forceinline__ void ln_rows_to_lds(const u32x4 (&xv)[2][2], lds_ptr img, const LAS float* tab, int wid, int lane) {
#pragma unroll
    for (int rr = 0; rr < 2; ++rr) {
        float v[16];
#pragma unroll
        for (int hh = 0; hh < 2; ++hh)
#pragma unroll
            for (int j = 0; j < 4; ++j) { v[hh * 8 + 2 * j] = bflo(xv[rr][hh][j]); v[hh * 8 + 2 * j + 1] = bfhi(xv[rr][hh][j]); }
        float s = 0.f;
#pragma unroll
        for (int j = 0; j < 16; ++j) s += v[j];
        const float mean = wsum(s) * (1.f / 1024.f);
        float q = 0.f;
#pragma unroll
        for (int j = 0; j < 16; ++j) { v[j] -= mean; q += v[j] * v[j]; }
        const float rstd = 1.f / sqrtf(wsum(q) * (1.f / 1024.f) + 1e-5f);
        const int row = 2 * wid + rr;
#pragma unroll
        for (int hh = 0; hh < 2; ++hh) {
            const int c = hh * 64 + lane;
            const f32x4 g0 = *(const LAS f32x4*)(tab + c * 8), g1 = *(const LAS f32x4*)(tab + c * 8 + 4);
            const f32x4 b0 = *(const LAS f32x4*)(tab + 1024 + c * 8), b1 = *(const LAS f32x4*)(tab + 1024 + c * 8 + 4);
            float h[8];
#pragma unroll
            for (int j = 0; j < 4; ++j) { h[j] = v[hh * 8 + j] * rstd * g0[j] + b0[j]; h[4 + j] = v[hh * 8 + 4 + j] * rstd * g1[j] + b1[j]; }
            const u32x4 o = {cvtpk(h[0], h[1]), cvtpk(h[2], h[3]), cvtpk(h[4], h[5]), cvtpk(h[6], h[7])};
            *(LAS u32x4*)(img + c * 256 + ((row ^ (c & 15)) << 4)) = o;
        }
    }
}

__global__ __launch_bounds__(512) void k_ln1kv(const u16* __restrict__ x, const u16* __restrict__ wk, const u16* __restrict__ wv, char* __restrict__ ws) {
    extern __shared__ __attribute__((aligned(16))) char smem[];
    lds_ptr lds = (lds_ptr)smem;
    const int tid = threadIdx.x, lane = tid & 63, wid = uni(tid >> 6);
    const int bid = blockIdx.x, rb = (bid >> 4) * 8 + (bid & 7), isv = (bid >> 3) & 1;
    const unsigned lds0 = uni((unsigned)(uintptr_t)lds);
    {
        const u16* w = isv ? wv : wk;
        const unsigned voff = (lane >> 2) * 64 + (((lane & 3) ^ (((lane >> 5) & 1) << 1)) << 4);
#pragma unroll
        for (int i = 0; i < 8; ++i) dma16((const char*)w + (wid * 8 + i) * 1024, voff, lds0 + (wid * 8 + i) * 1024);
    }
    u32x4 xv[2][2];
#pragma unroll
    for (int rr = 0; rr < 2; ++rr)
#pragma unroll
        for (int hh = 0; hh < 2; ++hh) xv[rr][hh] = *(const u32x4*)(x + (size_t)(rb * 16 + 2 * wid + rr) * D + hh * 512 + lane * 8);
    LAS float* tab = (LAS float*)(lds + 98304);
    {
        const int t4 = tid * 4;
        if (t4 < 1024) { f32x4 v = mod4(ws, 1024 + t4); *(LAS f32x4*)(tab + t4) = (f32x4){rbf(1.f + v[0]), rbf(1.f + v[1]), rbf(1.f + v[2]), rbf(1.f + v[3])}; }
        else { *(LAS f32x4*)(tab + t4) = mod4(ws, t4 - 1024); }
    }
    __syncthreads();
    lds_ptr img = lds + 65536;
    ln_rows_to_lds(xv, img, tab, wid, lane);
    WAIT_V(0);
    __syncthreads();
    f32x4 acc[2] = {{0.f, 0.f, 0.f, 0.f}, {0.f, 0.f, 0.f, 0.f}};
    const int g = lane >> 4, q = (lane & 15) >> 2, p = lane & 3;
#pragma unroll
    for (int i = 0; i < 4; ++i) {
        const int s = wid * 4 + i, c = 4 * s + g;
        const bf16x8 hf = ldsr128(img + c * 256 + (((lane & 15) ^ (c & 15)) << 4));
        if ((i & 1) == isv) *(bf16x8*)(ws + WS_H1 + ((size_t)rb * 128 + 4 * s) * 256 + lane * 16) = hf;
#pragma unroll
        for (int nf = 0; nf < 2; ++nf) {
            const bf16x8 wf = ldstr2(lds + (32 * s + 8 * g + q) * 64 + ((nf ^ (g & 1)) << 5) + 8 * p, 256);
            acc[nf] = MFMA16(wf, hf, acc[nf]);
        }
    }
    LAS float* red = (LAS float*)(lds + 106496);
#pragma unroll
    for (int nf = 0; nf < 2; ++nf) *(LAS f32x4*)(red + wid * 512 + (lane & 15) * 32 + nf * 16 + 4 * g) = acc[nf];
    __syncthreads();
    {
        const int key = tid >> 5, d = tid & 31;
        float v = 0.f;
#pragma unroll
        for (int w = 0; w < 8; ++w) v += red[w * 512 + tid];
        const unsigned vb = cvtpk(v, v) & 0xffffu;
        const float vr = bflo(vb);
        const int T = rb >> 1, hf = rb & 1;
        u16* kv = (u16*)(ws + WS_KV + (size_t)T * 4096);
        if (!isv) {
            const int s = d >> 4, dd = d & 15, j = ((dd >> 3) << 2) | (dd & 3), h = (dd >> 2) & 1;
            kv[s * 512 + (h * 32 + hf * 16 + key) * 8 + j] = (u16)vb;
            float n2 = vr * vr;
#pragma unroll
            for (int o = 1; o < 32; o <<= 1) n2 += __shfl_xor(n2, o);
            if (d == 0) ((float*)(ws + WS_KN))[rb * 16 + key] = n2;
        } else {
            const int j = ((key >> 3) << 2) | (key & 3), h = (key >> 2) & 1;
            kv[1024 + hf * 512 + (h * 32 + d) * 8 + j] = (u16)vb;
            float m = fabsf(vr);
#pragma unroll
            for (int o = 1; o < 32; o <<= 1) m = fmaxf(m, __shfl_xor(m, o));
            if (d == 0) ((float*)(ws + WS_VM))[rb * 16 + key] = m;
        }
    }
}

__device__ __forceinline__ f32x16 gemm128x64(const char* Ap  , const char* Wp  , lds_ptr lds, unsigned lds0, int wid, int lane) {
    const unsigned voffA = (unsigned)((2 * (wid >> 1) + ((lane >> 4) & 1)) * 128 * 256 + (4 * (wid & 1) + (lane >> 5)) * 256 + (lane & 15) * 16);
    const unsigned voffW = (unsigned)((8 * wid + (lane >> 3)) * 2048 + (((lane & 7) ^ (((lane >> 4) & 1) << 2)) << 4));
    const unsigned dA = lds0 + ((wid >> 1) * 4 + 2 * (wid & 1)) * 1024, dW = lds0 + 16384 + wid * 1024;
#define G64_STAGE(t, slot) do { const char* a_ = Ap + (size_t)(t) * 2048; dma16(a_, voffA, dA + (slot) * 24576); dma16(a_ + 512, voffA, dA + (slot) * 24576 + 1024); \
        dma16(Wp + (size_t)(t) * 131072, voffW, dW + (slot) * 24576); } while (0)
    G64_STAGE(0, 0); G64_STAGE(1, 1); G64_STAGE(2, 2);
    const int tb = wid & 3, nh = wid >> 2, h = lane >> 5, blk = (lane >> 4) & 1, q = (lane & 15) >> 2, p = lane & 3;
    const lds_ptr pa = lds + tb * 4096 + lane * 16;
    const lds_ptr pw = lds + 16384 + (8 * h + q) * 128 + ((nh ^ ((q >> 1) & 1)) << 6) + blk * 32 + p * 8;
    f32x16 acc = {};
#pragma unroll
    for (int t = 0; t < 16; ++t) {
        if (t < 14) { WAITVL_BAR(6); } else if (t == 14) { WAITVL_BAR(3); } else { WAITVL_BAR(0); }
        if (t + 3 < 16) G64_STAGE(t + 3, (t + 3) & 3);
        const int so = (t & 3) * 24576;
        bf16x8 af[4], wf[4];
#pragma unroll
        for (int ks = 0; ks < 4; ++ks) { wf[ks] = ldstr2(pw + so + ks * 2048, 512); af[ks] = ldsr128(pa + so + ks * 1024); }
#pragma unroll
        for (int ks = 0; ks < 4; ++ks) acc = MFMA32(wf[ks], af[ks], acc);
    }
#undef G64_STAGE
    return acc;
}

__global__ __launch_bounds__(512, 2) void k_attn(const u16* __restrict__ wq, char* __restrict__ ws) {
    extern __shared__ __attribute__((aligned(16))) char smem[];
    lds_ptr lds = (lds_ptr)smem;
    const int tid = threadIdx.x, lane = tid & 63, wid = uni(tid >> 6);
    const int xcd = blockIdx.x & 7, idx = blockIdx.x >> 3, rb = (xcd >> 1) * 4 + (idx & 3), hp = (xcd & 1) * 8 + (idx >> 2);
    const unsigned lds0 = uni((unsigned)(uintptr_t)lds);
    const char* kvg = ws + WS_KV;
    const unsigned voffL = lane * 16;
#define KV_SLOT(st) ((((st) % 5) < 2) ? (98304 + ((st) % 5) * 32768) : ((((st) % 5) - 2) * 32768))
#define KV_STAGE(st) do { _Pragma("unroll") for (int i_ = 0; i_ < 4; ++i_) dma16(kvg + (st) * 32768 + (wid * 4 + i_) * 1024, voffL, lds0 + KV_SLOT(st) + (wid * 4 + i_) * 1024); } while (0)
    const f32x4 kn4 = *(const f32x4*)(ws + WS_KN + tid * 16), vm4 = *(const f32x4*)(ws + WS_VM + tid * 16);
    float knm = fmaxf(fmaxf(kn4[0], kn4[1]), fmaxf(kn4[2], kn4[3])), vmm = fmaxf(fmaxf(vm4[0], vm4[1]), fmaxf(vm4[2], vm4[3]));
#pragma unroll
    for (int o = 1; o < 64; o <<= 1) { knm = fmaxf(knm, __shfl_xor(knm, o)); vmm = fmaxf(vmm, __shfl_xor(vmm, o)); }
    {
        LAS float* t = (LAS float*)lds;
        if (lane == 0) { t[wid] = knm; t[8 + wid] = vmm; }
        __syncthreads();
#pragma unroll
        for (int w = 0; w < 8; ++w) { knm = fmaxf(knm, t[w]); vmm = fmaxf(vmm, t[8 + w]); }
        __syncthreads();
    }
    KV_STAGE(0); KV_STAGE(1);
    f32x16 qa = gemm128x64(ws + WS_H1 + (size_t)rb * 8 * 128 * 256, (const char*)wq + hp * 128, lds, lds0, wid, lane);
    WAITVL_BAR(0);
    KV_STAGE(2); KV_STAGE(3); KV_STAGE(4);
    constexpr float QS = 0.17677669529663687f * 1.4426950408889634f;
    u32x4 qw0, qw1;
#pragma unroll
    for (int j = 0; j < 4; ++j) { qw0[j] = cvtpk(qa[2 * j] * QS, qa[2 * j + 1] * QS); qw1[j] = cvtpk(qa[8 + 2 * j] * QS, qa[8 + 2 * j + 1] * QS); }
    float qn = 0.f;
#pragma unroll
    for (int j = 0; j < 4; ++j) { qn += bflo(qw0[j]) * bflo(qw0[j]) + bfhi(qw0[j]) * bfhi(qw0[j]) + bflo(qw1[j]) * bflo(qw1[j]) + bfhi(qw1[j]) * bfhi(qw1[j]); }
    qn += __shfl_xor(qn, 32);
    const bool lane_ok = (qn * knm <= 88.f * 88.f) && (vmm <= 16777216.f) && (vmm >= 9.5367431640625e-7f);
    const bool fast = __all(lane_ok);
    const bf16x8 q0 = __builtin_bit_cast(bf16x8, qw0), q1 = __builtin_bit_cast(bf16x8, qw1);
    f32x16 o = {};
    float linv;
    const lds_ptr pl = lds + lane * 16;
    if (fast) {
        const short one = (short)0x3f80;
        const short ov = (((lane & 15) == 0 && ((lane >> 4) & 1) == 0) || ((lane & 15) == 1 && ((lane >> 4) & 1) == 1)) ? one : (short)0;
        const bf16x8 sel = {ov, ov, ov, ov, ov, ov, ov, ov};
        f32x4 ls = {0.f, 0.f, 0.f, 0.f};
        const f32x16 zero = {};
#pragma unroll
        for (int st = 0; st < 8; ++st) {
            if (st <= 4) { WAITVL_BAR(12); } else if (st == 5) { WAITVL_BAR(8); } else if (st == 6) { WAITVL_BAR(4); } else { WAITVL_BAR(0); }
            if (st >= 1 && st + 4 < 8) KV_STAGE(st + 4);
            const lds_ptr pb = pl + KV_SLOT(st);
#pragma unroll 2
            for (int t = 0; t < 8; ++t) {
                const bf16x8 k0 = ldsr128(pb + t * 4096), k1 = ldsr128(pb + t * 4096 + 1024), v0 = ldsr128(pb + t * 4096 + 2048), v1 = ldsr128(pb + t * 4096 + 3072);
                f32x16 s = MFMA32(k0, q0, zero);
                s = MFMA32(k1, q1, s);
                u32x4 p0, p1;
#pragma unroll
                for (int j = 0; j < 4; ++j) {
                    p0[j] = cvtpk(__builtin_amdgcn_exp2f(s[2 * j]), __builtin_amdgcn_exp2f(s[2 * j + 1]));
                    p1[j] = cvtpk(__builtin_amdgcn_exp2f(s[8 + 2 * j]), __builtin_amdgcn_exp2f(s[8 + 2 * j + 1]));
                }
                const bf16x8 pf0 = __builtin_bit_cast(bf16x8, p0), pf1 = __builtin_bit_cast(bf16x8, p1);
                o = MFMA32(v0, pf0, o);
                ls = MFMA16(sel, pf0, ls);
                o = MFMA32(v1, pf1, o);
                ls = MFMA16(sel, pf1, ls);
            }
        }
        const float la = __shfl(ls[0], lane & 15), lb = __shfl(ls[1], lane & 15);
        linv = 1.f / ((lane & 16) ? lb : la);
    } else {
        float m = -INFINITY, l = 0.f;
        const f32x16 zero = {};
#pragma unroll
        for (int st = 0; st < 8; ++st) {
            if (st <= 4) { WAITVL_BAR(12); } else if (st == 5) { WAITVL_BAR(8); } else if (st == 6) { WAITVL_BAR(4); } else { WAITVL_BAR(0); }
            if (st >= 1 && st + 4 < 8) KV_STAGE(st + 4);
            const lds_ptr pb = pl + KV_SLOT(st);
#pragma unroll 1
            for (int t = 0; t < 8; ++t) {
                const bf16x8 k0 = ldsr128(pb + t * 4096), k1 = ldsr128(pb + t * 4096 + 1024), v0 = ldsr128(pb + t * 4096 + 2048), v1 = ldsr128(pb + t * 4096 + 3072);
                f32x16 s = MFMA32(k0, q0, zero);
                s = MFMA32(k1, q1, s);
                float tm = s[0];
#pragma unroll
                for (int j = 1; j < 16; ++j) tm = fmaxf(tm, s[j]);
                tm = fmaxf(tm, __shfl_xor(tm, 32));
                const float mn = fmaxf(m, tm), al = __builtin_amdgcn_exp2f(m - mn);
                m = mn; l *= al;
#pragma unroll
                for (int j = 0; j < 16; ++j) o[j] *= al;
                u32x4 p0, p1;
#pragma unroll
                for (int j = 0; j < 4; ++j) {
                    p0[j] = cvtpk(__builtin_amdgcn_exp2f(s[2 * j] - mn), __builtin_amdgcn_exp2f(s[2 * j + 1] - mn));
                    p1[j] = cvtpk(__builtin_amdgcn_exp2f(s[8 + 2 * j] - mn), __builtin_amdgcn_exp2f(s[8 + 2 * j + 1] - mn));
                    l += (bflo(p0[j]) + bfhi(p0[j])) + (bflo(p1[j]) + bfhi(p1[j]));
                }
                o = MFMA32(v0, __builtin_bit_cast(bf16x8, p0), o);
                o = MFMA32(v1, __builtin_bit_cast(bf16x8, p1), o);
            }
        }
        l += __shfl_xor(l, 32);
        linv = 1.f / l;
    }
#undef KV_STAGE
#undef KV_SLOT
    {
        const int tb = wid & 3, head = hp * 2 + (wid >> 2), h = lane >> 5, tok = lane & 31;
        unsigned w[8];
#pragma unroll
        for (int j = 0; j < 8; ++j) w[j] = cvtpk(o[2 * j] * linv, o[2 * j + 1] * linv);
        char* ob = ws + WS_O + ((size_t)(rb * 8 + tb * 2 + (tok >> 4)) * 128 + head * 4 + h) * 256 + (tok & 15) * 16;
#pragma unroll
        for (int gp = 0; gp < 2; ++gp) {
            const auto r0 = __builtin_amdgcn_permlane32_swap(w[4 * gp], w[4 * gp + 2], false, false);
            const auto r1 = __builtin_amdgcn_permlane32_swap(w[4 * gp + 1], w[4 * gp + 3], false, false);
            *(u32x4*)(ob + gp * 512) = (u32x4){r0[0], r1[0], r0[1], r1[1]};
        }
    }
}

__global__ __launch_bounds__(512, 2) void k_oproj(const u16* __restrict__ x, const u16* __restrict__ wo, char* __restrict__ ws) {
    extern __shared__ __attribute__((aligned(16))) char smem[];
    lds_ptr lds = (lds_ptr)smem;
    const int tid = threadIdx.x, lane = tid & 63, wid = uni(tid >> 6);
    const int xcd = blockIdx.x & 7, idx = blockIdx.x >> 3, rb = (xcd >> 1) * 4 + (idx & 3), ct = (xcd & 1) * 8 + (idx >> 2);
    const unsigned lds0 = uni((unsigned)(uintptr_t)lds);
    const int tb = wid & 3, nh = wid >> 2, h = lane >> 5, row = rb * 128 + tb * 32 + (lane & 31), col0 = ct * 64 + nh * 32 + 4 * h;
    u32x2 xv[4]; f32x4 ga[4];
#pragma unroll
    for (int G = 0; G < 4; ++G) { xv[G] = *(const u32x2*)(x + (size_t)row * D + col0 + 8 * G); ga[G] = mod4(ws, 2048 + col0 + 8 * G); }
    const f32x16 acc = gemm128x64(ws + WS_O + (size_t)rb * 8 * 128 * 256, (const char*)wo + ct * 128, lds, lds0, wid, lane);
    u16* x1 = (u16*)(ws + WS_X1);
#pragma unroll
    for (int G = 0; G < 4; ++G) {
        const float a = bflo(xv[G][0]) + ga[G][0] * acc[4 * G], b = bfhi(xv[G][0]) + ga[G][1] * acc[4 * G + 1];
        const float c = bflo(xv[G][1]) + ga[G][2] * acc[4 * G + 2], d = bfhi(xv[G][1]) + ga[G][3] * acc[4 * G + 3];
        *(u32x2*)(x1 + (size_t)row * D + col0 + 8 * G) = (u32x2){cvtpk(a, b), cvtpk(c, d)};
    }
}

__global__ __launch_bounds__(512) void k_ln2(char* __restrict__ ws) {
    __shared__ __attribute__((aligned(16))) char smem[32768 + 8192];
    lds_ptr lds = (lds_ptr)smem;
    const int tid = threadIdx.x, lane = tid & 63, wid = uni(tid >> 6), rb = blockIdx.x;
    const u16* x1 = (const u16*)(ws + WS_X1);
    u32x4 xv[2][2];
#pragma unroll
    for (int rr = 0; rr < 2; ++rr)
#pragma unroll
        for (int hh = 0; hh < 2; ++hh) xv[rr][hh] = *(const u32x4*)(x1 + (size_t)(rb * 16 + 2 * wid + rr) * D + hh * 512 + lane * 8);
    LAS float* tab = (LAS float*)(lds + 32768);
    {
        const int t4 = tid * 4;
        if (t4 < 1024) { f32x4 v = mod4(ws, 4096 + t4); *(LAS f32x4*)(tab + t4) = (f32x4){rbf(1.f + v[0]), rbf(1.f + v[1]), rbf(1.f + v[2]), rbf(1.f + v[3])}; }
        else { *(LAS f32x4*)(tab + t4) = mod4(ws, 3072 + t4 - 1024); }
    }
    __syncthreads();
    ln_rows_to_lds(xv, lds, tab, wid, lane);
    __syncthreads();
#pragma unroll
    for (int i = 0; i < 4; ++i) {
        const int s = wid * 4 + i, c = 4 * s + (lane >> 4);
        *(bf16x8*)(ws + WS_Y + ((size_t)rb * 128 + 4 * s) * 256 + lane * 16) = ldsr128(lds + c * 256 + (((lane & 15) ^ (c & 15)) << 4));
    }
}

constexpr int GNS = 5;
__global__ __launch_bounds__(512, 2) void k_glu(const u16* __restrict__ wg, const u16* __restrict__ wu, char* __restrict__ ws) {
    extern __shared__ __attribute__((aligned(16))) char smem[];
    lds_ptr lds = (lds_ptr)smem;
    const int tid = threadIdx.x, lane = tid & 63, wid = uni(tid >> 6);
    const int xcd = blockIdx.x & 7, idx = blockIdx.x >> 3, rp = (xcd & 1) * 4 + (idx & 3), ct = (xcd >> 1) * 7 + (idx >> 2);
    const unsigned lds0 = uni((unsigned)(uintptr_t)lds);
    const int wm = wid >> 2, wn = wid & 3, g = lane >> 4, q = (lane & 15) >> 2, p = lane & 3;
    const char* Ab = ws + WS_Y + (size_t)(rp * 16 + 2 * wid) * 128 * 256;
    const unsigned voffA = lane * 16;
    const int kr = 4 * wid + g;
    const unsigned voffW = (unsigned)(kr * (F * 2) + (((lane & 15) ^ (((kr & 3) | (((kr >> 3) & 1) << 2)) << 1)) << 4));
    const char* Gb = (const char*)wg + ct * 256;
    const char* Ub = (const char*)wu + ct * 256;
    const unsigned dA = lds0 + wid * 2048, dW = lds0 + 16384 + wid * 1024;
#define GLU_STAGE(t, so) do { const char* a_ = Ab + (size_t)(t) * 1024; dma16(a_, voffA, dA + (so)); dma16(a_ + 32768, voffA, dA + (so) + 1024); \
        dma16(Gb + (size_t)(t) * (32 * F * 2), voffW, dW + (so)); dma16(Ub + (size_t)(t) * (32 * F * 2), voffW, dW + (so) + 8192); } while (0)
#pragma unroll
    for (int t = 0; t < GNS - 1; ++t) GLU_STAGE(t, t * 32768);
    const int fl = q | ((g & 1) << 2);
    const lds_ptr pa = lds + wm * 8192 + lane * 16;
    const lds_ptr pw0 = lds + 16384 + (8 * g + q) * 256 + (((2 * wn) ^ fl) << 5) + 8 * p;
    const lds_ptr pw1 = lds + 16384 + (8 * g + q) * 256 + (((2 * wn + 1) ^ fl) << 5) + 8 * p;
    f32x4 acc[8][4];
#pragma unroll
    for (int m = 0; m < 8; ++m)
#pragma unroll
        for (int n = 0; n < 4; ++n) acc[m][n] = (f32x4){0.f, 0.f, 0.f, 0.f};
    bf16x8 ya[8], wA[4], wB[4];
    WAITV_BAR(12);
    wA[0] = ldstr2(pw0, 1024); wA[1] = ldstr2(pw1, 1024); wA[2] = ldstr2(pw0 + 8192, 1024); wA[3] = ldstr2(pw1 + 8192, 1024);
#pragma unroll
    for (int m = 0; m < 8; ++m) ya[m] = ldsr128(pa + m * 1024);
    int sn = 32768;
    int sf = (GNS - 1) * 32768;
#define GLU_STEP(WC, WN, t, VM, DO_STAGE, DO_LOAD) do { \
        WAITV_BAR(VM); SBAR(); \
        _Pragma("unroll") for (int m = 0; m < 8; ++m) { \
            _Pragma("unroll") for (int n = 0; n < 4; ++n) acc[m][n] = MFMA16(WC[n], ya[m], acc[m][n]); \
            if (DO_LOAD) { ya[m] = ldsr128(pa + sn + m * 1024); \
                if (m == 0) WN[0] = ldstr2(pw0 + sn, 1024); if (m == 1) WN[1] = ldstr2(pw1 + sn, 1024); \
                if (m == 2) WN[2] = ldstr2(pw0 + sn + 8192, 1024); if (m == 3) WN[3] = ldstr2(pw1 + sn + 8192, 1024); } \
            if (DO_STAGE) { const char* a_ = Ab + (size_t)((t) + GNS - 1) * 1024; \
                if (m == 4) dma16(a_, voffA, dA + sf); if (m == 5) dma16(a_ + 32768, voffA, dA + sf + 1024); \
                if (m == 6) dma16(Gb + (size_t)((t) + GNS - 1) * (32 * F * 2), voffW, dW + sf); if (m == 7) dma16(Ub + (size_t)((t) + GNS - 1) * (32 * F * 2), voffW, dW + sf + 8192); } \
            SBAR(); } \
        sf = sf + 32768 == GNS * 32768 ? 0 : sf + 32768; sn = sn + 32768 == GNS * 32768 ? 0 : sn + 32768; } while (0)
    for (int t = 0; t < 28; t += 2) {
        GLU_STEP(wA, wB, t, 8, true, true);
        GLU_STEP(wB, wA, t + 1, 8, true, true);
    }
    GLU_STEP(wA, wB, 28, 8, false, true);
    GLU_STEP(wB, wA, 29, 4, false, true);
    GLU_STEP(wA, wB, 30, 0, false, true);
    GLU_STEP(wB, wA, 31, 0, false, false);
#undef GLU_STEP
#undef GLU_STAGE
    char* hb = ws + WS_HH + ((size_t)(rp * 16 + wm * 8) * (F / 8) + ct * 16 + wn * 4 + (((g & 1) << 1) | (g >> 1))) * 256 + (lane & 15) * 16;
#pragma unroll
    for (int m = 0; m < 8; ++m) {
        unsigned u[2][2];
#pragma unroll
        for (int b = 0; b < 2; ++b) {
            float hv[4];
#pragma unroll
            for (int r = 0; r < 4; ++r) hv[r] = fmaxf(acc[m][b][r], 0.f) * acc[m][2 + b][r];
            u[b][0] = cvtpk(hv[0], hv[1]); u[b][1] = cvtpk(hv[2], hv[3]);
        }
        const auto r0 = __builtin_amdgcn_permlane16_swap(u[0][0], u[1][0], false, false);
        const auto r1 = __builtin_amdgcn_permlane16_swap(u[0][1], u[1][1], false, false);
        *(u32x4*)(hb + (size_t)m * (F / 8) * 256) = (u32x4){r0[0], r1[0], r0[1], r1[1]};
    }
}

constexpr int DNS = 5;
__global__ __launch_bounds__(512, 2) void k_down(const u16* __restrict__ wd, char* __restrict__ ws, u16* __restrict__ out) {
    extern __shared__ __attribute__((aligned(16))) char smem[];
    lds_ptr lds = (lds_ptr)smem;
    const int tid = threadIdx.x, lane = tid & 63, wid = uni(tid >> 6);
    const int xcd = blockIdx.x & 7, idx = blockIdx.x >> 3, kh = xcd & 1, rb = (xcd >> 1) * 4 + (idx & 3), ct = idx >> 2;
    const unsigned lds0 = uni((unsigned)(uintptr_t)lds);
    const int wm = wid >> 2, wn = wid & 3, g = lane >> 4, q = (lane & 15) >> 2, p = lane & 3;
    const char* Ab = ws + WS_HH + ((size_t)(rb * 8 + wid) * (F / 8) + kh * 224) * 256;
    const unsigned voffA = lane * 16;
    const int kr = 8 * wid + g;
    const unsigned voffW = (unsigned)(kr * 2048 + (((lane & 15) ^ (((kr & 3) | (((kr >> 3) & 1) << 2)) << 1)) << 4));
    const char* Wb = (const char*)wd + (size_t)kh * 1792 * 2048 + ct * 256;
    const unsigned dA = lds0 + wid * 2048, dW = lds0 + 16384 + wid * 2048;
#define DN_STAGE(t, so) do { const char* a_ = Ab + (size_t)(t) * 2048; dma16(a_, voffA, dA + (so)); dma16(a_ + 1024, voffA, dA + (so) + 1024); \
        const char* w_ = Wb + (size_t)(t) * 131072; dma16(w_, voffW, dW + (so)); dma16(w_ + 8192, voffW, dW + (so) + 1024); } while (0)
#pragma unroll
    for (int t = 0; t < DNS - 1; ++t) DN_STAGE(t, t * 32768);
    const int fl = q | ((g & 1) << 2);
    const lds_ptr pa = lds + wm * 8192 + lane * 16;
    const lds_ptr pw0 = lds + 16384 + (8 * g + q) * 256 + (((2 * wn) ^ fl) << 5) + 8 * p;
    const lds_ptr pw1 = lds + 16384 + (8 * g + q) * 256 + (((2 * wn + 1) ^ fl) << 5) + 8 * p;
    f32x4 acc[4][2];
#pragma unroll
    for (int m = 0; m < 4; ++m) { acc[m][0] = (f32x4){0.f, 0.f, 0.f, 0.f}; acc[m][1] = (f32x4){0.f, 0.f, 0.f, 0.f}; }
    bf16x8 aA[8], wA[4], aB[8], wB[4];
#define DN_LOAD(AF, WF, so) do { \
        WF[0] = ldstr2(pw0 + (so), 1024); WF[1] = ldstr2(pw1 + (so), 1024); \
        _Pragma("unroll") for (int m = 0; m < 4; ++m) AF[2 * m] = ldsr128(pa + (so) + m * 2048); \
        WF[2] = ldstr2(pw0 + (so) + 8192, 1024); WF[3] = ldstr2(pw1 + (so) + 8192, 1024); \
        _Pragma("unroll") for (int m = 0; m < 4; ++m) AF[2 * m + 1] = ldsr128(pa + (so) + m * 2048 + 1024); } while (0)
    WAITV_BAR(12);
    DN_LOAD(aA, wA, 0);
    int sn = 32768, sf = (DNS - 1) * 32768;
#define DN_STEP(AC, WC, AN, WN, t, VM, DO_STAGE, DO_LOAD) do { \
        WAITV_BAR(VM); \
        if (DO_STAGE) DN_STAGE((t) + DNS - 1, sf); \
        if (DO_LOAD) DN_LOAD(AN, WN, sn); \
        _Pragma("unroll") for (int ks = 0; ks < 2; ++ks) _Pragma("unroll") for (int m = 0; m < 4; ++m) { \
            acc[m][0] = MFMA16(WC[2 * ks], AC[2 * m + ks], acc[m][0]); acc[m][1] = MFMA16(WC[2 * ks + 1], AC[2 * m + ks], acc[m][1]); } \
        SBAR(); sf = sf + 32768 == DNS * 32768 ? 0 : sf + 32768; sn = sn + 32768 == DNS * 32768 ? 0 : sn + 32768; } while (0)
    for (int t = 0; t < 24; t += 2) {
        DN_STEP(aA, wA, aB, wB, t, 8, true, true);
        DN_STEP(aB, wB, aA, wA, t + 1, 8, true, true);
    }
    DN_STEP(aA, wA, aB, wB, 24, 8, false, true);
    DN_STEP(aB, wB, aA, wA, 25, 4, false, true);
    DN_STEP(aA, wA, aB, wB, 26, 0, false, true);
    DN_STEP(aB, wB, aA, wA, 27, 0, false, false);
#undef DN_STEP
#undef DN_LOAD
#undef DN_STAGE
    const int tile = rb * 8 + ct;
    char* xo = ws + WS_XCH + ((size_t)(tile * 2 + kh) * 8 + wid) * 4096 + lane * 16;
    const char* xi = ws + WS_XCH + ((size_t)(tile * 2 + (kh ^ 1)) * 8 + wid) * 4096 + lane * 16;
    f32x4 keep[2][2], give[2][2];
#pragma unroll
    for (int mm = 0; mm < 2; ++mm)
#pragma unroll
        for (int b = 0; b < 2; ++b) { keep[mm][b] = kh ? acc[2 + mm][b] : acc[mm][b]; give[mm][b] = kh ? acc[mm][b] : acc[2 + mm][b]; }
#pragma unroll
    for (int mm = 0; mm < 2; ++mm)
#pragma unroll
        for (int b = 0; b < 2; ++b) asm volatile("global_store_dwordx4 %0, %1, off sc1\n\ts_nop 1" ::"v"(xo + (mm * 2 + b) * 1024), "v"(give[mm][b]) : "memory");
    const int gc = ((g & 1) << 1) | (g >> 1);
    const int colb = ct * 128 + wn * 32;
    const f32x4 gb0 = mod4(ws, 5120 + colb + 4 * g), gb1 = mod4(ws, 5120 + colb + 16 + 4 * g);
    const u16* x1 = (const u16*)(ws + WS_X1);
    u32x2 xr[2][2];
#pragma unroll
    for (int mm = 0; mm < 2; ++mm) {
        const size_t ro = (size_t)(rb * 128 + wm * 64 + (2 * kh + mm) * 16 + (lane & 15)) * D + colb + 4 * g;
        xr[mm][0] = *(const u32x2*)(x1 + ro); xr[mm][1] = *(const u32x2*)(x1 + ro + 16);
    }
    WAIT_V(0);
    __syncthreads();
    GAS unsigned* flags = (GAS unsigned*)(ws + WS_FLAGS);
    if (tid == 0) __hip_atomic_store(flags + (tile * 2 + kh) * 64, 1u, __ATOMIC_RELAXED, __HIP_MEMORY_SCOPE_AGENT);
    if (wid == 0) {
        unsigned spins = 0;
        while (uni(__hip_atomic_load(flags + (tile * 2 + (kh ^ 1)) * 64, __ATOMIC_RELAXED, __HIP_MEMORY_SCOPE_AGENT)) != 1u) {
            __builtin_amdgcn_s_sleep(1);
            if (++spins > (1u << 22)) break;
        }
    }
    __syncthreads();
    f32x4 got[2][2];
    asm volatile("global_load_dwordx4 %0, %4, off sc1\n\tglobal_load_dwordx4 %1, %4, off offset:1024 sc1\n\tglobal_load_dwordx4 %2, %4, off offset:2048 sc1\n\t"
                 "global_load_dwordx4 %3, %4, off offset:3072 sc1\n\ts_waitcnt vmcnt(0)"
                 : "=&v"(got[0][0]), "=&v"(got[0][1]), "=&v"(got[1][0]), "=&v"(got[1][1]) : "v"(xi) : "memory");
#pragma unroll
    for (int mm = 0; mm < 2; ++mm) {
        unsigned u[2][2];
#pragma unroll
        for (int b = 0; b < 2; ++b) {
            const f32x4 s = keep[mm][b] + got[mm][b], gg = b ? gb1 : gb0;
            u[b][0] = cvtpk(bflo(xr[mm][b][0]) + gg[0] * s[0], bfhi(xr[mm][b][0]) + gg[1] * s[1]);
            u[b][1] = cvtpk(bflo(xr[mm][b][1]) + gg[2] * s[2], bfhi(xr[mm][b][1]) + gg[3] * s[3]);
        }
        const auto r0 = __builtin_amdgcn_permlane16_swap(u[0][0], u[1][0], false, false);
        const auto r1 = __builtin_amdgcn_permlane16_swap(u[0][1], u[1][1], false, false);
        *(u32x4*)(out + (size_t)(rb * 128 + wm * 64 + (2 * kh + mm) * 16 + (lane & 15)) * D + colb + gc * 8) = (u32x4){r0[0], r1[0], r0[1], r1[1]};
    }
}

extern "C" void kernel_launch(void* const* d_in, const int* in_sizes, int n_in, void* d_out, int out_size, void* d_ws, size_t ws_size, hipStream_t stream) {
    const u16* x = (const u16*)d_in[0];
    const u16* wq = (const u16*)d_in[1];
    const u16* wk = (const u16*)d_in[2];
    const u16* wv = (const u16*)d_in[3];
    const u16* wo = (const u16*)d_in[4];
    const u16* wg = (const u16*)d_in[6];
    const u16* wu = (const u16*)d_in[7];
    const u16* wd = (const u16*)d_in[8];
    const u16* c = (const u16*)d_in[9];
    const u16* wmod = (const u16*)d_in[10];
    char* ws = (char*)d_ws;
    (void)hipFuncSetAttribute((const void*)k_ln1kv, hipFuncAttributeMaxDynamicSharedMemorySize, 122880);
    (void)hipFuncSetAttribute((const void*)k_attn, hipFuncAttributeMaxDynamicSharedMemorySize, 163840);
    (void)hipFuncSetAttribute((const void*)k_oproj, hipFuncAttributeMaxDynamicSharedMemorySize, 98304);
    (void)hipFuncSetAttribute((const void*)k_glu, hipFuncAttributeMaxDynamicSharedMemorySize, GNS * 32768);
    (void)hipFuncSetAttribute((const void*)k_down, hipFuncAttributeMaxDynamicSharedMemorySize, DNS * 32768);
    k_mod<<<384, 256, 0, stream>>>(c, wmod, ws);
    k_ln1kv<<<256, 512, 122880, stream>>>(x, wk, wv, ws);
    k_attn<<<256, 512, 163840, stream>>>(wq, ws);
    k_oproj<<<256, 512, 98304, stream>>>(x, wo, ws);
    k_ln2<<<128, 512, 0, stream>>>(ws);
    k_glu<<<224, 512, GNS * 32768, stream>>>(wg, wu, ws);
    k_down<<<256, 512, DNS * 32768, stream>>>(wd, ws, (u16*)d_out);
}
```

```cpp
#include <hip/hip_runtime.h>
#include <hip/hip_bf16.h>

#define LAS __attribute__((address_space(3)))
#define GAS __attribute__((address_space(1)))
typedef unsigned short u16;
typedef short bf16x8 __attribute__((ext_vector_type(8)));
typedef short s16x4 __attribute__((ext_vector_type(4)));
typedef float f32x4 __attribute__((ext_vector_type(4)));
typedef float f32x16 __attribute__((ext_vector_type(16)));
typedef unsigned u32x4 __attribute__((ext_vector_type(4)));
typedef unsigned u32x2 __attribute__((ext_vector_type(2)));
typedef LAS char* lds_ptr;

constexpr int S = 2048, D = 1024, F = 3584;
constexpr size_t WS_FLAGS = 0;
constexpr size_t WS_MODP = 65536;
constexpr size_t WS_SC = WS_MODP + 4 * 6144 * 4;
constexpr size_t WS_KN = WS_SC + 2048;
constexpr size_t WS_ST1 = WS_KN + 1024;
constexpr size_t WS_KV = 262144;
constexpr size_t WS_H1 = 1u << 20;
constexpr size_t WS_O = WS_H1 + (4u << 20);
constexpr size_t WS_X1 = WS_O + (4u << 20);
constexpr size_t WS_Y = WS_X1 + (4u << 20);
constexpr size_t WS_HH = WS_Y + (4u << 20);
constexpr size_t WS_XCH = WS_HH + (16u << 20);
constexpr size_t WS_WQ = WS_XCH + (8u << 20);
constexpr size_t WS_WO = WS_WQ + (2u << 20);
constexpr size_t WS_WGU = WS_WO + (2u << 20);
constexpr size_t WS_WD = WS_WGU + (16u << 20);

__device__ __forceinline__ float bf2f(u16 b) { return __uint_as_float(((unsigned)b) << 16); }
__device__ __forceinline__ float bflo(unsigned w) { return __uint_as_float(w << 16); }
__device__ __forceinline__ float bfhi(unsigned w) { return __uint_as_float(w & 0xffff0000u); }
typedef float f32x2 __attribute__((ext_vector_type(2)));
typedef __bf16 bf16x2v __attribute__((ext_vector_type(2)));
__device__ __forceinline__ unsigned cvtpk(float lo, float hi) { return __builtin_bit_cast(unsigned, __builtin_convertvector((f32x2){lo, hi}, bf16x2v)); }
__device__ __forceinline__ float rbf(float x) { return bflo(cvtpk(x, x)); }

__device__ __forceinline__ void dma16(const void* gbase, unsigned voff, unsigned ldsaddr) {
    asm volatile("s_mov_b32 m0, %0\n\ts_nop 0\n\tglobal_load_lds_dwordx4 %1, %2" ::"s"(ldsaddr), "v"(voff), "s"(gbase) : "memory");
}
#define WAIT_V(n) asm volatile("s_waitcnt vmcnt(" #n ")" ::: "memory")
#define BARRIER() asm volatile("s_barrier" ::: "memory")
#define WAITV_BAR(n) asm volatile("s_waitcnt vmcnt(" #n ")\n\ts_barrier" ::: "memory")
#define WAITVL_BAR(n) asm volatile("s_waitcnt vmcnt(" #n ") lgkmcnt(0)\n\ts_barrier" ::: "memory")
#define PIN(x) asm volatile("" : "+v"(x))
#define SBAR() __builtin_amdgcn_sched_barrier(0)
#define MFMA32(a, b, c) __builtin_amdgcn_mfma_f32_32x32x16_bf16(a, b, c, 0, 0, 0)
#define MFMA16(a, b, c) __builtin_amdgcn_mfma_f32_16x16x32_bf16(a, b, c, 0, 0, 0)
__device__ __forceinline__ bf16x8 ldsr128(lds_ptr p) { return *(const LAS bf16x8*)p; }
__device__ __forceinline__ s16x4 ldstr(lds_ptr p) { return __builtin_amdgcn_ds_read_tr16_b64_v4i16((LAS s16x4*)p); }
__device__ __forceinline__ bf16x8 ldstr2(lds_ptr p, int d) { s16x4 a = ldstr(p), b = ldstr(p + d); return (bf16x8){a[0], a[1], a[2], a[3], b[0], b[1], b[2], b[3]}; }
__device__ __forceinline__ unsigned uni(unsigned x) { return (unsigned)__builtin_amdgcn_readfirstlane((int)x); }

#define STAMP_DECL do {} while (0)
#define STAMP(kid, i) do {} while (0)
#define STAMP_FLUSH(kid) do {} while (0)

template <int CTRL> __device__ __forceinline__ float dppf(float v) { return __builtin_bit_cast(float, __builtin_amdgcn_mov_dpp(__builtin_bit_cast(int, v), CTRL, 0xf, 0xf, true)); }
__device__ __forceinline__ float rows_sum(float v) {
    auto r = __builtin_amdgcn_permlane16_swap(__float_as_uint(v), __float_as_uint(v), false, false); v = __uint_as_float(r[0]) + __uint_as_float(r[1]);
    r = __builtin_amdgcn_permlane32_swap(__float_as_uint(v), __float_as_uint(v), false, false); return __uint_as_float(r[0]) + __uint_as_float(r[1]);
}
__device__ __forceinline__ float rows_max(float v) {
    auto r = __builtin_amdgcn_permlane16_swap(__float_as_uint(v), __float_as_uint(v), false, false); v = fmaxf(__uint_as_float(r[0]), __uint_as_float(r[1]));
    r = __builtin_amdgcn_permlane32_swap(__float_as_uint(v), __float_as_uint(v), false, false); return fmaxf(__uint_as_float(r[0]), __uint_as_float(r[1]));
}
__device__ __forceinline__ float row16_sum(float v) { v += dppf<0xB1>(v); v += dppf<0x4E>(v); v += dppf<0x141>(v); v += dppf<0x140>(v); return v; }
__device__ __forceinline__ float row16_max(float v) { v = fmaxf(v, dppf<0xB1>(v)); v = fmaxf(v, dppf<0x4E>(v)); v = fmaxf(v, dppf<0x141>(v)); v = fmaxf(v, dppf<0x140>(v)); return v; }
__device__ __forceinline__ float wsum(float v) { return rows_sum(row16_sum(v)); }
__device__ __forceinline__ float wmax(float v) { return rows_max(row16_max(v)); }
__device__ __forceinline__ float silu_bf(u16 cb) { const float cv = bf2f(cb); return rbf(cv / (1.f + __expf(-cv))); }

__device__ __forceinline__ void pack_load(const char* src  , unsigned pitch, unsigned ldsaddr, int lane) {
    const unsigned vo = (unsigned)(lane >> 3) * pitch, sw = (lane & 7) ^ (((lane >> 4) & 1) << 1);
#pragma unroll
    for (int i = 0; i < 4; ++i) dma16(src + (size_t)i * 8 * pitch, vo + ((sw ^ ((i & 1) << 2)) << 4), ldsaddr + i * 1024);
}
__device__ __forceinline__ bf16x8 pack_frag(lds_ptr area, int nf, int lane) {
    const int g = lane >> 4, q = (lane & 15) >> 2;
    return ldstr2(area + (8 * g + q) * 128 + ((nf ^ (((q >> 1) & 1) | ((g & 1) << 1))) << 5) + (lane & 3) * 8, 512);
}
__device__ __forceinline__ void store16_wt(char* base, unsigned voff, bf16x8 v) {
    asm volatile("global_store_dwordx4 %0, %1, %2 sc1\n\ts_nop 1" ::"v"(voff), "v"(v), "s"(base) : "memory");
}

__global__ __launch_bounds__(256) void k_pre(const u16* __restrict__ c, const u16* __restrict__ wmod, const u16* __restrict__ x, const u16* __restrict__ wq, char* __restrict__ ws) {
    __shared__ __attribute__((aligned(16))) char smem[16384];
    float (*red)[32] = (float (*)[32])smem;
    STAMP_DECL; STAMP(0, 0);
    const int tid = threadIdx.x, lane = tid & 63, bid = blockIdx.x;
    if (bid < 256) {
        const int idx = bid >> 3, cs = ((((idx & 3) << 3) | (bid & 7)) << 1) | ((idx >> 2) & 1), kq = idx >> 3;
        const int r = tid >> 2, ch = tid & 3;
        u32x4 w[4]; u16 cc[4];
#pragma unroll
        for (int p = 0; p < 4; ++p) { w[p] = *(const u32x4*)(wmod + (size_t)(kq * 256 + p * 64 + r) * 6144 + cs * 32 + ch * 8); cc[p] = c[kq * 256 + p * 64 + r]; }
        if (bid < 64) ((unsigned*)(ws + WS_FLAGS))[bid * 256 + tid] = 0u;
        float acc[8] = {0.f, 0.f, 0.f, 0.f, 0.f, 0.f, 0.f, 0.f};
#pragma unroll
        for (int p = 0; p < 4; ++p) {
            const float sv = silu_bf(cc[p]);
            if (cs == 0 && ch == 0) ((u16*)(ws + WS_SC))[kq * 256 + p * 64 + r] = (u16)(__float_as_uint(sv) >> 16);
#pragma unroll
            for (int j = 0; j < 4; ++j) { acc[2 * j] += sv * bflo(w[p][j]); acc[2 * j + 1] += sv * bfhi(w[p][j]); }
        }
#pragma unroll
        for (int j = 0; j < 8; ++j) { acc[j] += dppf<0x124>(acc[j]); acc[j] += dppf<0x128>(acc[j]); acc[j] = rows_sum(acc[j]); }
        if (lane < 4) {
#pragma unroll
            for (int j = 0; j < 8; ++j) red[tid >> 6][ch * 8 + j] = acc[j];
        }
        __syncthreads();
        if (tid < 32) ((float*)(ws + WS_MODP))[kq * 6144 + cs * 32 + tid] = (red[0][tid] + red[1][tid]) + (red[2][tid] + red[3][tid]);
    } else if (bid >= 512) {
        const int wid = uni(tid >> 6), u = (bid - 512) * 4 + wid, kb = u >> 4, cg = u & 15;
        lds_ptr area = (lds_ptr)smem + wid * 4096;
        pack_load((const char*)wq + (size_t)kb * 32 * 2048 + cg * 128, 2048, uni((unsigned)(uintptr_t)area), lane);
        WAIT_V(0);
#pragma unroll
        for (int nf = 0; nf < 4; ++nf) store16_wt(ws + WS_WQ + (size_t)(cg * 32 + kb) * 4096, lane * 16 + nf * 1024, pack_frag(area, nf, lane));
    } else {
        const int row0 = (bid - 256) * 8 + (tid >> 6) * 2;
        u32x4 xv[2][2];
#pragma unroll
        for (int rr = 0; rr < 2; ++rr)
#pragma unroll
            for (int hh = 0; hh < 2; ++hh) xv[rr][hh] = *(const u32x4*)(x + (size_t)(row0 + rr) * D + hh * 512 + lane * 8);
#pragma unroll
        for (int rr = 0; rr < 2; ++rr) {
            float v[16];
#pragma unroll
            for (int hh = 0; hh < 2; ++hh)
#pragma unroll
                for (int j = 0; j < 4; ++j) { v[hh * 8 + 2 * j] = bflo(xv[rr][hh][j]); v[hh * 8 + 2 * j + 1] = bfhi(xv[rr][hh][j]); }
            float sm = 0.f;
#pragma unroll
            for (int j = 0; j < 16; ++j) sm += v[j];
            const float mean = wsum(sm) * (1.f / 1024.f);
            float q = 0.f;
#pragma unroll
            for (int j = 0; j < 16; ++j) { const float d = v[j] - mean; q += d * d; }
            const float rstd = 1.f / sqrtf(wsum(q) * (1.f / 1024.f) + 1e-5f);
            if (lane == 0) *(f32x2*)(ws + WS_ST1 + (row0 + rr) * 8) = (f32x2){mean, rstd};
        }
    }
    STAMP(0, 1); STAMP_FLUSH(0);
}

__device__ __forceinline__ f32x4 mod4(const char* ws, int col) {
    const float* p = (const float*)(ws + WS_MODP) + col;
    const f32x4 a = *(const f32x4*)p, b = *(const f32x4*)(p + 6144), c = *(const f32x4*)(p + 2 * 6144), d = *(const f32x4*)(p + 3 * 6144);
    const f32x4 s = (a + b) + (c + d);
    return (f32x4){rbf(s[0]), rbf(s[1]), rbf(s[2]), rbf(s[3])};
}

template <bool HAVE_STATS>
__device__ __forceinline__ void ln_rows_to_lds(const u32x4 (&xv)[2][2], const f32x4 st, lds_ptr img, const LAS float* tab, int wid, int lane) {
#pragma unroll
    for (int rr = 0; rr < 2; ++rr) {
        float v[16];
#pragma unroll
        for (int hh = 0; hh < 2; ++hh)
#pragma unroll
            for (int j = 0; j < 4; ++j) { v[hh * 8 + 2 * j] = bflo(xv[rr][hh][j]); v[hh * 8 + 2 * j + 1] = bfhi(xv[rr][hh][j]); }
        float mean, rstd;
        if (HAVE_STATS) {
            mean = st[2 * rr]; rstd = st[2 * rr + 1];
#pragma unroll
            for (int j = 0; j < 16; ++j) v[j] -= mean;
        } else {
            float s = 0.f;
#pragma unroll
            for (int j = 0; j < 16; ++j) s += v[j];
            mean = wsum(s) * (1.f / 1024.f);
            float q = 0.f;
#pragma unroll
            for (int j = 0; j < 16; ++j) { v[j] -= mean; q += v[j] * v[j]; }
            rstd = 1.f / sqrtf(wsum(q) * (1.f / 1024.f) + 1e-5f);
        }
        const int row = 2 * wid + rr;
#pragma unroll
        for (int hh = 0; hh < 2; ++hh) {
            const int c = hh * 64 + lane;
            const f32x4 g0 = *(const LAS f32x4*)(tab + c * 8), g1 = *(const LAS f32x4*)(tab + c * 8 + 4);
            const f32x4 b0 = *(const LAS f32x4*)(tab + 1024 + c * 8), b1 = *(const LAS f32x4*)(tab + 1024 + c * 8 + 4);
            float h[8];
#pragma unroll
            for (int j = 0; j < 4; ++j) { h[j] = v[hh * 8 + j] * rstd * g0[j] + b0[j]; h[4 + j] = v[hh * 8 + 4 + j] * rstd * g1[j] + b1[j]; }
            const u32x4 o = {cvtpk(h[0], h[1]), cvtpk(h[2], h[3]), cvtpk(h[4], h[5]), cvtpk(h[6], h[7])};
            *(LAS u32x4*)(img + c * 256 + ((row ^ (c & 15)) << 4)) = o;
        }
    }
}

__global__ __launch_bounds__(512) void k_ln1kv(const u16* __restrict__ x, const u16* __restrict__ wk, const u16* __restrict__ wv, char* __restrict__ ws) {
    STAMP_DECL; STAMP(1, 0);
    extern __shared__ __attribute__((aligned(16))) char smem[];
    lds_ptr lds = (lds_ptr)smem;
    const int tid = threadIdx.x, lane = tid & 63, wid = uni(tid >> 6);
    const int bid = blockIdx.x, rb = (bid >> 4) * 8 + (bid & 7), isv = (bid >> 3) & 1;
    const unsigned lds0 = uni((unsigned)(uintptr_t)lds);
    f32x4 mp[4]; u32x4 xv[2][2]; f32x4 st;
    {
        const char* mb = ws + WS_MODP;
        const unsigned mo = (unsigned)((tid * 4 + 1024) & 2047) * 4u;
#pragma unroll
        for (int k = 0; k < 4; ++k) asm volatile("global_load_dwordx4 %0, %1, %2" : "=v"(mp[k]) : "v"(mo + k * 6144 * 4), "s"(mb) : "memory");
        const unsigned xo = (unsigned)((rb * 16 + 2 * wid) * D + lane * 8) * 2u;
#pragma unroll
        for (int rr = 0; rr < 2; ++rr)
#pragma unroll
            for (int hh = 0; hh < 2; ++hh) asm volatile("global_load_dwordx4 %0, %1, %2" : "=v"(xv[rr][hh]) : "v"(xo + rr * 2048 + hh * 1024), "s"(x) : "memory");
        asm volatile("global_load_dwordx4 %0, %1, %2" : "=v"(st) : "v"((unsigned)(rb * 16 + 2 * wid) * 8u), "s"(ws + WS_ST1) : "memory");
    }
    {
        const u16* w = isv ? wv : wk;
        const unsigned voff = (lane >> 2) * 64 + (((lane & 3) ^ (((lane >> 5) & 1) << 1)) << 4);
#pragma unroll
        for (int i = 0; i < 8; ++i) dma16((const char*)w + (wid * 8 + i) * 1024, voff, lds0 + (wid * 8 + i) * 1024);
    }
    LAS float* tab = (LAS float*)(lds + 98304);
    WAIT_V(13);
    {
        asm volatile("" : "+v"(mp[0]), "+v"(mp[1]), "+v"(mp[2]), "+v"(mp[3]));
        const f32x4 s = (mp[0] + mp[1]) + (mp[2] + mp[3]);
        f32x4 v = {rbf(s[0]), rbf(s[1]), rbf(s[2]), rbf(s[3])};
        if (wid < 4) v = (f32x4){rbf(1.f + v[0]), rbf(1.f + v[1]), rbf(1.f + v[2]), rbf(1.f + v[3])};
        *(LAS f32x4*)(tab + tid * 4) = v;
    }
    WAITVL_BAR(8);
    asm volatile("" : "+v"(xv[0][0]), "+v"(xv[0][1]), "+v"(xv[1][0]), "+v"(xv[1][1]), "+v"(st));
    STAMP(1, 1);
    lds_ptr img = lds + 65536;
    ln_rows_to_lds<true>(xv, st, img, tab, wid, lane);
    WAITVL_BAR(0);
    STAMP(1, 2);
    f32x4 acc[2] = {{0.f, 0.f, 0.f, 0.f}, {0.f, 0.f, 0.f, 0.f}};
    const int g = lane >> 4, q = (lane & 15) >> 2, p = lane & 3;
    bf16x8 hf[4];
#pragma unroll
    for (int i = 0; i < 4; ++i) {
        const int s = wid * 4 + i, cc = 4 * s + g;
        hf[i] = ldsr128(img + cc * 256 + (((lane & 15) ^ (cc & 15)) << 4));
#pragma unroll
        for (int nf = 0; nf < 2; ++nf) {
            const bf16x8 wf = ldstr2(lds + (32 * s + 8 * g + q) * 64 + ((nf ^ (g & 1)) << 5) + 8 * p, 256);
            acc[nf] = MFMA16(wf, hf[i], acc[nf]);
        }
    }
    LAS float* red = (LAS float*)(lds + 106496);
#pragma unroll
    for (int nf = 0; nf < 2; ++nf) *(LAS f32x4*)(red + wid * 512 + (lane & 15) * 32 + nf * 16 + 4 * g) = acc[nf];
    asm volatile("s_waitcnt lgkmcnt(0)\n\ts_barrier" ::: "memory");
    LAS float* sm = (LAS float*)(lds + 122880);
    {
        const int key = tid >> 5, d = tid & 31;
        float v = 0.f;
#pragma unroll
        for (int w = 0; w < 8; ++w) v += red[w * 512 + tid];
        const unsigned vb = cvtpk(v, v) & 0xffffu;
        const float vr = bflo(vb);
        const int T = rb >> 1, hfl = rb & 1;
        u16* kv = (u16*)(ws + WS_KV + (size_t)T * 4096);
        float mx;
        if (!isv) {
            kv[(d >> 4) * 512 + (((d >> 3) & 1) * 32 + hfl * 16 + key) * 8 + (d & 7)] = (u16)vb;
            const float n2 = row16_sum(vr * vr);
            const auto r = __builtin_amdgcn_permlane16_swap(__float_as_uint(n2), __float_as_uint(n2), false, false);
            mx = __uint_as_float(r[0]) + __uint_as_float(r[1]);
        } else {
            const int j = ((key >> 3) << 2) | (key & 3), h = (key >> 2) & 1;
            kv[1024 + hfl * 512 + (h * 32 + d) * 8 + j] = (u16)vb;
            mx = row16_max(fabsf(vr));
        }
        mx = rows_max(mx);
        if (!(vr - vr == 0.f)) mx = INFINITY;
        mx = wmax(mx);
        if (lane == 0) sm[wid] = mx;
    }
    {
        char* hb = ws + WS_H1 + ((size_t)rb * 128 + 16 * wid) * 256 + lane * 16;
        if (isv) { *(bf16x8*)(hb + 1024) = hf[1]; *(bf16x8*)(hb + 3072) = hf[3]; } else { *(bf16x8*)hb = hf[0]; *(bf16x8*)(hb + 2048) = hf[2]; }
    }
    STAMP(1, 3);
    asm volatile("s_waitcnt lgkmcnt(0)\n\ts_barrier" ::: "memory");
    if (tid == 0) {
        float m = sm[0];
#pragma unroll
        for (int w = 1; w < 8; ++w) m = fmaxf(m, sm[w]);
        ((float*)(ws + WS_KN))[isv * 128 + rb] = m;
    }
    STAMP(1, 4); STAMP_FLUSH(1);
}

template <int NT, int ACH, int NTAIL, class Tail>
__device__ __forceinline__ void gemm128x64_produce(const char* Ap, const char* Wp, unsigned lds0, int pw, int lane, Tail tail) {
    const char* Aw = Ap + (size_t)(2 * pw) * ACH * 256;
    const unsigned voffA = lane * 16;
    const char* Ww = Wp + pw * 2048;
    const unsigned dA = lds0 + pw * 4096, dW = lds0 + 16384 + pw * 2048;
#define G64_STAGE(t, so) do { const char* w_ = Ww + (size_t)(t) * 8192; const char* a_ = Aw + (size_t)(t) * 2048; \
        dma16(w_, voffA, dW + (so)); dma16(a_, voffA, dA + (so)); dma16(a_ + ACH * 256, voffA, dA + (so) + 2048); \
        dma16(w_ + 1024, voffA, dW + (so) + 1024); dma16(a_ + 1024, voffA, dA + (so) + 1024); dma16(a_ + ACH * 256 + 1024, voffA, dA + (so) + 3072); } while (0)
#pragma unroll
    for (int t = 0; t < 5; ++t) G64_STAGE(t, t * 24576);
    WAITV_BAR(24);
    int sf = 5 * 24576;
#pragma unroll 1
    for (int t = 0; t < NT - 5; ++t) {
        WAITV_BAR(18);
        G64_STAGE(t + 5, sf);
        sf = sf == 5 * 24576 ? 0 : sf + 24576;
    }
    WAITV_BAR(18);
    WAITV_BAR(12);
    tail();
    asm volatile("s_waitcnt vmcnt(%0)\n\ts_barrier" ::"i"(6 + NTAIL) : "memory");
    asm volatile("s_waitcnt vmcnt(%0)\n\ts_barrier" ::"i"(NTAIL) : "memory");
#undef G64_STAGE
}
template <int NT, class St>
__device__ __forceinline__ void gemm128x64_consume(f32x4 (&acc)[2][2], lds_ptr lds, int wid, int lane, St stamp) {
    const lds_ptr pa = lds + (wid & 3) * 4096 + lane * 16, pw = lds + 16384 + (wid >> 2) * 2048 + lane * 16;
#pragma unroll
    for (int m = 0; m < 2; ++m) { acc[m][0] = (f32x4){0.f, 0.f, 0.f, 0.f}; acc[m][1] = (f32x4){0.f, 0.f, 0.f, 0.f}; }
    bf16x8 aA[4], wA[4], aB[4], wB[4];
    BARRIER(); SBAR();
    stamp(1);
#pragma unroll
    for (int ks = 0; ks < 2; ++ks) { wA[2 * ks] = ldsr128(pw + ks * 4096); aA[2 * ks] = ldsr128(pa + ks * 1024); wA[2 * ks + 1] = ldsr128(pw + ks * 4096 + 1024); aA[2 * ks + 1] = ldsr128(pa + 2048 + ks * 1024); }
    SBAR();
    int sn = 24576;
#define G64_STEP(AC, WC, AN, WN, DO_NEXT) do { \
        if (DO_NEXT) BARRIER(); SBAR(); \
        _Pragma("unroll") for (int i = 0; i < 4; ++i) { const int ks = i >> 1, m = i & 1; \
            if (DO_NEXT) { WN[i] = ldsr128(pw + sn + ks * 4096 + m * 1024); AN[i] = ldsr128(pa + sn + m * 2048 + ks * 1024); } \
            acc[m][0] = MFMA16(WC[2 * ks], AC[i], acc[m][0]); acc[m][1] = MFMA16(WC[2 * ks + 1], AC[i], acc[m][1]); PIN(acc[m][0]); PIN(acc[m][1]); \
            SBAR(); } \
        sn = sn == 5 * 24576 ? 0 : sn + 24576; } while (0)
#pragma unroll 1
    for (int t = 0; t < NT - 2; t += 2) {
        G64_STEP(aA, wA, aB, wB, true);
        G64_STEP(aB, wB, aA, wA, true);
    }
    stamp(2);
    G64_STEP(aA, wA, aB, wB, true);
    G64_STEP(aB, wB, aA, wA, false);
#undef G64_STEP
}

__global__ __launch_bounds__(768, 3) void k_attn(const u16* __restrict__ wo, const u16* __restrict__ wg, const u16* __restrict__ wu,
                                                 const u16* __restrict__ wd, const u16* __restrict__ wmod, char* __restrict__ ws) {
    STAMP_DECL; STAMP(2, 0);
    extern __shared__ __attribute__((aligned(16))) char smem[];
    lds_ptr lds = (lds_ptr)smem;
    const int tid = threadIdx.x, lane = tid & 63, wid = uni(tid >> 6);
    const int xcd = blockIdx.x & 7, idx = blockIdx.x >> 3, rb = (xcd >> 1) * 4 + (idx & 3), hp = (xcd & 1) * 8 + (idx >> 2);
    const unsigned lds0 = uni((unsigned)(uintptr_t)lds);
    if (wid >= 8) {
        const int pw = wid - 8;
        const char* kvg = ws + WS_KV + pw * 8192;
        const unsigned voffL = lane * 16, kvd = lds0 + pw * 8192;
#define KV_STAGE(st, so) do { _Pragma("unroll") for (int i_ = 0; i_ < 8; ++i_) dma16(kvg + (st) * 32768 + i_ * 1024, voffL, kvd + (so) + i_ * 1024); } while (0)
        gemm128x64_produce<16, 128, 16>(ws + WS_H1 + (size_t)rb * 8 * 128 * 256, ws + WS_WQ + (size_t)hp * 131072, lds0, pw, lane, [&]() { KV_STAGE(0, 98304); KV_STAGE(1, 131072); });
        WAITV_BAR(8);
        const lds_ptr area = lds + pw * 16384; const unsigned area0 = lds0 + pw * 16384;
        const int cs2 = blockIdx.x & 63, kq2 = blockIdx.x >> 6;
        bf16x8 sc0, sc1;
        {
            const char* wb = (const char*)wmod + (size_t)(kq2 * 256 + pw * 64) * 12288 + 4096 + cs2 * 128;
            pack_load(wb, 12288, area0, lane); pack_load(wb + 32 * 12288, 12288, area0 + 4096, lane);
            const unsigned so = (unsigned)(kq2 * 256 + pw * 64 + (lane >> 4) * 8) * 2u;
            asm volatile("global_load_dwordx4 %0, %1, %2" : "=v"(sc0) : "v"(so), "s"(ws + WS_SC) : "memory");
            asm volatile("global_load_dwordx4 %0, %1, %2" : "=v"(sc1) : "v"(so + 64), "s"(ws + WS_SC) : "memory");
        }
        const int ubase = blockIdx.x * 4 + pw;
        auto unit_load = [&](int j) {
            int u = j * 1024 + ubase; u = u < 5888 ? u : u - 1024;
            const unsigned dst = area0 + 8192 + (j & 1) * 4096;
            if (u < 512) pack_load((const char*)wo + (size_t)(u >> 4) * 32 * 2048 + (u & 15) * 128, 2048, dst, lane);
            else if (u < 4096) { const int v = u < 2304 ? u - 512 : u - 2304, kb = v / 56, cg = v - kb * 56; pack_load((const char*)(u < 2304 ? wg : wu) + (size_t)kb * 32 * 7168 + cg * 128, 7168, dst, lane); }
            else { const int v = u - 4096; pack_load((const char*)wd + (size_t)(v >> 4) * 32 * 2048 + (v & 15) * 128, 2048, dst, lane); }
        };
        auto unit_store = [&](int j) {
            int u = j * 1024 + ubase; u = u < 5888 ? u : u - 1024;
            const lds_ptr src = area + 8192 + (j & 1) * 4096;
            bf16x8 f[4];
#pragma unroll
            for (int nf = 0; nf < 4; ++nf) f[nf] = pack_frag(src, nf, lane);
            if (u < 512 || u >= 4096) {
                char* d = u < 512 ? ws + WS_WO + (size_t)((u & 15) * 32 + (u >> 4)) * 4096 : ws + WS_WD + (size_t)(((u - 4096) & 15) * 112 + ((u - 4096) >> 4)) * 4096;
#pragma unroll
                for (int nf = 0; nf < 4; ++nf) store16_wt(d, lane * 16 + nf * 1024, f[nf]);
            } else {
                const int mat = u >= 2304, v = mat ? u - 2304 : u - 512, kb = v / 56, cg = v - kb * 56;
#pragma unroll
                for (int nf = 0; nf < 4; ++nf) {
                    const int cj = cg * 8 + 2 * nf + ((lane >> 3) & 1), ct = cj / 14, r = cj - ct * 14, wn = r >= 7, c = r - wn * 7;
                    const int fr = c < 6 ? mat * 3 + (c >> 1) : 6, hf = c < 6 ? (c & 1) : mat;
                    store16_wt(ws + WS_WGU + (size_t)kb * 14336, (unsigned)((ct * 32 * 14 + wn * 7 + fr) * 1024 + (lane >> 4) * 256 + (hf * 8 + (lane & 7)) * 16), f[nf]);
                }
            }
        };
        unit_load(0);
        WAITV_BAR(14);
        KV_STAGE(2, 65536);
        WAIT_V(8);
        {
            asm volatile("" : "+v"(sc0), "+v"(sc1));
            f32x4 d[4];
#pragma unroll
            for (int nf = 0; nf < 4; ++nf) { d[nf] = MFMA16(pack_frag(area, nf, lane), sc0, ((f32x4){0.f, 0.f, 0.f, 0.f})); d[nf] = MFMA16(pack_frag(area + 4096, nf, lane), sc1, d[nf]); }
            if ((lane & 15) == 0) {
#pragma unroll
                for (int nf = 0; nf < 4; ++nf) *(LAS f32x4*)(area + nf * 64 + (lane >> 4) * 16) = d[nf];
            }
        }
        unit_store(0); unit_load(1);
        WAITVL_BAR(8);
        KV_STAGE(3, 98304);
        if (pw == 0) {
            const LAS float* r = (const LAS float*)lds + lane;
            ((float*)(ws + WS_MODP))[kq2 * 6144 + 2048 + cs2 * 64 + lane] = (r[0] + r[4096]) + (r[8192] + r[12288]);
        }
        WAIT_V(8); unit_store(1); unit_load(2);
        WAITVL_BAR(8); KV_STAGE(4, 131072); WAIT_V(8); unit_store(2); unit_load(3);
        WAITVL_BAR(8); KV_STAGE(5, 65536); WAIT_V(8); unit_store(3); unit_load(4);
        WAITVL_BAR(8); KV_STAGE(6, 98304); WAIT_V(8); unit_store(4); unit_load(5);
        WAITVL_BAR(8); KV_STAGE(7, 131072); WAIT_V(8); unit_store(5);
        WAITVL_BAR(4);
#undef KV_STAGE
        return;
    }
    const f32x2 kn2 = *(const f32x2*)(ws + WS_KN + lane * 8), vm2 = *(const f32x2*)(ws + WS_KN + 512 + lane * 8);
    f32x4 qa[2][2];
    gemm128x64_consume<16>(qa, lds, wid, lane, [&](int i) {});
    asm volatile("s_waitcnt lgkmcnt(0)\n\ts_barrier" ::: "memory");
    STAMP(2, 2);
    const float knm = wmax(fmaxf(kn2[0], kn2[1])), vmm = wmax(fmaxf(vm2[0], vm2[1]));
    constexpr float QS = 0.17677669529663687f * 1.4426950408889634f;
    u32x4 qw0, qw1;
#pragma unroll
    for (int j = 0; j < 2; ++j) {
        const auto r0 = __builtin_amdgcn_permlane16_swap(cvtpk(qa[0][0][2 * j] * QS, qa[0][0][2 * j + 1] * QS), cvtpk(qa[1][0][2 * j] * QS, qa[1][0][2 * j + 1] * QS), false, false);
        const auto r1 = __builtin_amdgcn_permlane16_swap(cvtpk(qa[0][1][2 * j] * QS, qa[0][1][2 * j + 1] * QS), cvtpk(qa[1][1][2 * j] * QS, qa[1][1][2 * j + 1] * QS), false, false);
        qw0[j] = r0[0]; qw0[2 + j] = r0[1]; qw1[j] = r1[0]; qw1[2 + j] = r1[1];
    }
    float qn = 0.f;
#pragma unroll
    for (int j = 0; j < 4; ++j) { qn += bflo(qw0[j]) * bflo(qw0[j]) + bfhi(qw0[j]) * bfhi(qw0[j]) + bflo(qw1[j]) * bflo(qw1[j]) + bfhi(qw1[j]) * bfhi(qw1[j]); }
    { const auto r = __builtin_amdgcn_permlane32_swap(__float_as_uint(qn), __float_as_uint(qn), false, false); qn = __uint_as_float(r[0]) + __uint_as_float(r[1]); }
    const bool lane_ok = (qn * knm <= 88.f * 88.f) && (vmm <= 16777216.f) && (vmm >= 9.5367431640625e-7f);
    const bool fast = __all(lane_ok);
    const bf16x8 q0 = __builtin_bit_cast(bf16x8, qw0), q1 = __builtin_bit_cast(bf16x8, qw1);
    f32x16 o = {};
    float linv;
    const lds_ptr pl = lds + lane * 16;
    if (fast) {
        const short one = (short)0x3f80;
        const short ov = (((lane & 15) == 0 && ((lane >> 4) & 1) == 0) || ((lane & 15) == 1 && ((lane >> 4) & 1) == 1)) ? one : (short)0;
        const bf16x8 sel = {ov, ov, ov, ov, ov, ov, ov, ov};
        f32x4 ls = {0.f, 0.f, 0.f, 0.f};
        const f32x16 zero = {};
        f32x16 sA, sB;
        bf16x8 k0, k1, v0, v1;
        u32x4 p0, p1 = {0u, 0u, 0u, 0u};
        {
            const lds_ptr pb = pl + 98304;
            k0 = ldsr128(pb); k1 = ldsr128(pb + 1024);
            sA = MFMA32(k0, q0, zero); sA = MFMA32(k1, q1, sA);
            k0 = ldsr128(pb + 4096); k1 = ldsr128(pb + 4096 + 1024); v0 = ldsr128(pb + 2048); v1 = v0;
        }
#define EX(x) __builtin_amdgcn_exp2f(x)
#define BF8(x) __builtin_bit_cast(bf16x8, x)
#define ATT_TILE(SC, SN, PK, PV0, PV1) do { \
            SN = MFMA32(k0, q0, zero); PIN(SN); SBAR(); k0 = ldsr128(PK); \
            SC[0] = EX(SC[0]); SC[1] = EX(SC[1]); SC[2] = EX(SC[2]); SC[3] = EX(SC[3]); PIN(SC); SBAR(); \
            o = MFMA32(v1, BF8(p1), o); PIN(o); SBAR(); v1 = ldsr128(PV1); \
            SC[4] = EX(SC[4]); SC[5] = EX(SC[5]); SC[6] = EX(SC[6]); PIN(SC); SBAR(); \
            ls = MFMA16(sel, BF8(p1), ls); PIN(ls); SBAR(); SC[7] = EX(SC[7]); p0[0] = cvtpk(SC[0], SC[1]); p0[1] = cvtpk(SC[2], SC[3]); PIN(SC); PIN(p0); SBAR(); \
            SN = MFMA32(k1, q1, SN); PIN(SN); SBAR(); k1 = ldsr128((PK) + 1024); \
            p0[2] = cvtpk(SC[4], SC[5]); p0[3] = cvtpk(SC[6], SC[7]); PIN(p0); \
            SC[8] = EX(SC[8]); SC[9] = EX(SC[9]); SC[10] = EX(SC[10]); PIN(SC); SBAR(); \
            o = MFMA32(v0, BF8(p0), o); PIN(o); SBAR(); v0 = ldsr128(PV0); \
            SC[11] = EX(SC[11]); SC[12] = EX(SC[12]); SC[13] = EX(SC[13]); PIN(SC); SBAR(); \
            ls = MFMA16(sel, BF8(p0), ls); PIN(ls); SBAR(); \
            SC[14] = EX(SC[14]); SC[15] = EX(SC[15]); \
            p1[0] = cvtpk(SC[8], SC[9]); p1[1] = cvtpk(SC[10], SC[11]); p1[2] = cvtpk(SC[12], SC[13]); p1[3] = cvtpk(SC[14], SC[15]); PIN(p1); SBAR(); } while (0)
        int so = 98304;
        STAMP(2, 1);
#pragma unroll 1
        for (int st = 0; st < 8; ++st) {
            const int sn = so == 131072 ? 65536 : so + 32768;
            const lds_ptr pc = pl + so, pn = pl + sn;
            ATT_TILE(sA, sB, pc + 2 * 4096, pc + 1 * 4096 + 2048, pc + 0 * 4096 + 3072);
            ATT_TILE(sB, sA, pc + 3 * 4096, pc + 2 * 4096 + 2048, pc + 1 * 4096 + 3072);
            ATT_TILE(sA, sB, pc + 4 * 4096, pc + 3 * 4096 + 2048, pc + 2 * 4096 + 3072);
            ATT_TILE(sB, sA, pc + 5 * 4096, pc + 4 * 4096 + 2048, pc + 3 * 4096 + 3072);
            ATT_TILE(sA, sB, pc + 6 * 4096, pc + 5 * 4096 + 2048, pc + 4 * 4096 + 3072);
            ATT_TILE(sB, sA, pc + 7 * 4096, pc + 6 * 4096 + 2048, pc + 5 * 4096 + 3072);
            if (st == 4) STAMP(2, 5);
            if (st < 7) asm volatile("s_waitcnt lgkmcnt(0)\n\ts_barrier" ::: "memory");
            if (st == 4) STAMP(2, 6);
            if (st == 5) STAMP(2, 7);
            SBAR();
            ATT_TILE(sA, sB, pn, pc + 7 * 4096 + 2048, pc + 6 * 4096 + 3072);
            ATT_TILE(sB, sA, pn + 4096, pn + 2048, pc + 7 * 4096 + 3072);
            so = sn;
        }
        o = MFMA32(v1, BF8(p1), o);
        ls = MFMA16(sel, BF8(p1), ls);
#undef ATT_TILE
        const float la = __shfl(ls[0], lane & 15), lb = __shfl(ls[1], lane & 15);
        linv = 1.f / ((lane & 16) ? lb : la);
    } else {
        float m = -INFINITY, l = 0.f;
        const f32x16 zero = {};
        int so = 98304;
#pragma unroll 1
        for (int st = 0; st < 8; ++st) {
            const lds_ptr pb = pl + so;
#pragma unroll 1
            for (int t = 0; t < 8; ++t) {
                if (t == 6 && st < 7) asm volatile("s_waitcnt lgkmcnt(0)\n\ts_barrier" ::: "memory");
                const bf16x8 k0 = ldsr128(pb + t * 4096), k1 = ldsr128(pb + t * 4096 + 1024), v0 = ldsr128(pb + t * 4096 + 2048), v1 = ldsr128(pb + t * 4096 + 3072);
                f32x16 s = MFMA32(k0, q0, zero);
                s = MFMA32(k1, q1, s);
                float tm = s[0];
#pragma unroll
                for (int j = 1; j < 16; ++j) tm = fmaxf(tm, s[j]);
                tm = fmaxf(tm, __shfl_xor(tm, 32));
                const float mn = fmaxf(m, tm), al = EX(m - mn);
                m = mn; l *= al;
#pragma unroll
                for (int j = 0; j < 16; ++j) o[j] *= al;
                u32x4 p0, p1;
#pragma unroll
                for (int j = 0; j < 4; ++j) {
                    p0[j] = cvtpk(EX(s[2 * j] - mn), EX(s[2 * j + 1] - mn));
                    p1[j] = cvtpk(EX(s[8 + 2 * j] - mn), EX(s[8 + 2 * j + 1] - mn));
                    l += (bflo(p0[j]) + bfhi(p0[j])) + (bflo(p1[j]) + bfhi(p1[j]));
                }
                o = MFMA32(v0, BF8(p0), o);
                o = MFMA32(v1, BF8(p1), o);
            }
            so = so == 131072 ? 65536 : so + 32768;
        }
        l += __shfl_xor(l, 32);
        linv = 1.f / l;
    }
    STAMP(2, 3);
    {
        const int tb = wid & 3, head = hp * 2 + (wid >> 2), h = lane >> 5, tok = lane & 31;
        unsigned w[8];
#pragma unroll
        for (int j = 0; j < 8; ++j) w[j] = cvtpk(o[2 * j] * linv, o[2 * j + 1] * linv);
        char* ob = ws + WS_O + ((size_t)(rb * 8 + tb * 2 + (tok >> 4)) * 128 + head * 4 + h) * 256 + (tok & 15) * 16;
#pragma unroll
        for (int gp = 0; gp < 2; ++gp) {
            const auto r0 = __builtin_amdgcn_permlane32_swap(w[4 * gp], w[4 * gp + 2], false, false);
            const auto r1 = __builtin_amdgcn_permlane32_swap(w[4 * gp + 1], w[4 * gp + 3], false, false);
            *(u32x4*)(ob + gp * 512) = (u32x4){r0[0], r1[0], r0[1], r1[1]};
        }
    }
    STAMP(2, 4); STAMP_FLUSH(2);
}

template <int NT, int KID>
__global__ __launch_bounds__(768, 3) void k_proj(const char* __restrict__ A, const char* __restrict__ W, const u16* __restrict__ res, int gcol, u16* __restrict__ dst, char* __restrict__ ws) {
    STAMP_DECL; STAMP(KID, 0);
    extern __shared__ __attribute__((aligned(16))) char smem[];
    lds_ptr lds = (lds_ptr)smem;
    const int tid = threadIdx.x, lane = tid & 63, wid = uni(tid >> 6);
    const int xcd = blockIdx.x & 7, idx = blockIdx.x >> 3, rb = (xcd >> 1) * 4 + (idx & 3), ct = (xcd & 1) * 8 + (idx >> 2);
    const unsigned lds0 = uni((unsigned)(uintptr_t)lds);
    if (wid >= 8) {
        const int pw = wid - 8;
        gemm128x64_produce<NT, NT * 8, 4>(A + (size_t)rb * 8 * NT * 8 * 256, W + (size_t)ct * NT * 8192, lds0, pw, lane, [&]() {
            const unsigned voffX = (unsigned)((rb * 128 + pw * 32 + (lane >> 3)) * 2048 + ct * 128 + (((lane & 7) ^ ((lane >> 3) & 7)) << 4));
#pragma unroll
            for (int i = 0; i < 4; ++i) dma16((const char*)res + i * 8 * 2048, voffX, lds0 + 147456 + pw * 4096 + i * 1024); });
        WAITV_BAR(0);
        return;
    }
    const int tb = wid & 3, nh = wid >> 2, g = lane >> 4;
    f32x4 gv[2], acc[2][2];
    gv[0] = mod4(ws, gcol + ct * 64 + nh * 32 + 4 * g); gv[1] = mod4(ws, gcol + ct * 64 + nh * 32 + 16 + 4 * g);
    gemm128x64_consume<NT>(acc, lds, wid, lane, [&](int i) { if (i == 1) STAMP(KID, 4); if (i == 2) STAMP(KID, 5); });
    BARRIER();
    STAMP(KID, 1);
#pragma unroll
    for (int m = 0; m < 2; ++m) {
        const int rl = tb * 32 + m * 16 + (lane & 15);
        unsigned u[2][2];
#pragma unroll
        for (int nf = 0; nf < 2; ++nf) {
            const u32x2 xv = *(const LAS u32x2*)(lds + 147456 + rl * 128 + (((nh * 4 + nf * 2 + (g >> 1)) ^ (rl & 7)) << 4) + 8 * (g & 1));
            u[nf][0] = cvtpk(bflo(xv[0]) + gv[nf][0] * acc[m][nf][0], bfhi(xv[0]) + gv[nf][1] * acc[m][nf][1]);
            u[nf][1] = cvtpk(bflo(xv[1]) + gv[nf][2] * acc[m][nf][2], bfhi(xv[1]) + gv[nf][3] * acc[m][nf][3]);
        }
        const auto r0 = __builtin_amdgcn_permlane16_swap(u[0][0], u[1][0], false, false);
        const auto r1 = __builtin_amdgcn_permlane16_swap(u[0][1], u[1][1], false, false);
        *(u32x4*)(dst + (size_t)(rb * 128 + rl) * D + ct * 64 + nh * 32 + (((g & 1) << 1) | (g >> 1)) * 8) = (u32x4){r0[0], r1[0], r0[1], r1[1]};
    }
    STAMP(KID, 2); STAMP_FLUSH(KID);
}

__global__ __launch_bounds__(512) void k_ln2(char* __restrict__ ws) {
    STAMP_DECL; STAMP(4, 0);
    __shared__ __attribute__((aligned(16))) char smem[32768 + 8192];
    lds_ptr lds = (lds_ptr)smem;
    const int tid = threadIdx.x, lane = tid & 63, wid = uni(tid >> 6), rb = blockIdx.x;
    const u16* x1 = (const u16*)(ws + WS_X1);
    u32x4 xv[2][2];
#pragma unroll
    for (int rr = 0; rr < 2; ++rr)
#pragma unroll
        for (int hh = 0; hh < 2; ++hh) xv[rr][hh] = *(const u32x4*)(x1 + (size_t)(rb * 16 + 2 * wid + rr) * D + hh * 512 + lane * 8);
    LAS float* tab = (LAS float*)(lds + 32768);
    {
        const int t4 = tid * 4;
        if (t4 < 1024) { const f32x4 v = mod4(ws, 4096 + t4); *(LAS f32x4*)(tab + t4) = (f32x4){rbf(1.f + v[0]), rbf(1.f + v[1]), rbf(1.f + v[2]), rbf(1.f + v[3])}; }
        else { *(LAS f32x4*)(tab + t4) = mod4(ws, 2048 + t4); }
    }
    __syncthreads();
    ln_rows_to_lds<false>(xv, (f32x4){0.f, 0.f, 0.f, 0.f}, lds, tab, wid, lane);
    __syncthreads();
#pragma unroll
    for (int i = 0; i < 4; ++i) {
        const int s = wid * 4 + i, c = 4 * s + (lane >> 4);
        *(bf16x8*)(ws + WS_Y + ((size_t)rb * 128 + 4 * s) * 256 + lane * 16) = ldsr128(lds + c * 256 + (((lane & 15) ^ (c & 15)) << 4));
    }
    STAMP(4, 1); STAMP_FLUSH(4);
}

constexpr int GSL = 30720;
__global__ __launch_bounds__(512, 2) void k_glu(char* __restrict__ ws) {
    STAMP_DECL; STAMP(5, 0);
    extern __shared__ __attribute__((aligned(16))) char smem[];
    lds_ptr lds = (lds_ptr)smem;
    const int tid = threadIdx.x, lane = tid & 63, wid = uni(tid >> 6);
    const int xcd = blockIdx.x & 7, idx = blockIdx.x >> 3, rp = (xcd & 1) * 4 + (idx & 3), ct = (xcd >> 1) * 8 + (idx >> 2);
    const unsigned lds0 = uni((unsigned)(uintptr_t)lds);
    const int wm = wid & 3, wn = wid >> 2, g = lane >> 4;
    const char* Ab = ws + WS_Y + (size_t)(rp * 16 + 2 * wid) * 128 * 256;
    const char* Wb = ws + WS_WGU + (size_t)ct * 32 * 14336 + wid * 1024;
    const unsigned voff = lane * 16, voff2 = wid < 6 ? lane * 16 + 8192 : 0u;
    const unsigned dA = lds0 + wid * 2048, dW = lds0 + 16384 + wid * 1024;
#define GLU_D0(t, so) dma16(Wb + (size_t)(t) * 14336, voff, dW + (so))
#define GLU_D1(t, so) dma16(Ab + (size_t)(t) * 1024, voff, dA + (so))
#define GLU_D2(t, so) dma16(Ab + (size_t)(t) * 1024 + 32768, voff, dA + (so) + 1024)
#define GLU_D3(t, so) dma16(Wb + (size_t)(t) * 14336, voff2, wid < 6 ? dW + (so) + 8192 : lds0 + 5 * GSL)
#pragma unroll
    for (int t = 0; t < 4; ++t) { GLU_D0(t, t * GSL); GLU_D1(t, t * GSL); GLU_D2(t, t * GSL); GLU_D3(t, t * GSL); }
    const lds_ptr pa = lds + wm * 4096 + lane * 16, pw = lds + 16384 + wn * 7168 + lane * 16;
    f32x4 acc[4][7];
    bf16x8 aA[4], aB[4], wf[7];
    WAITV_BAR(12);
    STAMP(5, 1);
#pragma unroll
    for (int m = 0; m < 4; ++m) aA[m] = ldsr128(pa + m * 1024);
#pragma unroll
    for (int n = 0; n < 7; ++n) { wf[n] = ldsr128(pw + n * 1024);
#pragma unroll
        for (int m = 0; m < 4; ++m) acc[m][n] = (f32x4){0.f, 0.f, 0.f, 0.f}; }
    int sn = GSL, sf = 4 * GSL;
#define GLU_STEP(AC, AN, t, VM, DO_STAGE, DO_LOAD) do { \
        WAITV_BAR(VM); SBAR(); \
        _Pragma("unroll") for (int n = 0; n < 7; ++n) { \
            _Pragma("unroll") for (int m = 0; m < 4; ++m) acc[m][n] = MFMA16(wf[n], AC[m], acc[m][n]); \
            if (DO_LOAD) { wf[n] = ldsr128(pw + sn + n * 1024); if (n < 4) AN[n] = ldsr128(pa + sn + n * 1024); } \
            if (DO_STAGE) { if (n == 3) GLU_D0((t) + 4, sf); if (n == 4) GLU_D1((t) + 4, sf); if (n == 5) GLU_D2((t) + 4, sf); if (n == 6) GLU_D3((t) + 4, sf); } \
            SBAR(); } \
        sf = sf == 4 * GSL ? 0 : sf + GSL; sn = sn == 4 * GSL ? 0 : sn + GSL; } while (0)
    for (int t = 0; t < 28; t += 2) { GLU_STEP(aA, aB, t, 8, true, true); GLU_STEP(aB, aA, t + 1, 8, true, true); }
    GLU_STEP(aA, aB, 28, 8, false, true); GLU_STEP(aB, aA, 29, 4, false, true); GLU_STEP(aA, aB, 30, 0, false, true); GLU_STEP(aB, aA, 31, 0, false, false);
    STAMP(5, 2);
#undef GLU_STEP
#undef GLU_D0
#undef GLU_D1
#undef GLU_D2
#undef GLU_D3
    char* hb = ws + WS_HH + ((size_t)(rp * 16 + wm * 4) * (F / 8) + ct * 14 + wn * 7) * 256 + (lane & 15) * 16;
    unsigned u2[4][2], ux[2][2];
#pragma unroll
    for (int m = 0; m < 4; ++m) {
        unsigned u[3][2];
#pragma unroll
        for (int f = 0; f < 3; ++f) {
            float hv[4];
#pragma unroll
            for (int r = 0; r < 4; ++r) hv[r] = fmaxf(acc[m][f][r], 0.f) * acc[m][3 + f][r];
            u[f][0] = cvtpk(hv[0], hv[1]); u[f][1] = cvtpk(hv[2], hv[3]);
        }
        const auto r0 = __builtin_amdgcn_permlane16_swap(u[0][0], u[1][0], false, false);
        const auto r1 = __builtin_amdgcn_permlane16_swap(u[0][1], u[1][1], false, false);
        *(u32x4*)(hb + ((size_t)m * (F / 8) + (((g & 1) << 1) | (g >> 1))) * 256) = (u32x4){r0[0], r1[0], r0[1], r1[1]};
        u2[m][0] = u[2][0]; u2[m][1] = u[2][1];
    }
#pragma unroll
    for (int mp = 0; mp < 2; ++mp) {
        const auto r0 = __builtin_amdgcn_permlane16_swap(u2[2 * mp][0], u2[2 * mp + 1][0], false, false);
        const auto r1 = __builtin_amdgcn_permlane16_swap(u2[2 * mp][1], u2[2 * mp + 1][1], false, false);
        *(u32x4*)(hb + ((size_t)(2 * mp + (g & 1)) * (F / 8) + 4 + (g >> 1)) * 256) = (u32x4){r0[0], r1[0], r0[1], r1[1]};
        float hv[4];
#pragma unroll
        for (int r = 0; r < 4; ++r) {
            const auto x = __builtin_amdgcn_permlane32_swap(__float_as_uint(acc[2 * mp][6][r]), __float_as_uint(acc[2 * mp + 1][6][r]), false, false);
            hv[r] = fmaxf(__uint_as_float(x[0]), 0.f) * __uint_as_float(x[1]);
        }
        ux[mp][0] = cvtpk(hv[0], hv[1]); ux[mp][1] = cvtpk(hv[2], hv[3]);
    }
    {
        const auto r0 = __builtin_amdgcn_permlane16_swap(ux[0][0], ux[1][0], false, false);
        const auto r1 = __builtin_amdgcn_permlane16_swap(ux[0][1], ux[1][1], false, false);
        *(u32x4*)(hb + ((size_t)(((g & 1) << 1) | (g >> 1)) * (F / 8) + 6) * 256) = (u32x4){r0[0], r1[0], r0[1], r1[1]};
    }
    STAMP(5, 3); STAMP_FLUSH(5);
}

extern "C" void kernel_launch(void* const* d_in, const int* in_sizes, int n_in, void* d_out, int out_size, void* d_ws, size_t ws_size, hipStream_t stream) {
    const u16* x = (const u16*)d_in[0];
    const u16* wq = (const u16*)d_in[1];
    const u16* wk = (const u16*)d_in[2];
    const u16* wv = (const u16*)d_in[3];
    const u16* wo = (const u16*)d_in[4];
    const u16* wg = (const u16*)d_in[6];
    const u16* wu = (const u16*)d_in[7];
    const u16* wd = (const u16*)d_in[8];
    const u16* c = (const u16*)d_in[9];
    const u16* wmod = (const u16*)d_in[10];
    char* ws = (char*)d_ws;
    (void)hipFuncSetAttribute((const void*)k_ln1kv, hipFuncAttributeMaxDynamicSharedMemorySize, 125440);
    (void)hipFuncSetAttribute((const void*)k_attn, hipFuncAttributeMaxDynamicSharedMemorySize, 163840);
    (void)hipFuncSetAttribute((const void*)k_proj<16, 3>, hipFuncAttributeMaxDynamicSharedMemorySize, 163840);
    (void)hipFuncSetAttribute((const void*)k_glu, hipFuncAttributeMaxDynamicSharedMemorySize, 5 * GSL + 1024);
    (void)hipFuncSetAttribute((const void*)k_proj<56, 6>, hipFuncAttributeMaxDynamicSharedMemorySize, 163840);
    k_pre<<<640, 256, 0, stream>>>(c, wmod, x, wq, ws);
    k_ln1kv<<<256, 512, 125440, stream>>>(x, wk, wv, ws);
    k_attn<<<256, 768, 163840, stream>>>(wo, wg, wu, wd, wmod, ws);
    k_proj<16, 3><<<256, 768, 163840, stream>>>(ws + WS_O, ws + WS_WO, x, 2048, (u16*)(ws + WS_X1), ws);
    k_ln2<<<128, 512, 0, stream>>>(ws);
    k_glu<<<256, 512, 5 * GSL + 1024, stream>>>(ws);
    k_proj<56, 6><<<256, 768, 163840, stream>>>(ws + WS_HH, ws + WS_WD, (const u16*)(ws + WS_X1), 5120, (u16*)d_out, ws);
}
```

```cpp
#include <hip/hip_runtime.h>
#include <hip/hip_bf16.h>

#define MEGA 1
#define LAS __attribute__((address_space(3)))
#define GAS __attribute__((address_space(1)))
typedef unsigned short u16;
typedef short bf16x8 __attribute__((ext_vector_type(8)));
typedef short s16x4 __attribute__((ext_vector_type(4)));
typedef float f32x2 __attribute__((ext_vector_type(2)));
typedef float f32x4 __attribute__((ext_vector_type(4)));
typedef float f32x16 __attribute__((ext_vector_type(16)));
typedef unsigned u32x4 __attribute__((ext_vector_type(4)));
typedef unsigned u32x2 __attribute__((ext_vector_type(2)));
typedef __bf16 bf16x2v __attribute__((ext_vector_type(2)));
typedef LAS char* lds_ptr;

constexpr int D = 1024, F = 3584;
constexpr size_t WS_CNT = 0;
constexpr int C_H1 = 0, C_SIDE = 16, C_O = 32, C_Y = 48, C_HH = 56, C_NUM = 72;
constexpr size_t WS_MP1 = 32768;
constexpr size_t WS_MP2 = WS_MP1 + 4 * 2048 * 4;
constexpr size_t WS_SC = WS_MP2 + 8 * 4096 * 4;
constexpr size_t WS_KN = WS_SC + 2048;
constexpr size_t WS_ST1 = WS_KN + 1024;
constexpr size_t WS_KV = 262144;
constexpr size_t WS_ST2 = 524288;
constexpr size_t WS_UT = 1u << 20;
constexpr size_t WS_H1 = 2u << 20;
constexpr size_t WS_O = WS_H1 + (4u << 20);
constexpr size_t WS_X1 = WS_O + (4u << 20);
constexpr size_t WS_Y = WS_X1 + (4u << 20);
constexpr size_t WS_HH = WS_Y + (4u << 20);
constexpr size_t WS_XCH = WS_HH + (16u << 20);
constexpr size_t WS_WQ = WS_XCH + (4u << 20);
constexpr size_t WS_WO = WS_WQ + (2u << 20);
constexpr size_t WS_WGU = WS_WO + (2u << 20);
constexpr size_t WS_WD = WS_WGU + (16u << 20);

__device__ __forceinline__ float bf2f(u16 b) { return __uint_as_float(((unsigned)b) << 16); }
__device__ __forceinline__ float bflo(unsigned w) { return __uint_as_float(w << 16); }
__device__ __forceinline__ float bfhi(unsigned w) { return __uint_as_float(w & 0xffff0000u); }
__device__ __forceinline__ unsigned cvtpk(float lo, float hi) { return __builtin_bit_cast(unsigned, __builtin_convertvector((f32x2){lo, hi}, bf16x2v)); }
__device__ __forceinline__ float rbf(float x) { return bflo(cvtpk(x, x)); }
__device__ __forceinline__ f32x4 rbf4(f32x4 s) { return (f32x4){rbf(s[0]), rbf(s[1]), rbf(s[2]), rbf(s[3])}; }

__device__ __forceinline__ void dma16(const void* gbase, unsigned voff, unsigned ldsaddr) {
    asm volatile("s_mov_b32 m0, %0\n\ts_nop 3\n\tglobal_load_lds_dwordx4 %1, %2" ::"s"(ldsaddr), "v"(voff), "s"(gbase) : "memory");
}
__device__ __forceinline__ void dmac(const void* gbase, unsigned voff, unsigned ldsaddr) {
    asm volatile("s_mov_b32 m0, %0\n\ts_nop 3\n\tglobal_load_lds_dwordx4 %1, %2 sc1" ::"s"(ldsaddr), "v"(voff), "s"(gbase) : "memory");
}
#define LD16(dst, base, voff) asm volatile("s_nop 4\n\tglobal_load_dwordx4 %0, %1, %2" : "=v"(dst) : "v"(voff), "s"(base) : "memory")
#define LD16C(dst, base, voff) asm volatile("s_nop 4\n\tglobal_load_dwordx4 %0, %1, %2 sc1" : "=v"(dst) : "v"(voff), "s"(base) : "memory")
#define LD8C(dst, base, voff) asm volatile("s_nop 4\n\tglobal_load_dwordx2 %0, %1, %2 sc1" : "=v"(dst) : "v"(voff), "s"(base) : "memory")
template <class T> __device__ __forceinline__ void st16(const void* base, unsigned voff, T v) {
    asm volatile("s_nop 4\n\tglobal_store_dwordx4 %0, %1, %2 sc1\n\ts_nop 1" ::"v"(voff), "v"(v), "s"(base) : "memory");
}
template <class T> __device__ __forceinline__ void st8(const void* base, unsigned voff, T v) { asm volatile("s_nop 4\n\tglobal_store_dwordx2 %0, %1, %2 sc1" ::"v"(voff), "v"(v), "s"(base) : "memory"); }
__device__ __forceinline__ void st4(const void* base, unsigned voff, float v) { asm volatile("s_nop 4\n\tglobal_store_dword %0, %1, %2 sc1" ::"v"(voff), "v"(v), "s"(base) : "memory"); }
#define WAIT_V(n) asm volatile("s_waitcnt vmcnt(" #n ")" ::: "memory")
#define BARRIER() asm volatile("s_barrier" ::: "memory")
#define BAR_L() asm volatile("s_waitcnt lgkmcnt(0)\n\ts_barrier" ::: "memory")
#define WAITV_BAR(n) asm volatile("s_waitcnt vmcnt(" #n ")\n\ts_barrier" ::: "memory")
#define WAITVL_BAR(n) asm volatile("s_waitcnt vmcnt(" #n ") lgkmcnt(0)\n\ts_barrier" ::: "memory")
#define PIN(x) asm volatile("" : "+v"(x))
#define SBAR() __builtin_amdgcn_sched_barrier(0)
#define MFMA32(a, b, c) __builtin_amdgcn_mfma_f32_32x32x16_bf16(a, b, c, 0, 0, 0)
#define MFMA16(a, b, c) __builtin_amdgcn_mfma_f32_16x16x32_bf16(a, b, c, 0, 0, 0)
#define MFMA16_ACC(c, a, b) asm volatile("v_mfma_f32_16x16x32_bf16 %0, %1, %2, %0" : "+v"(c) : "v"(a), "v"(b))
#define ZERO4 ((f32x4){0.f, 0.f, 0.f, 0.f})
__device__ __forceinline__ bf16x8 ldsr128(lds_ptr p) { return *(const LAS bf16x8*)p; }
__device__ __forceinline__ s16x4 ldstr(lds_ptr p) { return __builtin_amdgcn_ds_read_tr16_b64_v4i16((LAS s16x4*)p); }
__device__ __forceinline__ bf16x8 ldstr2(lds_ptr p, int d) { s16x4 a = ldstr(p), b = ldstr(p + d); return (bf16x8){a[0], a[1], a[2], a[3], b[0], b[1], b[2], b[3]}; }
__device__ __forceinline__ unsigned uni(unsigned x) { return (unsigned)__builtin_amdgcn_readfirstlane((int)x); }
__device__ __forceinline__ int lane_id() { int l = __builtin_amdgcn_mbcnt_hi(~0u, __builtin_amdgcn_mbcnt_lo(~0u, 0u)); PIN(l); return l; }
__device__ __forceinline__ u32x4 swap16(unsigned a0, unsigned a1, unsigned b0, unsigned b1) {
    const auto r0 = __builtin_amdgcn_permlane16_swap(a0, b0, false, false), r1 = __builtin_amdgcn_permlane16_swap(a1, b1, false, false);
    return (u32x4){r0[0], r1[0], r0[1], r1[1]};
}

#define STAMP_DECL do {} while (0)
#define STAMP(i) do {} while (0)
#define STAMP_FLUSH(kid) do {} while (0)
#define PSTAMP_FLUSH(kid) do {} while (0)

template <int CTRL> __device__ __forceinline__ float dppf(float v) { return __builtin_bit_cast(float, __builtin_amdgcn_mov_dpp(__builtin_bit_cast(int, v), CTRL, 0xf, 0xf, true)); }
__device__ __forceinline__ float half_sum(float v) { const auto r = __builtin_amdgcn_permlane32_swap(__float_as_uint(v), __float_as_uint(v), false, false); return __uint_as_float(r[0]) + __uint_as_float(r[1]); }
__device__ __forceinline__ float rows_sum(float v) {
    const auto r = __builtin_amdgcn_permlane16_swap(__float_as_uint(v), __float_as_uint(v), false, false); return half_sum(__uint_as_float(r[0]) + __uint_as_float(r[1]));
}
__device__ __forceinline__ float rows_max(float v) {
    auto r = __builtin_amdgcn_permlane16_swap(__float_as_uint(v), __float_as_uint(v), false, false); v = fmaxf(__uint_as_float(r[0]), __uint_as_float(r[1]));
    r = __builtin_amdgcn_permlane32_swap(__float_as_uint(v), __float_as_uint(v), false, false); return fmaxf(__uint_as_float(r[0]), __uint_as_float(r[1]));
}
__device__ __forceinline__ float row16_sum(float v) { v += dppf<0xB1>(v); v += dppf<0x4E>(v); v += dppf<0x141>(v); v += dppf<0x140>(v); return v; }
__device__ __forceinline__ float row16_max(float v) { v = fmaxf(v, dppf<0xB1>(v)); v = fmaxf(v, dppf<0x4E>(v)); v = fmaxf(v, dppf<0x141>(v)); v = fmaxf(v, dppf<0x140>(v)); return v; }
__device__ __forceinline__ float wsum(float v) { return rows_sum(row16_sum(v)); }
__device__ __forceinline__ float wmax(float v) { return rows_max(row16_max(v)); }
__device__ __forceinline__ f32x4 sum8(f32x4 (&p)[8]) {
    asm volatile("" : "+v"(p[0]), "+v"(p[1]), "+v"(p[2]), "+v"(p[3]), "+v"(p[4]), "+v"(p[5]), "+v"(p[6]), "+v"(p[7]));
    return ((p[0] + p[1]) + (p[2] + p[3])) + ((p[4] + p[5]) + (p[6] + p[7]));
}

__device__ __forceinline__ void signal(char* ws, int idx, int lane) {
    if (MEGA) { if (lane == 0) __hip_atomic_fetch_add((GAS unsigned*)(ws + WS_CNT) + idx * 64, 1u, __ATOMIC_RELAXED, __HIP_MEMORY_SCOPE_AGENT); }
}
template <int SLEEP>
__device__ __forceinline__ void await(char* ws, int idx, int n, unsigned need, int lane) {
    if (MEGA) {
        GAS unsigned* p = (GAS unsigned*)(ws + WS_CNT) + (idx + (lane < n ? lane : 0)) * 64;
        for (unsigned spins = 0; spins < (1u << 15); ++spins) {
            if (__all(__hip_atomic_load(p, __ATOMIC_RELAXED, __HIP_MEMORY_SCOPE_AGENT) >= need)) break;
            __builtin_amdgcn_s_sleep(SLEEP);
        }
        asm volatile("s_waitcnt vmcnt(0)" ::: "memory");
    }
}
__device__ __forceinline__ void await_input(char* ws, int pw, int idx, unsigned need, int lane) { if (MEGA) { if (pw == 0) await<6>(ws, idx, 1, need, lane); BARRIER(); } }
#define INPUT_BARRIER() do { if (MEGA) BARRIER(); } while (0)

__device__ __forceinline__ void pack_load(const char* src  , unsigned pitch, unsigned ldsaddr, int lane) {
    const unsigned vo = (unsigned)(lane >> 3) * pitch, sw = (lane & 7) ^ (((lane >> 4) & 1) << 1);
#pragma unroll
    for (int i = 0; i < 4; ++i) dma16(src + (size_t)i * 8 * pitch, vo + ((sw ^ ((i & 1) << 2)) << 4), ldsaddr + i * 1024);
}
__device__ __forceinline__ bf16x8 pack_frag(lds_ptr area, int nf, int lane) {
    const int g = lane >> 4, q = (lane & 15) >> 2;
    return ldstr2(area + (8 * g + q) * 128 + ((nf ^ (((q >> 1) & 1) | ((g & 1) << 1))) << 5) + (lane & 3) * 8, 512);
}
__device__ __forceinline__ void mod_unit(lds_ptr area, bf16x8 sc, lds_ptr red, int lane) {
    PIN(sc);
    f32x4 d[4];
#pragma unroll
    for (int nf = 0; nf < 4; ++nf) d[nf] = MFMA16(pack_frag(area, nf, lane), sc, ZERO4);
    if ((lane & 15) == 0) {
#pragma unroll
        for (int nf = 0; nf < 4; ++nf) *(LAS f32x4*)(red + nf * 64 + (lane >> 4) * 16) = d[nf];
    }
}

__global__ __launch_bounds__(256) void k_pre(const u16* __restrict__ c, const u16* __restrict__ wmod, const u16* __restrict__ x, const u16* __restrict__ wq, char* __restrict__ ws) {
    __shared__ __attribute__((aligned(16))) char smem[16384];
    float (*red)[32] = (float (*)[32])smem;
    STAMP_DECL; STAMP(0);
    const int tid = threadIdx.x, lane = tid & 63, bid = blockIdx.x;
    if (bid < 256) {
        const int idx = bid >> 3, cs = ((((idx & 3) << 3) | (bid & 7)) << 1) | ((idx >> 2) & 1), kq = idx >> 3;
        const int r = tid >> 2, ch = tid & 3;
        u32x4 w[4]; u16 cc[4];
#pragma unroll
        for (int p = 0; p < 4; ++p) { w[p] = *(const u32x4*)(wmod + (size_t)(kq * 256 + p * 64 + r) * 6144 + cs * 32 + ch * 8); cc[p] = c[kq * 256 + p * 64 + r]; }
        if (bid < C_NUM / 4) ((unsigned*)(ws + WS_CNT))[bid * 256 + tid] = 0u;
        float acc[8] = {0.f, 0.f, 0.f, 0.f, 0.f, 0.f, 0.f, 0.f};
#pragma unroll
        for (int p = 0; p < 4; ++p) {
            const float cv = bf2f(cc[p]), sv = rbf(cv / (1.f + __expf(-cv)));
            if (cs == 0 && ch == 0) ((u16*)(ws + WS_SC))[kq * 256 + p * 64 + r] = (u16)(__float_as_uint(sv) >> 16);
#pragma unroll
            for (int j = 0; j < 4; ++j) { acc[2 * j] += sv * bflo(w[p][j]); acc[2 * j + 1] += sv * bfhi(w[p][j]); }
        }
#pragma unroll
        for (int j = 0; j < 8; ++j) { acc[j] += dppf<0x124>(acc[j]); acc[j] += dppf<0x128>(acc[j]); acc[j] = rows_sum(acc[j]); }
        if (lane < 4) {
#pragma unroll
            for (int j = 0; j < 8; ++j) red[tid >> 6][ch * 8 + j] = acc[j];
        }
        __syncthreads();
        if (tid < 32) ((float*)(ws + WS_MP1))[kq * 2048 + cs * 32 + tid] = (red[0][tid] + red[1][tid]) + (red[2][tid] + red[3][tid]);
    } else if (bid >= 512) {
        const int wid = uni(tid >> 6), u = (bid - 512) * 4 + wid, kb = u >> 4, cg = u & 15;
        lds_ptr area = (lds_ptr)smem + wid * 4096;
        pack_load((const char*)wq + (size_t)kb * 32 * 2048 + cg * 128, 2048, uni((unsigned)(uintptr_t)area), lane);
        WAIT_V(0);
#pragma unroll
        for (int nf = 0; nf < 4; ++nf) st16(ws + WS_WQ + (size_t)(cg * 32 + kb) * 4096, lane * 16 + nf * 1024, pack_frag(area, nf, lane));
    } else {
        const int row0 = (bid - 256) * 8 + (tid >> 6) * 2;
        u32x4 xv[2][2];
#pragma unroll
        for (int rr = 0; rr < 2; ++rr)
#pragma unroll
            for (int hh = 0; hh < 2; ++hh) xv[rr][hh] = *(const u32x4*)(x + (size_t)(row0 + rr) * D + hh * 512 + lane * 8);
#pragma unroll
        for (int rr = 0; rr < 2; ++rr) {
            float v[16];
#pragma unroll
            for (int hh = 0; hh < 2; ++hh)
#pragma unroll
                for (int j = 0; j < 4; ++j) { v[hh * 8 + 2 * j] = bflo(xv[rr][hh][j]); v[hh * 8 + 2 * j + 1] = bfhi(xv[rr][hh][j]); }
            float sm = 0.f;
#pragma unroll
            for (int j = 0; j < 16; ++j) sm += v[j];
            const float mean = wsum(sm) * (1.f / 1024.f);
            float q = 0.f;
#pragma unroll
            for (int j = 0; j < 16; ++j) { const float d = v[j] - mean; q += d * d; }
            const float rstd = 1.f / sqrtf(wsum(q) * (1.f / 1024.f) + 1e-5f);
            if (lane == 0) *(f32x2*)(ws + WS_ST1 + (row0 + rr) * 8) = (f32x2){mean, rstd};
        }
    }
    STAMP(1); STAMP_FLUSH(0);
}

struct Ctx { char* ws; lds_ptr lds; unsigned lds0; int bid, w; };

__device__ __forceinline__ void ln1kv_consume(const Ctx& cx, const u16* __restrict__ x) {
    char* ws = cx.ws; const lds_ptr lds = cx.lds; const int wid = cx.w, lane = lane_id(), tid = wid * 64 + lane;
    STAMP_DECL; STAMP(0);
    const int rb = (cx.bid >> 4) * 8 + (cx.bid & 7), isv = (cx.bid >> 3) & 1;
    const float* mp = (const float*)(ws + WS_MP1) + ((tid * 4 + 1024) & 2047);
    const f32x4 m0 = *(const f32x4*)mp, m1 = *(const f32x4*)(mp + 2048), m2 = *(const f32x4*)(mp + 4096), m3 = *(const f32x4*)(mp + 6144);
    u32x4 xv[2][2];
#pragma unroll
    for (int rr = 0; rr < 2; ++rr)
#pragma unroll
        for (int hh = 0; hh < 2; ++hh) xv[rr][hh] = *(const u32x4*)(x + (size_t)(rb * 16 + 2 * wid + rr) * D + hh * 512 + lane * 8);
    const f32x4 st = *(const f32x4*)(ws + WS_ST1 + (rb * 16 + 2 * wid) * 8);
    LAS float* tab = (LAS float*)(lds + 98304);
    {
        f32x4 v = rbf4((m0 + m1) + (m2 + m3));
        if (wid < 4) v = rbf4(v + 1.f);
        *(LAS f32x4*)(tab + tid * 4) = v;
    }
    BAR_L();
    STAMP(1);
    const lds_ptr img = lds + 65536;
#pragma unroll
    for (int hh = 0; hh < 2; ++hh) {
        const int c = hh * 64 + lane;
        const f32x4 g0 = *(const LAS f32x4*)(tab + c * 8), g1 = *(const LAS f32x4*)(tab + c * 8 + 4);
        const f32x4 b0 = *(const LAS f32x4*)(tab + 1024 + c * 8), b1 = *(const LAS f32x4*)(tab + 1024 + c * 8 + 4);
#pragma unroll
        for (int rr = 0; rr < 2; ++rr) {
            const float mean = st[2 * rr], rstd = st[2 * rr + 1];
            float h[8];
#pragma unroll
            for (int j = 0; j < 2; ++j) {
                h[2 * j] = (bflo(xv[rr][hh][j]) - mean) * rstd * g0[2 * j] + b0[2 * j]; h[2 * j + 1] = (bfhi(xv[rr][hh][j]) - mean) * rstd * g0[2 * j + 1] + b0[2 * j + 1];
                h[4 + 2 * j] = (bflo(xv[rr][hh][2 + j]) - mean) * rstd * g1[2 * j] + b1[2 * j]; h[5 + 2 * j] = (bfhi(xv[rr][hh][2 + j]) - mean) * rstd * g1[2 * j + 1] + b1[2 * j + 1];
            }
            *(LAS u32x4*)(img + c * 256 + (((2 * wid + rr) ^ (c & 15)) << 4)) = (u32x4){cvtpk(h[0], h[1]), cvtpk(h[2], h[3]), cvtpk(h[4], h[5]), cvtpk(h[6], h[7])};
        }
    }
    BAR_L();
    STAMP(2);
    f32x4 acc[2] = {ZERO4, ZERO4};
    const int g = lane >> 4, q = (lane & 15) >> 2, p = lane & 3;
    bf16x8 hf[4], wf[4][2];
#pragma unroll
    for (int i = 0; i < 4; ++i) {
        const int s = wid * 4 + i, cc = 4 * s + g;
        hf[i] = ldsr128(img + cc * 256 + (((lane & 15) ^ (cc & 15)) << 4));
#pragma unroll
        for (int nf = 0; nf < 2; ++nf) wf[i][nf] = ldstr2(lds + (32 * s + 8 * g + q) * 64 + ((nf ^ (g & 1)) << 5) + 8 * p, 256);
    }
    LAS float* red = (LAS float*)(lds + 147456);
    const char* hb = ws + WS_H1 + ((size_t)rb * 128 + 16 * wid) * 256;
    if (isv) {
#pragma unroll
        for (int i = 0; i < 4; ++i) { acc[0] = MFMA16(hf[i], wf[i][0], acc[0]); acc[1] = MFMA16(hf[i], wf[i][1], acc[1]); }
        st16(hb, lane * 16 + 1024, hf[1]); st16(hb, lane * 16 + 3072, hf[3]);
#pragma unroll
        for (int nf = 0; nf < 2; ++nf) *(LAS f32x4*)(red + wid * 512 + (nf * 16 + (lane & 15)) * 16 + 4 * g) = acc[nf];
    } else {
#pragma unroll
        for (int i = 0; i < 4; ++i) { acc[0] = MFMA16(wf[i][0], hf[i], acc[0]); acc[1] = MFMA16(wf[i][1], hf[i], acc[1]); }
        st16(hb, lane * 16, hf[0]); st16(hb, lane * 16 + 2048, hf[2]);
#pragma unroll
        for (int nf = 0; nf < 2; ++nf) *(LAS f32x4*)(red + wid * 512 + (lane & 15) * 32 + nf * 16 + 4 * g) = acc[nf];
    }
    BAR_L();
    STAMP(3);
    if (wid == 0) {
        const int o0 = isv ? (lane & 31) * 16 + 4 * (lane >> 5) : (lane & 15) * 32 + g * 8, o1 = o0 + (isv ? 8 : 4);
        f32x4 a = ZERO4, b = ZERO4;
#pragma unroll
        for (int w = 0; w < 8; ++w) { a += *(const LAS f32x4*)(red + w * 512 + o0); b += *(const LAS f32x4*)(red + w * 512 + o1); }
        const u32x4 v = {cvtpk(a[0], a[1]), cvtpk(a[2], a[3]), cvtpk(b[0], b[1]), cvtpk(b[2], b[3])};
        st16(ws + WS_KV + (size_t)(rb >> 1) * 4096, isv ? 2048 + (rb & 1) * 1024 + lane * 16 : (g >> 1) * 1024 + ((g & 1) * 32 + (rb & 1) * 16 + (lane & 15)) * 16, v);
        float n2 = 0.f, mx = 0.f;
#pragma unroll
        for (int j = 0; j < 4; ++j) { n2 += bflo(v[j]) * bflo(v[j]) + bfhi(v[j]) * bfhi(v[j]); mx = fmaxf(mx, fmaxf(fabsf(bflo(v[j])), fabsf(bfhi(v[j])))); }
        if (!isv) mx = rows_sum(n2);
        if (!(n2 - n2 == 0.f)) mx = INFINITY;
        mx = wmax(mx);
        if (lane == 0) st4(ws + WS_KN, (unsigned)(isv * 128 + rb) * 4u, mx);
    }
    WAITV_BAR(0);
    STAMP(4); STAMP_FLUSH(1);
}
template <class Next>
__device__ __forceinline__ void ln1kv_produce(const Ctx& cx, const u16* __restrict__ wk, const u16* __restrict__ wv, const u16* __restrict__ wmod, Next next_pre) {
    char* ws = cx.ws; const int pw = cx.w, lane = lane_id();
    __builtin_amdgcn_s_sleep(8);
    {
        const char* w = (const char*)(((cx.bid >> 3) & 1) ? wv : wk) + pw * 16384;
        const unsigned voff = (lane >> 2) * 64 + (((lane & 3) ^ (((lane >> 5) & 1) << 1)) << 4);
#pragma unroll
        for (int i = 0; i < 16; ++i) dma16(w + i * 1024, voff, cx.lds0 + pw * 16384 + i * 1024);
    }
    const int k0 = (cx.bid >> 5) * 128 + pw * 32, col2 = 1024 + (cx.bid & 31) * 64;
    bf16x8 sc;
    pack_load((const char*)wmod + (size_t)k0 * 12288 + (2048 + col2) * 2, 12288, cx.lds0 + 126976 + pw * 4096, lane);
    LD16(sc, ws + WS_SC, (unsigned)(k0 + (lane >> 4) * 8) * 2u);
    BARRIER();
    WAITV_BAR(5);
    BARRIER();
    WAIT_V(0);
    mod_unit(cx.lds + 126976 + pw * 4096, sc, cx.lds + 143360 + pw * 256, lane);
    next_pre();
    BAR_L();
    if (pw == 0) {
        signal(ws, C_H1 + (cx.bid >> 4), lane);
        const LAS float* r = (const LAS float*)(cx.lds + 143360) + lane;
        st4(ws + WS_MP2, (unsigned)((cx.bid >> 5) * 4096 + col2 + lane) * 4u, (r[0] + r[64]) + (r[128] + r[192]));
    }
}

__device__ __forceinline__ void g64_pre(const Ctx& cx, const char* Wp, int lane) {
#pragma unroll
    for (int t = 0; t < 5; ++t) { dmac(Wp + cx.w * 2048 + t * 8192, lane * 16, cx.lds0 + 16384 + cx.w * 2048 + t * 24576); dmac(Wp + cx.w * 2048 + t * 8192 + 1024, lane * 16, cx.lds0 + 16384 + cx.w * 2048 + t * 24576 + 1024); }
}
template <int NTAIL, class First, class Tail>
__device__ __forceinline__ void g64_produce(const Ctx& cx, const char* Ap, const char* Wp, int lane, First first, Tail tail) {
    const char* Aw = Ap + (size_t)(2 * cx.w) * 32768; const char* Ww = Wp + cx.w * 2048;
    const unsigned voff = lane * 16, dA = cx.lds0 + cx.w * 4096, dW = cx.lds0 + 16384 + cx.w * 2048;
#define G64_A(t, so) do { const char* a_ = Aw + (size_t)(t) * 2048; dmac(a_, voff, dA + (so)); dmac(a_ + 32768, voff, dA + (so) + 2048); dmac(a_ + 1024, voff, dA + (so) + 1024); dmac(a_ + 33792, voff, dA + (so) + 3072); } while (0)
#define G64_STAGE(t, so) do { const char* w_ = Ww + (size_t)(t) * 8192; dmac(w_, voff, dW + (so)); dmac(w_ + 1024, voff, dW + (so) + 1024); G64_A(t, so); } while (0)
#pragma unroll
    for (int t = 0; t < 5; ++t) G64_A(t, t * 24576);
    WAITV_BAR(16);
    first();
    WAITV_BAR(12); G64_STAGE(5, 5 * 24576);
    WAITV_BAR(14); G64_STAGE(6, 0);
    WAITV_BAR(16); G64_STAGE(7, 24576);
    int sf = 2 * 24576;
#pragma unroll 1
    for (int t = 3; t < 11; ++t) {
        WAITV_BAR(18);
        G64_STAGE(t + 5, sf);
        sf = sf == 5 * 24576 ? 0 : sf + 24576;
    }
    WAITV_BAR(18);
    WAITV_BAR(12);
    tail();
    asm volatile("s_waitcnt vmcnt(%0)\n\ts_barrier" ::"i"(6 + NTAIL) : "memory");
    asm volatile("s_waitcnt vmcnt(%0) lgkmcnt(0)\n\ts_barrier" ::"i"(NTAIL) : "memory");
#undef G64_STAGE
#undef G64_A
}
template <class Mid, class Late, class St>
__device__ __forceinline__ void g64_consume(f32x4 (&acc)[2][2], const Ctx& cx, int lane, Mid mid, Late late, St stamp) {
    const lds_ptr pa = cx.lds + (cx.w & 3) * 4096 + lane * 16, pw = cx.lds + 16384 + (cx.w >> 2) * 2048 + lane * 16;
#pragma unroll
    for (int m = 0; m < 2; ++m) { acc[m][0] = ZERO4; acc[m][1] = ZERO4; }
    bf16x8 aA[4], wA[4], aB[4], wB[4];
    INPUT_BARRIER();
    BARRIER(); SBAR();
    stamp();
#pragma unroll
    for (int ks = 0; ks < 2; ++ks) { wA[2 * ks] = ldsr128(pw + ks * 4096); aA[2 * ks] = ldsr128(pa + ks * 1024); wA[2 * ks + 1] = ldsr128(pw + ks * 4096 + 1024); aA[2 * ks + 1] = ldsr128(pa + 2048 + ks * 1024); }
    SBAR();
    int sn = 24576;
#define G64_STEP(AC, WC, AN, WN, DO_NEXT, HOOK) do { \
        if (DO_NEXT) BARRIER(); SBAR(); HOOK; \
        _Pragma("unroll") for (int i = 0; i < 4; ++i) { const int ks = i >> 1, m = i & 1; \
            if (DO_NEXT) { WN[i] = ldsr128(pw + sn + ks * 4096 + m * 1024); AN[i] = ldsr128(pa + sn + m * 2048 + ks * 1024); } \
            acc[m][0] = MFMA16(WC[2 * ks], AC[i], acc[m][0]); acc[m][1] = MFMA16(WC[2 * ks + 1], AC[i], acc[m][1]); PIN(acc[m][0]); PIN(acc[m][1]); \
            SBAR(); } \
        sn = sn == 5 * 24576 ? 0 : sn + 24576; } while (0)
#pragma unroll 1
    for (int t = 0; t < 14; t += 2) {
        mid(t);
        G64_STEP(aA, wA, aB, wB, true, (void)0);
        G64_STEP(aB, wB, aA, wA, true, (void)0);
    }
    G64_STEP(aA, wA, aB, wB, true, late());
    G64_STEP(aB, wB, aA, wA, false, (void)0);
#undef G64_STEP
}

__device__ __forceinline__ void tile128x64(int bid, int& rb, int& ct) { const int xcd = bid & 7, idx = bid >> 3; rb = (xcd >> 1) * 4 + (idx & 3); ct = (xcd & 1) * 8 + (idx >> 2); }
__device__ __forceinline__ void attn_pre(const Ctx& cx) { int rb, hp; tile128x64(cx.bid, rb, hp); g64_pre(cx, cx.ws + WS_WQ + (size_t)hp * 131072, lane_id()); }
template <class Next>
__device__ __forceinline__ void attn_produce(const Ctx& cx, const u16* __restrict__ wo, const u16* __restrict__ wg, const u16* __restrict__ wu, const u16* __restrict__ wd, const u16* __restrict__ wmod, Next next_pre) {
    char* ws = cx.ws; const int pw = cx.w, lane = lane_id();
    int rb, hp; tile128x64(cx.bid, rb, hp);
    const char* kvg = ws + WS_KV + pw * 8192;
    const unsigned voff = lane * 16, kvd = cx.lds0 + pw * 8192;
#define KV_STAGE(st, so) do { _Pragma("unroll") for (int i_ = 0; i_ < 8; ++i_) dmac(kvg + (st) * 32768 + i_ * 1024, voff, kvd + (so) + i_ * 1024); } while (0)
    STAMP_DECL; STAMP(0);
    await_input(ws, pw, C_H1 + rb, 16, lane);
    STAMP(1);
    if (pw == 0) signal(ws, C_SIDE + (cx.bid & 15), lane);
    g64_produce<16>(cx, ws + WS_H1 + (size_t)rb * 8 * 32768, ws + WS_WQ + (size_t)hp * 131072, lane, [&]() { STAMP(2); },
                    [&]() { KV_STAGE(0, 98304); KV_STAGE(1, 131072); });
    WAITV_BAR(8);
    const lds_ptr area = cx.lds + pw * 16384; const unsigned area0 = cx.lds0 + pw * 16384;
    const int k0 = (cx.bid >> 5) * 128 + pw * 32, col2 = (cx.bid & 15) * 64 + ((cx.bid >> 4) & 1) * 3072;
    const int gw = cx.bid * 4 + pw, isgu = gw < 896, mat = gw >= 448, k8 = (gw - mat * 448) / 56, cg = gw - mat * 448 - k8 * 56;
    bf16x8 sc; f32x4 vp[8];
    pack_load((const char*)wmod + (size_t)k0 * 12288 + (2048 + col2) * 2, 12288, area0, lane);
    LD16(sc, ws + WS_SC, (unsigned)(k0 + (lane >> 4) * 8) * 2u);
    auto unit_load = [&](int j) {
        const unsigned dst = area0 + 8192 + (j & 1) * 4096;
        if (!isgu) { const int u = (gw - 896) * 4 + (j & 3); pack_load((const char*)wo + (size_t)(u >> 4) * 65536 + (u & 15) * 128, 2048, dst, lane); }
        else if (j < 4) pack_load((const char*)(mat ? wu : wg) + (size_t)(k8 * 4 + j) * 32 * 7168 + cg * 128, 7168, dst, lane);
        else { const int u = (j - 4) * 896 + gw; pack_load((const char*)wd + (size_t)(u >> 4) * 65536 + (u & 15) * 128, 2048, dst, lane); }
    };
    f32x4 ut[4] = {ZERO4, ZERO4, ZERO4, ZERO4};
    unsigned guo[4];
#pragma unroll
    for (int nf = 0; nf < 4; ++nf) {
        const int cj = cg * 8 + 2 * nf + ((lane >> 3) & 1), ct = cj / 14, r = cj - ct * 14, wn = r >= 7, c = r - wn * 7;
        const int fr = c < 6 ? mat * 3 + (c >> 1) : 6, hf = c < 6 ? (c & 1) : mat;
        guo[nf] = (unsigned)((ct * 32 * 14 + wn * 7 + fr) * 1024 + (lane >> 4) * 256 + (hf * 8 + (lane & 7)) * 16);
        PIN(guo[nf]);
    }
    auto unit_store = [&](int j) {
        const lds_ptr src = area + 8192 + (j & 1) * 4096;
        bf16x8 f[4];
#pragma unroll
        for (int nf = 0; nf < 4; ++nf) f[nf] = pack_frag(src, nf, lane);
        if (!isgu || j >= 4) {
            const int u = isgu ? (j - 4) * 896 + gw : (gw - 896) * 4 + (j & 3);
            const char* d = isgu ? ws + WS_WD + (size_t)((((u & 15) >> 1) * 112 + (u >> 4)) * 2 + (u & 1)) * 4096 : ws + WS_WO + (size_t)((u & 15) * 32 + (u >> 4)) * 4096;
#pragma unroll
            for (int nf = 0; nf < 4; ++nf) st16(d, voff + nf * 1024, f[nf]);
        } else {
            bf16x8 vf = ldsr128(area + 4096 + (lane & 1) * 256 + j * 64 + (lane >> 4) * 16);
#pragma unroll
            for (int nf = 0; nf < 4; ++nf) {
                st16(ws + WS_WGU + (size_t)(k8 * 4 + j) * 14336, guo[nf], f[nf]);
                ut[nf] = MFMA16(f[nf], vf, ut[nf]);
            }
        }
    };
    {
        const unsigned vo = (unsigned)((lane < 32 ? 2048 : 1024) + (k8 & 7) * 128 + (lane & 31) * 4) * 4u;
#pragma unroll
        for (int i = 0; i < 8; ++i) LD16C(vp[i], ws + WS_MP2, vo + i * 16384);
    }
    unit_load(0);
    STAMP(3);
    WAITV_BAR(17);
    KV_STAGE(2, 65536);
    WAIT_V(8);
    mod_unit(area, sc, area + 5120, lane);
    {
        f32x4 v = rbf4(sum8(vp));
        if (lane < 32) v = rbf4(v + 1.f);
        *(LAS u32x2*)(area + 4096 + lane * 8) = (u32x2){cvtpk(v[0], v[1]), cvtpk(v[2], v[3])};
    }
    unit_store(0); unit_load(1);
    STAMP(4);
    WAITVL_BAR(8);
    KV_STAGE(3, 98304);
    if (pw == 0) {
        const LAS float* r = (const LAS float*)(cx.lds + 5120) + lane;
        st4(ws + WS_MP2, (unsigned)((cx.bid >> 5) * 4096 + col2 + lane) * 4u, (r[0] + r[4096]) + (r[8192] + r[12288]));
    }
    WAIT_V(8); unit_store(1); unit_load(2);
    STAMP(5);
    WAITVL_BAR(8); KV_STAGE(4, 131072); WAIT_V(8); unit_store(2); unit_load(3);
    WAITVL_BAR(8); KV_STAGE(5, 65536); WAIT_V(8); unit_store(3); unit_load(4);
    if (isgu && (lane & 15) < 2) {
#pragma unroll
        for (int nf = 0; nf < 4; ++nf) st16(ws + WS_UT, (unsigned)((k8 * 4 + mat * 2 + (lane & 15)) * F + cg * 64 + nf * 16 + (lane >> 4) * 4) * 4u, ut[nf]);
    }
    WAITVL_BAR(8); KV_STAGE(6, 98304); WAIT_V(8); unit_store(4); unit_load(5);
    WAITVL_BAR(8); KV_STAGE(7, 131072); WAIT_V(8); unit_store(5);
    {
        const u32x4 e = {~0u, ~0u, ~0u, ~0u};
#pragma unroll
        for (int i = 0; i < 4; ++i) st16(ws + WS_XCH + (size_t)gw * 4096, voff + i * 1024, e);
    }
    WAITVL_BAR(0);
    if (pw == 0) signal(ws, C_SIDE + (cx.bid & 15), lane);
#undef KV_STAGE
    if (MEGA && pw == 0) { __builtin_amdgcn_s_sleep(40); await<20>(ws, C_SIDE, 16, 32, lane); }
    BARRIER();
    STAMP(6);
    next_pre();
    STAMP(7);
    BARRIER();
    if (pw == 0) signal(ws, C_O + rb, lane);
    PSTAMP_FLUSH(10);
}
__device__ __forceinline__ void attn_consume(const Ctx& cx) {
    char* ws = cx.ws; const lds_ptr lds = cx.lds; const int wid = cx.w, lane = lane_id();
    STAMP_DECL; STAMP(0);
    f32x4 qa[2][2]; f32x2 kn2, vm2;
    unsigned cv = 0u;
    g64_consume(qa, cx, lane, [&](int t) {
            if (MEGA && wid == 0) {
                if (t == 6) asm volatile("s_nop 4\n\tglobal_load_dword %0, %1, %2 sc1" : "+v"(cv) : "v"((lane & 31) * 256), "s"(ws + WS_CNT) : "memory");
                if (t == 10) { asm volatile("s_waitcnt vmcnt(0)" : "+v"(cv) :: "memory"); if (!__all(cv >= 16u)) await<6>(ws, C_H1, 32, 16, lane); }
            } },
        [&]() { LD8C(kn2, ws + WS_KN, lane * 8); LD8C(vm2, ws + WS_KN, lane * 8 + 512); }, [&]() { STAMP(5); });
    BAR_L();
    STAMP(2);
    constexpr float QS = 0.17677669529663687f * 1.4426950408889634f;
    u32x4 qw0, qw1;
    qw0 = swap16(cvtpk(qa[0][0][0] * QS, qa[0][0][1] * QS), cvtpk(qa[0][0][2] * QS, qa[0][0][3] * QS), cvtpk(qa[1][0][0] * QS, qa[1][0][1] * QS), cvtpk(qa[1][0][2] * QS, qa[1][0][3] * QS));
    qw1 = swap16(cvtpk(qa[0][1][0] * QS, qa[0][1][1] * QS), cvtpk(qa[0][1][2] * QS, qa[0][1][3] * QS), cvtpk(qa[1][1][0] * QS, qa[1][1][1] * QS), cvtpk(qa[1][1][2] * QS, qa[1][1][3] * QS));
    float qn = 0.f;
#pragma unroll
    for (int j = 0; j < 4; ++j) { qn += bflo(qw0[j]) * bflo(qw0[j]) + bfhi(qw0[j]) * bfhi(qw0[j]) + bflo(qw1[j]) * bflo(qw1[j]) + bfhi(qw1[j]) * bfhi(qw1[j]); }
    qn = half_sum(qn);
    asm volatile("s_waitcnt vmcnt(0)" : "+v"(kn2), "+v"(vm2) :: "memory");
    const float knm = wmax(fmaxf(kn2[0], kn2[1])), vmm = wmax(fmaxf(vm2[0], vm2[1]));
    const bool fast = __all((qn * knm <= 88.f * 88.f) && (vmm <= 16777216.f) && (vmm >= 9.5367431640625e-7f));
    const bf16x8 q0 = __builtin_bit_cast(bf16x8, qw0), q1 = __builtin_bit_cast(bf16x8, qw1);
    f32x16 o = {};
    float linv;
    const lds_ptr pl = lds + lane * 16;
#define EX(x) __builtin_amdgcn_exp2f(x)
#define BF8(x) __builtin_bit_cast(bf16x8, x)
    if (fast) {
        const short ov = ((lane & 15) == ((lane >> 4) & 1)) ? (short)0x3f80 : (short)0;
        const bf16x8 sel = {ov, ov, ov, ov, ov, ov, ov, ov};
        f32x4 ls = ZERO4;
        const f32x16 zero = {};
        f32x16 sA, sB;
        bf16x8 k0, k1, v0, v1;
        u32x4 p0, p1 = {0u, 0u, 0u, 0u};
        {
            const lds_ptr pb = pl + 98304;
            k0 = ldsr128(pb); k1 = ldsr128(pb + 1024);
            sA = MFMA32(k0, q0, zero); sA = MFMA32(k1, q1, sA);
            k0 = ldsr128(pb + 4096); k1 = ldsr128(pb + 4096 + 1024); v0 = ldsr128(pb + 2048); v1 = v0;
        }
#define ATT_TILE(SC, SN, PK, PV0, PV1) do { \
            SN = MFMA32(k0, q0, zero); PIN(SN); SBAR(); k0 = ldsr128(PK); \
            SC[0] = EX(SC[0]); SC[1] = EX(SC[1]); SC[2] = EX(SC[2]); SC[3] = EX(SC[3]); PIN(SC); SBAR(); \
            o = MFMA32(v1, BF8(p1), o); PIN(o); SBAR(); v1 = ldsr128(PV1); \
            SC[4] = EX(SC[4]); SC[5] = EX(SC[5]); SC[6] = EX(SC[6]); PIN(SC); SBAR(); \
            ls = MFMA16(sel, BF8(p1), ls); PIN(ls); SBAR(); SC[7] = EX(SC[7]); p0[0] = cvtpk(SC[0], SC[1]); p0[1] = cvtpk(SC[2], SC[3]); PIN(SC); PIN(p0); SBAR(); \
            SN = MFMA32(k1, q1, SN); PIN(SN); SBAR(); k1 = ldsr128((PK) + 1024); \
            p0[2] = cvtpk(SC[4], SC[5]); p0[3] = cvtpk(SC[6], SC[7]); PIN(p0); \
            SC[8] = EX(SC[8]); SC[9] = EX(SC[9]); SC[10] = EX(SC[10]); PIN(SC); SBAR(); \
            o = MFMA32(v0, BF8(p0), o); PIN(o); SBAR(); v0 = ldsr128(PV0); \
            SC[11] = EX(SC[11]); SC[12] = EX(SC[12]); SC[13] = EX(SC[13]); PIN(SC); SBAR(); \
            ls = MFMA16(sel, BF8(p0), ls); PIN(ls); SBAR(); \
            SC[14] = EX(SC[14]); SC[15] = EX(SC[15]); \
            p1[0] = cvtpk(SC[8], SC[9]); p1[1] = cvtpk(SC[10], SC[11]); p1[2] = cvtpk(SC[12], SC[13]); p1[3] = cvtpk(SC[14], SC[15]); PIN(p1); SBAR(); } while (0)
        int so = 98304;
        STAMP(1);
#pragma unroll 1
        for (int st = 0; st < 8; ++st) {
            const int sn = so == 131072 ? 65536 : so + 32768;
            const lds_ptr pc = pl + so, pn = pl + sn;
            ATT_TILE(sA, sB, pc + 2 * 4096, pc + 1 * 4096 + 2048, pc + 0 * 4096 + 3072);
            ATT_TILE(sB, sA, pc + 3 * 4096, pc + 2 * 4096 + 2048, pc + 1 * 4096 + 3072);
            ATT_TILE(sA, sB, pc + 4 * 4096, pc + 3 * 4096 + 2048, pc + 2 * 4096 + 3072);
            ATT_TILE(sB, sA, pc + 5 * 4096, pc + 4 * 4096 + 2048, pc + 3 * 4096 + 3072);
            ATT_TILE(sA, sB, pc + 6 * 4096, pc + 5 * 4096 + 2048, pc + 4 * 4096 + 3072);
            ATT_TILE(sB, sA, pc + 7 * 4096, pc + 6 * 4096 + 2048, pc + 5 * 4096 + 3072);
            if (st < 7) BAR_L();
            if (st == 1) STAMP(6);
            if (st == 4) STAMP(7);
            SBAR();
            ATT_TILE(sA, sB, pn, pc + 7 * 4096 + 2048, pc + 6 * 4096 + 3072);
            ATT_TILE(sB, sA, pn + 4096, pn + 2048, pc + 7 * 4096 + 3072);
            so = sn;
        }
        o = MFMA32(v1, BF8(p1), o);
        ls = MFMA16(sel, BF8(p1), ls);
#undef ATT_TILE
        const float la = __shfl(ls[0], lane & 15), lb = __shfl(ls[1], lane & 15);
        linv = 1.f / ((lane & 16) ? lb : la);
    } else {
        float m = -INFINITY, l = 0.f;
        const f32x16 zero = {};
        int so = 98304;
#pragma unroll 1
        for (int st = 0; st < 8; ++st) {
            const lds_ptr pb = pl + so;
#pragma unroll 1
            for (int t = 0; t < 8; ++t) {
                if (t == 6 && st < 7) BAR_L();
                const bf16x8 k0 = ldsr128(pb + t * 4096), k1 = ldsr128(pb + t * 4096 + 1024), v0 = ldsr128(pb + t * 4096 + 2048), v1 = ldsr128(pb + t * 4096 + 3072);
                f32x16 s = MFMA32(k0, q0, zero);
                s = MFMA32(k1, q1, s);
                float tm = s[0];
#pragma unroll
                for (int j = 1; j < 16; ++j) tm = fmaxf(tm, s[j]);
                tm = fmaxf(tm, __shfl_xor(tm, 32));
                const float mn = fmaxf(m, tm), al = EX(m - mn);
                m = mn; l *= al;
#pragma unroll
                for (int j = 0; j < 16; ++j) o[j] *= al;
                u32x4 p0, p1;
#pragma unroll
                for (int j = 0; j < 4; ++j) {
                    p0[j] = cvtpk(EX(s[2 * j] - mn), EX(s[2 * j + 1] - mn));
                    p1[j] = cvtpk(EX(s[8 + 2 * j] - mn), EX(s[8 + 2 * j + 1] - mn));
                    l += (bflo(p0[j]) + bfhi(p0[j])) + (bflo(p1[j]) + bfhi(p1[j]));
                }
                o = MFMA32(v0, BF8(p0), o);
                o = MFMA32(v1, BF8(p1), o);
            }
            so = so == 131072 ? 65536 : so + 32768;
        }
        linv = 1.f / half_sum(l);
    }
#undef EX
#undef BF8
    STAMP(3);
    {
        int rb, hp; tile128x64(cx.bid, rb, hp);
        const char* ob = ws + WS_O + ((size_t)(rb * 8 + (wid & 3) * 2) * 128 + (hp * 2 + (wid >> 2)) * 4) * 256;
        const unsigned vo = (unsigned)(((lane >> 4) & 1) * 32768 + (lane >> 5) * 256 + (lane & 15) * 16);
#pragma unroll
        for (int gp = 0; gp < 2; ++gp) {
            const auto r0 = __builtin_amdgcn_permlane32_swap(cvtpk(o[8 * gp] * linv, o[8 * gp + 1] * linv), cvtpk(o[8 * gp + 4] * linv, o[8 * gp + 5] * linv), false, false);
            const auto r1 = __builtin_amdgcn_permlane32_swap(cvtpk(o[8 * gp + 2] * linv, o[8 * gp + 3] * linv), cvtpk(o[8 * gp + 6] * linv, o[8 * gp + 7] * linv), false, false);
            st16(ob, vo + gp * 512, ((u32x4){r0[0], r1[0], r0[1], r1[1]}));
        }
    }
    BAR_L();
    WAITV_BAR(0);
    STAMP(4); STAMP_FLUSH(2);
}

struct OPre { f32x4 p[8]; };
__device__ __forceinline__ void oproj_pre(const Ctx& cx, OPre& o) {
    const int lane = lane_id(); int rb, ct; tile128x64(cx.bid, rb, ct);
    const unsigned vo = (unsigned)(((lane >> 4) & 1) * 2048 + ct * 64 + (lane & 15) * 4) * 4u;
#pragma unroll
    for (int i = 0; i < 8; ++i) LD16C(o.p[i], cx.ws + WS_MP2, vo + i * 16384);
    g64_pre(cx, cx.ws + WS_WO + (size_t)ct * 131072, lane);
}
template <class Next>
__device__ __forceinline__ void oproj_produce(const Ctx& cx, OPre& o, const u16* __restrict__ x, Next next_pre) {
    char* ws = cx.ws; const int pw = cx.w, lane = lane_id();
    int rb, ct; tile128x64(cx.bid, rb, ct);
    STAMP_DECL; STAMP(0);
    await_input(ws, pw, C_O + rb, 16, lane);
    STAMP(1);
    f32x4 tv;
    g64_produce<4>(cx, ws + WS_O + (size_t)rb * 8 * 32768, ws + WS_WO + (size_t)ct * 131072, lane,
        [&]() { STAMP(2); tv = rbf4(sum8(o.p)); if (lane & 16) tv = rbf4(tv + 1.f); PIN(tv); },
        [&]() {
            const unsigned voffX = (unsigned)((rb * 128 + pw * 32 + (lane >> 3)) * 2048 + ct * 128 + (((lane & 7) ^ ((lane >> 3) & 7)) << 4));
#pragma unroll
            for (int i = 0; i < 4; ++i) dma16((const char*)x + i * 8 * 2048, voffX, cx.lds0 + 147456 + pw * 4096 + i * 1024);
            if (pw == 0 && lane < 32) *(LAS f32x4*)(cx.lds + 98304 + lane * 16) = tv; });
    WAITV_BAR(0);
    BARRIER();
    next_pre();
    BARRIER();
    if (pw == 0) signal(ws, C_Y + (rb >> 1), lane);
    PSTAMP_FLUSH(11);
}
__device__ __forceinline__ void oproj_consume(const Ctx& cx) {
    char* ws = cx.ws; const lds_ptr lds = cx.lds; const int wid = cx.w, lane = lane_id();
    STAMP_DECL; STAMP(0);
    f32x4 acc[2][2];
    g64_consume(acc, cx, lane, [](int) {}, []() {}, [&]() { STAMP(4); });
    BAR_L();
    STAMP(1);
    int rb, ct; tile128x64(cx.bid, rb, ct);
    const int tb = wid & 3, nh = wid >> 2, g = lane >> 4, gc = ((g & 1) << 1) | (g >> 1);
    f32x4 ga[2], cb[2];
#pragma unroll
    for (int nf = 0; nf < 2; ++nf) { ga[nf] = *(const LAS f32x4*)(lds + 98304 + (nh * 8 + nf * 4 + g) * 16); cb[nf] = *(const LAS f32x4*)(lds + 98304 + 256 + (nh * 8 + nf * 4 + g) * 16); }
#pragma unroll
    for (int m = 0; m < 2; ++m) {
        const int rl = tb * 32 + m * 16 + (lane & 15);
        unsigned u[2][2], y[2][2]; float v[8];
#pragma unroll
        for (int nf = 0; nf < 2; ++nf) {
            const u32x2 xv = *(const LAS u32x2*)(lds + 147456 + rl * 128 + (((nh * 4 + nf * 2 + (g >> 1)) ^ (rl & 7)) << 4) + 8 * (g & 1));
            u[nf][0] = cvtpk(bflo(xv[0]) + ga[nf][0] * acc[m][nf][0], bfhi(xv[0]) + ga[nf][1] * acc[m][nf][1]);
            u[nf][1] = cvtpk(bflo(xv[1]) + ga[nf][2] * acc[m][nf][2], bfhi(xv[1]) + ga[nf][3] * acc[m][nf][3]);
            v[4 * nf] = bflo(u[nf][0]); v[4 * nf + 1] = bfhi(u[nf][0]); v[4 * nf + 2] = bflo(u[nf][1]); v[4 * nf + 3] = bfhi(u[nf][1]);
            y[nf][0] = cvtpk(v[4 * nf] * cb[nf][0], v[4 * nf + 1] * cb[nf][1]); y[nf][1] = cvtpk(v[4 * nf + 2] * cb[nf][2], v[4 * nf + 3] * cb[nf][3]);
        }
        st16(ws + WS_Y + ((size_t)(rb * 8 + tb * 2 + m) * 128 + ct * 8 + nh * 4) * 256, (unsigned)(gc * 256 + (lane & 15) * 16), swap16(y[0][0], y[0][1], y[1][0], y[1][1]));
        st16(ws + WS_X1 + ((size_t)(rb * 8 + tb * 2 + m) * 128 + ct * 8 + nh * 4) * 256, (unsigned)(gc * 256 + (lane & 15) * 16), swap16(u[0][0], u[0][1], u[1][0], u[1][1]));
        const float mean = rows_sum(((v[0] + v[1]) + (v[2] + v[3])) + ((v[4] + v[5]) + (v[6] + v[7]))) * (1.f / 32.f);
        float q = 0.f;
#pragma unroll
        for (int j = 0; j < 8; ++j) q += (v[j] - mean) * (v[j] - mean);
        q = rows_sum(q);
        if (g == 0) *(LAS f32x2*)(lds + 99328 + (rl * 2 + nh) * 8) = (f32x2){mean, q};
    }
    BAR_L();
    if (wid < 2) {
        const int rl = wid * 64 + lane;
        const f32x4 s = *(const LAS f32x4*)(lds + 99328 + rl * 16);
        const float dm = s[0] - s[2];
        st8(ws + WS_ST2 + (size_t)(ct * 2048 + rb * 128) * 8, (unsigned)rl * 8u, ((f32x2){0.5f * (s[0] + s[2]), s[1] + s[3] + 16.f * dm * dm}));
    }
    WAITV_BAR(0);
    STAMP(2); STAMP_FLUSH(3);
}

constexpr int GSL = 30720, G_ROW = 5 * GSL + 1024, G_COL = G_ROW + 2048;
__device__ __forceinline__ void tile_glu(int bid, int& rp, int& ct) { const int xcd = bid & 7, idx = bid >> 3; rp = (xcd & 1) * 4 + (idx & 3); ct = (xcd >> 1) * 8 + (idx >> 2); }
#define GLU_W(t, so) do { const char* w_ = Wb + (size_t)(t) * 14336; const unsigned d2_ = pw < 3 ? dW + (so) + 2048 : cx.lds0 + 5 * GSL; \
        dmac(w_, voff, dW + (so)); dmac(w_ + 1024, voff, dW + (so) + 1024); dmac(w_, voff2, d2_); dmac(w_ + 1024, voff2, pw < 3 ? d2_ + 1024 : d2_); } while (0)
__device__ __forceinline__ void glu_pre(const Ctx& cx) {
    const int pw = cx.w, lane = lane_id(); int rp, ct; tile_glu(cx.bid, rp, ct);
    const char* Wb = cx.ws + WS_WGU + (size_t)ct * 32 * 14336 + pw * 4096;
    const unsigned voff = lane * 16, dW = cx.lds0 + 16384 + pw * 4096;
#pragma unroll
    for (int t = 0; t < 4; ++t) {
        dmac(Wb + t * 14336, voff, dW + t * GSL); dmac(Wb + t * 14336 + 1024, voff, dW + t * GSL + 1024);
        if (pw < 3) { dmac(Wb + t * 14336 + 2048, voff, dW + t * GSL + 2048); dmac(Wb + t * 14336 + 3072, voff, dW + t * GSL + 3072); }
    }
}
template <class Next>
__device__ __forceinline__ void glu_produce(const Ctx& cx, Next next_pre) {
    char* ws = cx.ws; const int pw = cx.w, lane = lane_id(); int rp, ct; tile_glu(cx.bid, rp, ct);
    const char* Ab = ws + WS_Y + (size_t)(rp * 16 + 4 * pw) * 32768;
    const char* Wb = ws + WS_WGU + (size_t)ct * 32 * 14336 + pw * 4096;
    const unsigned voff = lane * 16, voff2 = pw < 3 ? lane * 16 + 2048 : 0u, dA = cx.lds0 + pw * 4096, dW = cx.lds0 + 16384 + pw * 4096;
#define GLU_A(t, so) do { const char* a_ = Ab + (size_t)(t) * 1024; dmac(a_, voff, dA + (so)); dmac(a_ + 32768, voff, dA + (so) + 1024); dmac(a_ + 65536, voff, dA + (so) + 2048); dmac(a_ + 98304, voff, dA + (so) + 3072); } while (0)
    STAMP_DECL; STAMP(0);
    await_input(ws, pw, C_Y + rp, 32, lane);
    STAMP(1);
    GLU_A(0, 0); GLU_A(1, GSL); GLU_A(2, 2 * GSL); GLU_A(3, 3 * GSL);
    STAMP(2);
    WAITV_BAR(12);
    STAMP(3);
    WAITV_BAR(8); GLU_W(4, 4 * GSL); GLU_A(4, 4 * GSL);
    WAITV_BAR(12); GLU_W(5, 0); GLU_A(5, 0);
    int sf = GSL;
#define GLU_NEXT(t, VM) do { WAITV_BAR(VM); GLU_W((t) + 4, sf); GLU_A((t) + 4, sf); sf = sf == 4 * GSL ? 0 : sf + GSL; } while (0)
#pragma unroll 1
    for (int t = 2; t < 16; ++t) GLU_NEXT(t, 16);
    {
        f32x2 sp[16]; f32x4 cp[8];
#pragma unroll
        for (int i = 0; i < 16; ++i) LD8C(sp[i], ws + WS_ST2 + (size_t)(rp * 256 + pw * 64) * 8, (unsigned)(lane * 8 + i * 16384));
        int i4 = pw * 64 + lane; i4 = i4 < 112 ? i4 : 0;
        const unsigned vo = (unsigned)((i4 / 28) * F + ct * 112 + (i4 % 28) * 4) * 4u;
#pragma unroll
        for (int i = 0; i < 8; ++i) LD16C(cp[i], ws + WS_UT, vo + i * (4 * F * 4));
        GLU_NEXT(16, 40); GLU_NEXT(17, 40); GLU_NEXT(18, 40); GLU_NEXT(19, 16);
        const f32x4 c = sum8(cp);
        if (pw * 64 + lane < 112) *(LAS f32x4*)(cx.lds + G_COL + (pw * 64 + lane) * 16) = c;
        float mean = 0.f, q = 0.f;
#pragma unroll
        for (int i = 0; i < 16; ++i) { PIN(sp[i]); mean += sp[i][0]; q += sp[i][1]; }
        mean *= 1.f / 16.f;
        float dq = 0.f;
#pragma unroll
        for (int i = 0; i < 16; ++i) dq += (sp[i][0] - mean) * (sp[i][0] - mean);
        const float rstd = 1.f / sqrtf((q + 64.f * dq) * (1.f / 1024.f) + 1e-5f);
        *(LAS f32x2*)(cx.lds + G_ROW + (pw * 64 + lane) * 8) = (f32x2){rstd, -mean * rstd};
    }
#pragma unroll 1
    for (int t = 20; t < 28; ++t) GLU_NEXT(t, 16);
    WAITV_BAR(16); WAITV_BAR(8); WAITVL_BAR(0);
#undef GLU_NEXT
#undef GLU_A
    BARRIER();
    next_pre();
    BARRIER();
    if (pw == 0) signal(ws, C_HH + rp * 2 + (ct >> 4), lane);
    PSTAMP_FLUSH(13);
}
#undef GLU_W
__device__ __forceinline__ void glu_consume(const Ctx& cx) {
    char* ws = cx.ws; const lds_ptr lds = cx.lds; const int wm = cx.w & 3, wn = cx.w >> 2;
    STAMP_DECL; STAMP(0);
    f32x4 acc[4][7];
    {
        const int lane = lane_id();
        const lds_ptr pa = lds + wm * 4096 + lane * 16, pw = lds + 16384 + wn * 7168 + lane * 16;
        bf16x8 af[4], wf[4];
#pragma unroll
        for (int n = 0; n < 7; ++n)
#pragma unroll
            for (int m = 0; m < 4; ++m) acc[m][n] = ZERO4;
        INPUT_BARRIER();
        BARRIER();
        STAMP(1);
#pragma unroll
        for (int m = 0; m < 4; ++m) { af[m] = ldsr128(pa + m * 1024); wf[m] = ldsr128(pw + m * 1024); }
        int sc = 0, sn = GSL;
#define GW(U, n) wf[(7 * (U) + (n)) & 3]
#define GLU_STEP(U, DO_NEXT) do { \
            if (DO_NEXT) BARRIER(); SBAR(); \
            const lds_ptr pwc = pw + sc, pwn = pw + sn, pan = pa + sn; \
            _Pragma("unroll") for (int m = 0; m < 4; ++m) { MFMA16_ACC(acc[m][0], GW(U, 0), af[m]); MFMA16_ACC(acc[m][1], GW(U, 1), af[m]); } \
            SBAR(); GW(U, 4) = ldsr128(pwc + 4096); GW(U, 5) = ldsr128(pwc + 5120); SBAR(); \
            _Pragma("unroll") for (int m = 0; m < 4; ++m) MFMA16_ACC(acc[m][2], GW(U, 2), af[m]); \
            SBAR(); GW(U, 6) = ldsr128(pwc + 6144); SBAR(); \
            _Pragma("unroll") for (int m = 0; m < 4; ++m) MFMA16_ACC(acc[m][3], GW(U, 3), af[m]); \
            SBAR(); if (DO_NEXT) GW(U, 7) = ldsr128(pwn); SBAR(); \
            _Pragma("unroll") for (int m = 0; m < 4; ++m) MFMA16_ACC(acc[m][4], GW(U, 4), af[m]); \
            SBAR(); if (DO_NEXT) GW(U, 8) = ldsr128(pwn + 1024); SBAR(); \
            _Pragma("unroll") for (int m = 0; m < 4; ++m) { MFMA16_ACC(acc[m][5], GW(U, 5), af[m]); MFMA16_ACC(acc[m][6], GW(U, 6), af[m]); \
                SBAR(); if (DO_NEXT) af[m] = ldsr128(pan + m * 1024); SBAR(); } \
            if (DO_NEXT) { GW(U, 9) = ldsr128(pwn + 2048); GW(U, 10) = ldsr128(pwn + 3072); } SBAR(); \
            sc = sn; sn = sn == 4 * GSL ? 0 : sn + GSL; } while (0)
#pragma unroll 1
        for (int t = 0; t < 28; t += 4) { GLU_STEP(0, true); GLU_STEP(1, true); GLU_STEP(2, true); GLU_STEP(3, true); }
        GLU_STEP(0, true); GLU_STEP(1, true); GLU_STEP(2, true); GLU_STEP(3, false);
#undef GLU_STEP
#undef GW
    }
    asm volatile("s_nop 7\n\ts_nop 7" : "+v"(acc[0][6]), "+v"(acc[1][6]), "+v"(acc[2][6]), "+v"(acc[3][6]), "+v"(acc[0][5]), "+v"(acc[1][5]), "+v"(acc[2][5]), "+v"(acc[3][5]) :: "memory");
    SBAR();
    STAMP(2);
    const int lane = lane_id(), g = lane >> 4, gc = ((g & 1) << 1) | (g >> 1);
    int rp, ct; tile_glu(cx.bid, rp, ct);
    const char* hb = ws + WS_HH + ((size_t)(rp * 16 + wm * 4) * (F / 8) + ct * 14 + wn * 7) * 256;
    const unsigned vo = (lane & 15) * 16;
    constexpr unsigned RB = (F / 8) * 256;
    f32x2 rw[4];
#pragma unroll
    for (int m = 0; m < 4; ++m) rw[m] = *(const LAS f32x2*)(lds + G_ROW + (wm * 64 + m * 16 + (lane & 15)) * 8);
    unsigned u[4][3][2];
#pragma unroll
    for (int f = 0; f < 3; ++f) {
        const lds_ptr pc = lds + G_COL + (wn * 14 + f * 4 + g) * 16;
        const f32x4 ug = *(const LAS f32x4*)pc, tg = *(const LAS f32x4*)(pc + 448), uu = *(const LAS f32x4*)(pc + 896), tu = *(const LAS f32x4*)(pc + 1344);
#pragma unroll
        for (int m = 0; m < 4; ++m) {
            const f32x4 gt = acc[m][f] * rw[m][0] + (ug * rw[m][1] + tg), up = acc[m][3 + f] * rw[m][0] + (uu * rw[m][1] + tu);
            u[m][f][0] = cvtpk(fmaxf(gt[0], 0.f) * up[0], fmaxf(gt[1], 0.f) * up[1]); u[m][f][1] = cvtpk(fmaxf(gt[2], 0.f) * up[2], fmaxf(gt[3], 0.f) * up[3]);
        }
        if (f == 1) {
#pragma unroll
            for (int m = 0; m < 4; ++m) st16(hb, vo + m * RB + gc * 256, swap16(u[m][0][0], u[m][0][1], u[m][1][0], u[m][1][1]));
        }
    }
#pragma unroll
    for (int mp = 0; mp < 2; ++mp) st16(hb, vo + (2 * mp + (g & 1)) * RB + (4 + (g >> 1)) * 256, swap16(u[2 * mp][2][0], u[2 * mp][2][1], u[2 * mp + 1][2][0], u[2 * mp + 1][2][1]));
    {
        const lds_ptr pc = lds + G_COL + (g >> 1) * 896 + (wn * 14 + 12 + (g & 1)) * 16;
        const f32x4 uc = *(const LAS f32x4*)pc, tc = *(const LAS f32x4*)(pc + 448);
        unsigned ux[2][2];
#pragma unroll
        for (int mp = 0; mp < 2; ++mp) {
            const f32x4 a = acc[2 * mp][6] * rw[2 * mp][0] + (uc * rw[2 * mp][1] + tc), b = acc[2 * mp + 1][6] * rw[2 * mp + 1][0] + (uc * rw[2 * mp + 1][1] + tc);
            float hv[4];
#pragma unroll
            for (int r = 0; r < 4; ++r) {
                const auto x = __builtin_amdgcn_permlane32_swap(__float_as_uint(a[r]), __float_as_uint(b[r]), false, false);
                hv[r] = fmaxf(__uint_as_float(x[0]), 0.f) * __uint_as_float(x[1]);
            }
            ux[mp][0] = cvtpk(hv[0], hv[1]); ux[mp][1] = cvtpk(hv[2], hv[3]);
        }
        st16(hb, vo + gc * RB + 6 * 256, swap16(ux[0][0], ux[0][1], ux[1][0], ux[1][1]));
    }
    STAMP(3);
    BARRIER();
    WAITV_BAR(0);
    STAMP(4); STAMP_FLUSH(5);
}

__device__ __forceinline__ void tile_down(int bid, int& rb, int& ct, int& kh) { const int xcd = bid & 7, idx = bid >> 3; kh = xcd & 1; rb = (xcd >> 1) * 4 + (idx & 3); ct = idx >> 2; }
#define DN_W(t, so) do { const char* w_ = Wb + (size_t)(t) * 16384; dmac(w_, voff, dW + (so)); dmac(w_ + 1024, voff, dW + (so) + 1024); dmac(w_ + 2048, voff, dW + (so) + 2048); dmac(w_ + 3072, voff, dW + (so) + 3072); } while (0)
__device__ __forceinline__ void down_pre(const Ctx& cx, OPre& o) {
    const int pw = cx.w, lane = lane_id(); int rb, ct, kh; tile_down(cx.bid, rb, ct, kh);
    const unsigned vo = (unsigned)(3072 + ct * 128 + (lane & 31) * 4) * 4u;
#pragma unroll
    for (int i = 0; i < 8; ++i) LD16C(o.p[i], cx.ws + WS_MP2, vo + i * 16384);
    const char* Wb = cx.ws + WS_WD + (size_t)(ct * 112 + kh * 56) * 8192 + pw * 4096;
    const unsigned voff = lane * 16, dW = cx.lds0 + 16384 + pw * 4096;
#pragma unroll
    for (int t = 0; t < 4; ++t) DN_W(t, t * 32768);
}
__device__ __forceinline__ void down_produce(const Ctx& cx, OPre& o) {
    char* ws = cx.ws; const int pw = cx.w, lane = lane_id(); int rb, ct, kh; tile_down(cx.bid, rb, ct, kh);
    const char* Ab = ws + WS_HH + ((size_t)(rb * 8 + 2 * pw) * (F / 8) + kh * 224) * 256;
    const char* Wb = ws + WS_WD + (size_t)(ct * 112 + kh * 56) * 8192 + pw * 4096;
    const unsigned voff = lane * 16, dA = cx.lds0 + pw * 4096, dW = cx.lds0 + 16384 + pw * 4096;
#define DN_A(t, so) do { const char* a_ = Ab + (size_t)(t) * 2048; dmac(a_, voff, dA + (so)); dmac(a_ + 1024, voff, dA + (so) + 1024); \
        dmac(a_ + (F / 8) * 256, voff, dA + (so) + 2048); dmac(a_ + (F / 8) * 256 + 1024, voff, dA + (so) + 3072); } while (0)
    await_input(ws, pw, C_HH + (rb >> 1) * 2 + kh, 16, lane);
    DN_A(0, 0); DN_A(1, 32768); DN_A(2, 65536); DN_A(3, 98304);
    WAITV_BAR(12);
    WAITV_BAR(8); DN_W(4, 131072); DN_A(4, 131072);
    f32x4 tv = rbf4(sum8(o.p)); PIN(tv);
    WAITV_BAR(12); DN_W(5, 0); DN_A(5, 0);
    int sf = 32768;
#pragma unroll 1
    for (int t = 2; t < 24; ++t) {
        WAITV_BAR(16);
        DN_W(t + 4, sf); DN_A(t + 4, sf);
        sf = sf == 131072 ? 0 : sf + 32768;
    }
    WAITV_BAR(16);
    {
        const char* xb = ws + WS_X1 + ((size_t)(rb * 8 + (pw >> 1) * 4 + kh * 2 + (pw & 1)) * 128 + ct * 16) * 256;
#pragma unroll
        for (int i = 0; i < 4; ++i) dmac(xb + i * 1024, voff, cx.lds0 + 98304 + pw * 4096 + i * 1024);
        if (pw == 0 && lane < 32) *(LAS f32x4*)(cx.lds + 114688 + lane * 16) = tv;
    }
    WAITV_BAR(12); WAITV_BAR(4); WAITVL_BAR(0);
#undef DN_A
}
#undef DN_W
__device__ __forceinline__ void down_consume(const Ctx& cx, u16* __restrict__ out) {
    char* ws = cx.ws; const lds_ptr lds = cx.lds; const int wid = cx.w, lane = lane_id(), wm = wid >> 2, wn = wid & 3, g = lane >> 4;
    STAMP_DECL; STAMP(0);
    const lds_ptr pa = lds + wm * 8192 + lane * 16, pw = lds + 16384 + wn * 2048 + lane * 16;
    f32x4 acc[4][2];
#pragma unroll
    for (int m = 0; m < 4; ++m) { acc[m][0] = ZERO4; acc[m][1] = ZERO4; }
    bf16x8 aA[8], wA[4], aB[8], wB[4];
    INPUT_BARRIER();
    BARRIER(); SBAR();
    STAMP(1);
#pragma unroll
    for (int ks = 0; ks < 2; ++ks) {
        wA[2 * ks] = ldsr128(pw + ks * 8192); wA[2 * ks + 1] = ldsr128(pw + ks * 8192 + 1024);
#pragma unroll
        for (int m = 0; m < 4; ++m) aA[4 * ks + m] = ldsr128(pa + m * 2048 + ks * 1024);
    }
    SBAR();
    int sn = 32768;
#define DN_STEP(AC, WC, AN, WN, DO_NEXT) do { \
        if (DO_NEXT) BARRIER(); SBAR(); \
        const lds_ptr pan = pa + sn, pwn = pw + sn; \
        _Pragma("unroll") for (int i = 0; i < 8; ++i) { const int ks = i >> 2, m = i & 3; \
            MFMA16_ACC(acc[m][0], WC[2 * ks], AC[i]); MFMA16_ACC(acc[m][1], WC[2 * ks + 1], AC[i]); SBAR(); \
            if (DO_NEXT) { if (m < 2) WN[2 * ks + m] = ldsr128(pwn + ks * 8192 + m * 1024); AN[i] = ldsr128(pan + m * 2048 + ks * 1024); } \
            SBAR(); } \
        sn = sn == 131072 ? 0 : sn + 32768; } while (0)
#pragma unroll 1
    for (int t = 0; t < 26; t += 2) { DN_STEP(aA, wA, aB, wB, true); DN_STEP(aB, wB, aA, wA, true); }
    DN_STEP(aA, wA, aB, wB, true); DN_STEP(aB, wB, aA, wA, false);
#undef DN_STEP
    asm volatile("s_nop 7\n\ts_nop 7" : "+v"(acc[0][0]), "+v"(acc[0][1]), "+v"(acc[1][0]), "+v"(acc[1][1]), "+v"(acc[2][0]), "+v"(acc[2][1]), "+v"(acc[3][0]), "+v"(acc[3][1]));
    SBAR();
    STAMP(2);
    int rb, ct, kh; tile_down(cx.bid, rb, ct, kh);
    f32x4 keep[2][2];
#pragma unroll
    for (int mm = 0; mm < 2; ++mm) {
        const f32x4 g0 = kh ? acc[mm][0] : acc[2 + mm][0], g1 = kh ? acc[mm][1] : acc[2 + mm][1];
        st16(ws + WS_XCH + ((size_t)cx.bid * 8 + wid) * 2048, lane * 16 + mm * 1024, ((u32x4){cvtpk(g0[0], g0[1]), cvtpk(g0[2], g0[3]), cvtpk(g1[0], g1[1]), cvtpk(g1[2], g1[3])}));
        keep[mm][0] = kh ? acc[2 + mm][0] : acc[mm][0]; keep[mm][1] = kh ? acc[2 + mm][1] : acc[mm][1];
    }
    BARRIER();
    u32x2 xr[2][2]; f32x4 gb[2];
#pragma unroll
    for (int nf = 0; nf < 2; ++nf) {
        gb[nf] = *(const LAS f32x4*)(lds + 114688 + (wn * 8 + nf * 4 + g) * 16);
#pragma unroll
        for (int mm = 0; mm < 2; ++mm) xr[mm][nf] = *(const LAS u32x2*)(lds + 98304 + ((wm * 2 + mm) * 16 + wn * 4 + nf * 2 + (g >> 1)) * 256 + (lane & 15) * 16 + 8 * (g & 1));
    }
    STAMP(5);
    u32x4 got[2];
    {
        const char* xi = ws + WS_XCH + ((size_t)(cx.bid ^ 1) * 8 + wid) * 2048;
        for (unsigned spins = 0; spins < (1u << 15); ++spins) {
            asm volatile("s_nop 4\n\tglobal_load_dwordx4 %0, %2, %3 sc1\n\tglobal_load_dwordx4 %1, %2, %3 offset:1024 sc1\n\ts_waitcnt vmcnt(0)" : "=&v"(got[0]), "=&v"(got[1]) : "v"(lane * 16), "s"(xi) : "memory");
            const bool ok = got[0][0] != ~0u && got[0][1] != ~0u && got[0][2] != ~0u && got[0][3] != ~0u && got[1][0] != ~0u && got[1][1] != ~0u && got[1][2] != ~0u && got[1][3] != ~0u;
            if (__all(ok)) break;
            __builtin_amdgcn_s_sleep(2);
        }
    }
    STAMP(3);
#pragma unroll
    for (int mm = 0; mm < 2; ++mm) {
        unsigned u[2][2];
#pragma unroll
        for (int nf = 0; nf < 2; ++nf) {
            const f32x4 gg = gb[nf], k = keep[mm][nf];
            u[nf][0] = cvtpk(bflo(xr[mm][nf][0]) + gg[0] * (k[0] + bflo(got[mm][2 * nf])), bfhi(xr[mm][nf][0]) + gg[1] * (k[1] + bfhi(got[mm][2 * nf])));
            u[nf][1] = cvtpk(bflo(xr[mm][nf][1]) + gg[2] * (k[2] + bflo(got[mm][2 * nf + 1])), bfhi(xr[mm][nf][1]) + gg[3] * (k[3] + bfhi(got[mm][2 * nf + 1])));
        }
        st16(out + (size_t)(rb * 128 + wm * 64 + (2 * kh + mm) * 16) * D + ct * 128 + wn * 32, (unsigned)((lane & 15) * 2048 + (((g & 1) << 1) | (g >> 1)) * 16), swap16(u[0][0], u[0][1], u[1][0], u[1][1]));
    }
    STAMP(4); STAMP_FLUSH(6);
}

#define CTX_INIT extern __shared__ __attribute__((aligned(16))) char smem[]; \
    const int wid_ = uni(threadIdx.x >> 6); const bool prod = wid_ >= 8; \
    const Ctx cx = {ws, (lds_ptr)smem, uni((unsigned)(uintptr_t)(lds_ptr)smem), (int)blockIdx.x, prod ? wid_ - 8 : wid_}; \
    if (prod) __builtin_amdgcn_s_setprio(3)
__global__ __launch_bounds__(768, 3) void k_main(const u16* __restrict__ x, const u16* __restrict__ wk, const u16* __restrict__ wv, const u16* __restrict__ wo, const u16* __restrict__ wg,
                                                 const u16* __restrict__ wu, const u16* __restrict__ wd, const u16* __restrict__ wmod, char* __restrict__ ws, u16* __restrict__ out) {
    CTX_INIT;
    if (prod) {
        OPre o;
        ln1kv_produce(cx, wk, wv, wmod, [&]() { attn_pre(cx); });
        attn_produce(cx, wo, wg, wu, wd, wmod, [&]() { oproj_pre(cx, o); });
        oproj_produce(cx, o, x, [&]() { glu_pre(cx); });
        glu_produce(cx, [&]() { down_pre(cx, o); });
        down_produce(cx, o);
    } else {
        ln1kv_consume(cx, x);
        attn_consume(cx);
        oproj_consume(cx);
        glu_consume(cx);
        down_consume(cx, out);
    }
}

extern "C" void kernel_launch(void* const* d_in, const int* in_sizes, int n_in, void* d_out, int out_size, void* d_ws, size_t ws_size, hipStream_t stream) {
    const u16* x = (const u16*)d_in[0];
    const u16* wq = (const u16*)d_in[1];
    const u16* wk = (const u16*)d_in[2];
    const u16* wv = (const u16*)d_in[3];
    const u16* wo = (const u16*)d_in[4];
    const u16* wg = (const u16*)d_in[6];
    const u16* wu = (const u16*)d_in[7];
    const u16* wd = (const u16*)d_in[8];
    const u16* c = (const u16*)d_in[9];
    const u16* wmod = (const u16*)d_in[10];
    char* ws = (char*)d_ws;
    constexpr int LDS = 163840;
    k_pre<<<640, 256, 0, stream>>>(c, wmod, x, wq, ws);
    (void)hipFuncSetAttribute((const void*)k_main, hipFuncAttributeMaxDynamicSharedMemorySize, LDS);
    k_main<<<256, 768, LDS, stream>>>(x, wk, wv, wo, wg, wu, wd, wmod, ws, (u16*)d_out);
}
```

```cpp
#include <hip/hip_runtime.h>
#include <hip/hip_bf16.h>

#define X_SLEEP 2
#define X_GDROP 1
#define X_HDROP 5
#define X_S1 18
#define X_S2 8
#define MEGA 1
#define LAS __attribute__((address_space(3)))
#define GAS __attribute__((address_space(1)))
typedef unsigned short u16;
typedef short bf16x8 __attribute__((ext_vector_type(8)));
typedef short s16x4 __attribute__((ext_vector_type(4)));
typedef float f32x2 __attribute__((ext_vector_type(2)));
typedef float f32x4 __attribute__((ext_vector_type(4)));
typedef float f32x16 __attribute__((ext_vector_type(16)));
typedef unsigned u32x4 __attribute__((ext_vector_type(4)));
typedef unsigned u32x2 __attribute__((ext_vector_type(2)));
typedef __bf16 bf16x2v __attribute__((ext_vector_type(2)));
typedef LAS char* lds_ptr;

constexpr int D = 1024, F = 3584;
constexpr size_t WS_CNT = 0;
constexpr int C_H1 = 0, C_SIDE = 16, C_O = 32, C_Y = 48, C_HH = 56, C_WQ = 72, C_X1 = 88, C_NUM = 104;
constexpr size_t WS_EPOCH = 27648;
constexpr size_t WS_MP1 = 32768;
constexpr size_t WS_MP2 = 786432;
constexpr size_t WS_KN = 28672;
constexpr size_t WS_KV = 262144;
constexpr size_t WS_ST2 = 524288;
constexpr size_t WS_UT = 1u << 20;
constexpr size_t WS_H1 = 2u << 20;
constexpr size_t WS_O = WS_H1 + (4u << 20);
constexpr size_t WS_X1 = WS_O + (4u << 20);
constexpr size_t WS_Y = WS_X1 + (4u << 20);
constexpr size_t WS_HH = WS_Y + (4u << 20);
constexpr size_t WS_XCH = WS_HH + (16u << 20);
constexpr size_t WS_WQ = WS_XCH + (4u << 20);
constexpr size_t WS_WO = WS_WQ + (2u << 20);
constexpr size_t WS_WGU = WS_WO + (2u << 20);
constexpr size_t WS_WD = WS_WGU + (16u << 20);

__device__ __forceinline__ float bf2f(u16 b) { return __uint_as_float(((unsigned)b) << 16); }
__device__ __forceinline__ float bflo(unsigned w) { return __uint_as_float(w << 16); }
__device__ __forceinline__ float bfhi(unsigned w) { return __uint_as_float(w & 0xffff0000u); }
__device__ __forceinline__ unsigned cvtpk(float lo, float hi) { return __builtin_bit_cast(unsigned, __builtin_convertvector((f32x2){lo, hi}, bf16x2v)); }
__device__ __forceinline__ float rbf(float x) { return bflo(cvtpk(x, x)); }
__device__ __forceinline__ f32x4 rbf4(f32x4 s) { return (f32x4){rbf(s[0]), rbf(s[1]), rbf(s[2]), rbf(s[3])}; }

__device__ __forceinline__ void dma16(const void* gbase, unsigned voff, unsigned ldsaddr) {
    asm volatile("s_mov_b32 m0, %0\n\ts_nop 3\n\tglobal_load_lds_dwordx4 %1, %2" ::"s"(ldsaddr), "v"(voff), "s"(gbase) : "memory");
}
__device__ __forceinline__ void dmac(const void* gbase, unsigned voff, unsigned ldsaddr) {
    asm volatile("s_mov_b32 m0, %0\n\ts_nop 3\n\tglobal_load_lds_dwordx4 %1, %2 sc1" ::"s"(ldsaddr), "v"(voff), "s"(gbase) : "memory");
}
#define LD16(dst, base, voff) asm volatile("s_nop 4\n\tglobal_load_dwordx4 %0, %1, %2" : "=v"(dst) : "v"(voff), "s"(base) : "memory")
#define LD16C(dst, base, voff) asm volatile("s_nop 4\n\tglobal_load_dwordx4 %0, %1, %2 sc1" : "=v"(dst) : "v"(voff), "s"(base) : "memory")
#define LD8C(dst, base, voff) asm volatile("s_nop 4\n\tglobal_load_dwordx2 %0, %1, %2 sc1" : "=v"(dst) : "v"(voff), "s"(base) : "memory")
#define TOUCH(reg, base, voff) asm volatile("s_nop 4\n\tglobal_load_dword %0, %1, %2 sc1" : "+v"(reg) : "v"(voff), "s"(base) : "memory")
template <class T> __device__ __forceinline__ void st16(const void* base, unsigned voff, T v) {
    asm volatile("s_nop 4\n\tglobal_store_dwordx4 %0, %1, %2 sc1\n\ts_nop 1" ::"v"(voff), "v"(v), "s"(base) : "memory");
}
template <class T> __device__ __forceinline__ void st8(const void* base, unsigned voff, T v) { asm volatile("s_nop 4\n\tglobal_store_dwordx2 %0, %1, %2 sc1" ::"v"(voff), "v"(v), "s"(base) : "memory"); }
__device__ __forceinline__ void st4(const void* base, unsigned voff, float v) { asm volatile("s_nop 4\n\tglobal_store_dword %0, %1, %2 sc1" ::"v"(voff), "v"(v), "s"(base) : "memory"); }
#define WAIT_V(n) asm volatile("s_waitcnt vmcnt(" #n ")" ::: "memory")
#define BARRIER() asm volatile("s_barrier" ::: "memory")
#define BAR_L() asm volatile("s_waitcnt lgkmcnt(0)\n\ts_barrier" ::: "memory")
#define WAITV_BAR(n) asm volatile("s_waitcnt vmcnt(" #n ")\n\ts_barrier" ::: "memory")
#define WAITVL_BAR(n) asm volatile("s_waitcnt vmcnt(" #n ") lgkmcnt(0)\n\ts_barrier" ::: "memory")
#define PIN(x) asm volatile("" : "+v"(x))
#define SBAR() __builtin_amdgcn_sched_barrier(0)
#define MFMA32(a, b, c) __builtin_amdgcn_mfma_f32_32x32x16_bf16(a, b, c, 0, 0, 0)
#define MFMA16(a, b, c) __builtin_amdgcn_mfma_f32_16x16x32_bf16(a, b, c, 0, 0, 0)
#define MFMA16_ACC(c, a, b) asm volatile("v_mfma_f32_16x16x32_bf16 %0, %1, %2, %0" : "+v"(c) : "v"(a), "v"(b))
#define ZERO4 ((f32x4){0.f, 0.f, 0.f, 0.f})
__device__ __forceinline__ bf16x8 ldsr128(lds_ptr p) { return *(const LAS bf16x8*)p; }
__device__ __forceinline__ s16x4 ldstr(lds_ptr p) { return __builtin_amdgcn_ds_read_tr16_b64_v4i16((LAS s16x4*)p); }
__device__ __forceinline__ bf16x8 ldstr2(lds_ptr p, int d) { s16x4 a = ldstr(p), b = ldstr(p + d); return (bf16x8){a[0], a[1], a[2], a[3], b[0], b[1], b[2], b[3]}; }
__device__ __forceinline__ unsigned uni(unsigned x) { return (unsigned)__builtin_amdgcn_readfirstlane((int)x); }
__device__ __forceinline__ int lane_id() { int l = __builtin_amdgcn_mbcnt_hi(~0u, __builtin_amdgcn_mbcnt_lo(~0u, 0u)); PIN(l); return l; }
__device__ __forceinline__ u32x4 swap16(unsigned a0, unsigned a1, unsigned b0, unsigned b1) {
    const auto r0 = __builtin_amdgcn_permlane16_swap(a0, b0, false, false), r1 = __builtin_amdgcn_permlane16_swap(a1, b1, false, false);
    return (u32x4){r0[0], r1[0], r0[1], r1[1]};
}

#define STAMP_DECL do {} while (0)
#define STAMP(i) do {} while (0)
#define STAMP_FLUSH(kid) do {} while (0)
#define PSTAMP_FLUSH(kid) do {} while (0)

template <int CTRL> __device__ __forceinline__ float dppf(float v) { return __builtin_bit_cast(float, __builtin_amdgcn_mov_dpp(__builtin_bit_cast(int, v), CTRL, 0xf, 0xf, true)); }
__device__ __forceinline__ float half_sum(float v) { const auto r = __builtin_amdgcn_permlane32_swap(__float_as_uint(v), __float_as_uint(v), false, false); return __uint_as_float(r[0]) + __uint_as_float(r[1]); }
__device__ __forceinline__ float rows_sum(float v) {
    const auto r = __builtin_amdgcn_permlane16_swap(__float_as_uint(v), __float_as_uint(v), false, false); return half_sum(__uint_as_float(r[0]) + __uint_as_float(r[1]));
}
__device__ __forceinline__ float rows_max(float v) {
    auto r = __builtin_amdgcn_permlane16_swap(__float_as_uint(v), __float_as_uint(v), false, false); v = fmaxf(__uint_as_float(r[0]), __uint_as_float(r[1]));
    r = __builtin_amdgcn_permlane32_swap(__float_as_uint(v), __float_as_uint(v), false, false); return fmaxf(__uint_as_float(r[0]), __uint_as_float(r[1]));
}
__device__ __forceinline__ float row16_sum(float v) { v += dppf<0xB1>(v); v += dppf<0x4E>(v); v += dppf<0x141>(v); v += dppf<0x140>(v); return v; }
__device__ __forceinline__ float row16_max(float v) { v = fmaxf(v, dppf<0xB1>(v)); v = fmaxf(v, dppf<0x4E>(v)); v = fmaxf(v, dppf<0x141>(v)); v = fmaxf(v, dppf<0x140>(v)); return v; }
__device__ __forceinline__ float wsum(float v) { return rows_sum(row16_sum(v)); }
__device__ __forceinline__ float wmax(float v) { return rows_max(row16_max(v)); }
__device__ __forceinline__ f32x4 sum8(f32x4 (&p)[8]) {
    asm volatile("" : "+v"(p[0]), "+v"(p[1]), "+v"(p[2]), "+v"(p[3]), "+v"(p[4]), "+v"(p[5]), "+v"(p[6]), "+v"(p[7]));
    return ((p[0] + p[1]) + (p[2] + p[3])) + ((p[4] + p[5]) + (p[6] + p[7]));
}

__device__ __forceinline__ void signal(char* ws, int idx, int lane) {
    if (MEGA) { if (lane == 0) __hip_atomic_fetch_add((GAS unsigned*)(ws + WS_CNT) + idx * 64, 1u, __ATOMIC_RELAXED, __HIP_MEMORY_SCOPE_AGENT); }
}
template <int SLEEP>
__device__ __forceinline__ void await(char* ws, int idx, int n, unsigned need, int lane) {
    if (MEGA) {
        GAS unsigned* p = (GAS unsigned*)(ws + WS_CNT) + (idx + (lane < n ? lane : 0)) * 64;
        for (unsigned spins = 0; spins < (1u << 15); ++spins) {
            if (__all(__hip_atomic_load(p, __ATOMIC_RELAXED, __HIP_MEMORY_SCOPE_AGENT) >= need)) break;
            __builtin_amdgcn_s_sleep(SLEEP);
        }
        asm volatile("s_waitcnt vmcnt(0)" ::: "memory");
    }
}
__device__ __forceinline__ void await_input(char* ws, int pw, int idx, unsigned need, int lane) { if (MEGA) { if (pw == 0) await<X_SLEEP>(ws, idx, 1, need, lane); BARRIER(); } }
#define INPUT_BARRIER() do { if (MEGA) BARRIER(); } while (0)

__device__ __forceinline__ void pack_load(const char* src  , unsigned pitch, unsigned ldsaddr, int lane) {
    const unsigned vo = (unsigned)(lane >> 3) * pitch, sw = (lane & 7) ^ (((lane >> 4) & 1) << 1);
#pragma unroll
    for (int i = 0; i < 4; ++i) dma16(src + (size_t)i * 8 * pitch, vo + ((sw ^ ((i & 1) << 2)) << 4), ldsaddr + i * 1024);
}
__device__ __forceinline__ bf16x8 pack_frag(lds_ptr area, int nf, int lane) {
    const int g = lane >> 4, q = (lane & 15) >> 2;
    return ldstr2(area + (8 * g + q) * 128 + ((nf ^ (((q >> 1) & 1) | ((g & 1) << 1))) << 5) + (lane & 3) * 8, 512);
}
__device__ __forceinline__ void mod_unit(lds_ptr area, bf16x8 sc, lds_ptr red, int lane) {
    f32x4 d[4];
#pragma unroll
    for (int nf = 0; nf < 4; ++nf) d[nf] = MFMA16(pack_frag(area, nf, lane), sc, ZERO4);
    if ((lane & 15) == 0) {
#pragma unroll
        for (int nf = 0; nf < 4; ++nf) *(LAS f32x4*)(red + nf * 64 + (lane >> 4) * 16) = d[nf];
    }
}

__device__ __forceinline__ float silu_bf(float cv) { return rbf(cv / (1.f + __expf(-cv))); }
__device__ __forceinline__ bf16x8 silu_frag(u32x4 cw) {
    PIN(cw);
    u32x4 r;
#pragma unroll
    for (int j = 0; j < 4; ++j) r[j] = cvtpk(silu_bf(bflo(cw[j])), silu_bf(bfhi(cw[j])));
    return __builtin_bit_cast(bf16x8, r);
}

__device__ __forceinline__ void tile128x64(int bid, int& rb, int& ct) { const int xcd = bid & 7, idx = bid >> 3; rb = (xcd >> 1) * 4 + (idx & 3); ct = (xcd & 1) * 8 + (idx >> 2); }
struct Ctx { char* ws; lds_ptr lds; unsigned lds0; int bid, w; };

__device__ __forceinline__ void ln1kv_consume(const Ctx& cx, const u16* __restrict__ x, const u16* __restrict__ c, const u16* __restrict__ wmod) {
    char* ws = cx.ws; const lds_ptr lds = cx.lds; const int wid = cx.w, lane = lane_id(), tid = wid * 64 + lane;
    STAMP_DECL; STAMP(0);
    const int rb = (cx.bid >> 4) * 8 + (cx.bid & 7), isv = (cx.bid >> 3) & 1;
    const int idx = cx.bid >> 3, cs = ((((idx & 3) << 3) | (cx.bid & 7)) << 1) | ((idx >> 2) & 1), kq = idx >> 3, kr = kq * 256 + (tid >> 2);
    const u32x4 w0 = *(const u32x4*)(wmod + (size_t)kr * 6144 + cs * 32 + (tid & 3) * 8), w1 = *(const u32x4*)(wmod + (size_t)(kr + 128) * 6144 + cs * 32 + (tid & 3) * 8);
    const u16 c0 = c[kr], c1 = c[kr + 128];
    unsigned long long tag = (__hip_atomic_load((GAS unsigned long long*)(ws + WS_EPOCH), __ATOMIC_RELAXED, __HIP_MEMORY_SCOPE_AGENT) + 1ull) << 24;
    u32x4 xv[2][2];
#pragma unroll
    for (int rr = 0; rr < 2; ++rr)
#pragma unroll
        for (int hh = 0; hh < 2; ++hh) xv[rr][hh] = *(const u32x4*)(x + (size_t)(rb * 16 + 2 * wid + rr) * D + hh * 512 + lane * 8);
    {
        const float s0 = silu_bf(bf2f(c0)), s1 = silu_bf(bf2f(c1));
        float a[8];
#pragma unroll
        for (int j = 0; j < 4; ++j) { a[2 * j] = s0 * bflo(w0[j]) + s1 * bflo(w1[j]); a[2 * j + 1] = s0 * bfhi(w0[j]) + s1 * bfhi(w1[j]); }
#pragma unroll
        for (int j = 0; j < 8; ++j) { a[j] += dppf<0x124>(a[j]); a[j] += dppf<0x128>(a[j]); a[j] = rows_sum(a[j]); }
        PIN(tag);
        if (lane < 4) { *(LAS f32x4*)(lds + 114688 + wid * 128 + lane * 32) = (f32x4){a[0], a[1], a[2], a[3]}; *(LAS f32x4*)(lds + 114688 + wid * 128 + lane * 32 + 16) = (f32x4){a[4], a[5], a[6], a[7]}; }
    }
    BAR_L();
    STAMP(6);
    if (wid == 0 && lane < 32) {
        const LAS float* r = (const LAS float*)(lds + 114688) + lane;
        const unsigned pv = __float_as_uint(((r[0] + r[32]) + (r[64] + r[96])) + ((r[128] + r[160]) + (r[192] + r[224])));
        __hip_atomic_store((GAS unsigned long long*)(ws + WS_MP1) + (kq * 2048 + ((cs * 32 + 1024) & 2047) + lane), tag | ((pv + 0x80u) >> 8), __ATOMIC_RELAXED, __HIP_MEMORY_SCOPE_AGENT);
    }
    STAMP(5);
    float xn[2][16];
#pragma unroll
    for (int rr = 0; rr < 2; ++rr) {
        float* v = xn[rr];
#pragma unroll
        for (int hh = 0; hh < 2; ++hh)
#pragma unroll
            for (int j = 0; j < 4; ++j) { v[hh * 8 + 2 * j] = bflo(xv[rr][hh][j]); v[hh * 8 + 2 * j + 1] = bfhi(xv[rr][hh][j]); }
        float sm = 0.f;
#pragma unroll
        for (int j = 0; j < 16; ++j) sm += v[j];
        const float mean = wsum(sm) * (1.f / 1024.f);
        float q = 0.f;
#pragma unroll
        for (int j = 0; j < 16; ++j) { v[j] -= mean; q += v[j] * v[j]; }
        const float rstd = 1.f / sqrtf(wsum(q) * (1.f / 1024.f) + 1e-5f);
#pragma unroll
        for (int j = 0; j < 16; ++j) { v[j] *= rstd; PIN(v[j]); }
    }
    LAS float* tab = (LAS float*)(lds + 98304);
    {
        u32x4 g[4][2];
        const unsigned tl = (unsigned)tag, th = (unsigned)(tag >> 32);
        __builtin_amdgcn_s_sleep(X_S1);
        for (unsigned spins = 0; spins < (1u << 15); ++spins) {
            asm volatile("s_nop 4\n\tglobal_load_dwordx4 %0, %8, %12 sc1\n\tglobal_load_dwordx4 %1, %8, %13 sc1\n\tglobal_load_dwordx4 %2, %9, %12 sc1\n\tglobal_load_dwordx4 %3, %9, %13 sc1\n\t"
                         "global_load_dwordx4 %4, %10, %12 sc1\n\tglobal_load_dwordx4 %5, %10, %13 sc1\n\tglobal_load_dwordx4 %6, %11, %12 sc1\n\tglobal_load_dwordx4 %7, %11, %13 sc1\n\ts_waitcnt vmcnt(0)"
                         : "=&v"(g[0][0]), "=&v"(g[0][1]), "=&v"(g[1][0]), "=&v"(g[1][1]), "=&v"(g[2][0]), "=&v"(g[2][1]), "=&v"(g[3][0]), "=&v"(g[3][1])
                         : "v"(tid * 16), "v"(tid * 16 + 16384), "v"(tid * 16 + 32768), "v"(tid * 16 + 49152), "s"(ws + WS_MP1), "s"(ws + WS_MP1 + 8192) : "memory");
            unsigned bad = 0u;
#pragma unroll
            for (int k = 0; k < 4; ++k)
                bad |= ((g[k][0][0] ^ tl) | (g[k][0][2] ^ tl) | (g[k][1][0] ^ tl) | (g[k][1][2] ^ tl)) >> 24 | (g[k][0][1] ^ th) | (g[k][0][3] ^ th) | (g[k][1][1] ^ th) | (g[k][1][3] ^ th);
            if (__all(bad == 0u)) break;
            __builtin_amdgcn_s_sleep(X_S2);
        }
        f32x4 v;
#pragma unroll
        for (int j = 0; j < 4; ++j)
            v[j] = (__uint_as_float(g[0][j >> 1][(j & 1) * 2] << 8) + __uint_as_float(g[1][j >> 1][(j & 1) * 2] << 8)) + (__uint_as_float(g[2][j >> 1][(j & 1) * 2] << 8) + __uint_as_float(g[3][j >> 1][(j & 1) * 2] << 8));
        v = rbf4(v);
        *(LAS f32x2*)(tab + tid * 2) = (f32x2){rbf(1.f + v[0]), rbf(1.f + v[1])};
        *(LAS f32x2*)(tab + 1024 + tid * 2) = (f32x2){v[2], v[3]};
    }
    BAR_L();
    if (cx.bid == 0 && tid == 0) __hip_atomic_store((GAS unsigned long long*)(ws + WS_EPOCH), tag >> 24, __ATOMIC_RELAXED, __HIP_MEMORY_SCOPE_AGENT);
    STAMP(1);
    const lds_ptr img = lds + 65536;
#pragma unroll
    for (int hh = 0; hh < 2; ++hh) {
        const int c = hh * 64 + lane;
        const f32x4 g0 = *(const LAS f32x4*)(tab + c * 8), g1 = *(const LAS f32x4*)(tab + c * 8 + 4);
        const f32x4 b0 = *(const LAS f32x4*)(tab + 1024 + c * 8), b1 = *(const LAS f32x4*)(tab + 1024 + c * 8 + 4);
#pragma unroll
        for (int rr = 0; rr < 2; ++rr) {
            float h[8];
#pragma unroll
            for (int j = 0; j < 4; ++j) { h[j] = xn[rr][hh * 8 + j] * g0[j] + b0[j]; h[4 + j] = xn[rr][hh * 8 + 4 + j] * g1[j] + b1[j]; }
            *(LAS u32x4*)(img + c * 256 + (((2 * wid + rr) ^ (c & 15)) << 4)) = (u32x4){cvtpk(h[0], h[1]), cvtpk(h[2], h[3]), cvtpk(h[4], h[5]), cvtpk(h[6], h[7])};
        }
    }
    BAR_L();
    STAMP(2);
    f32x4 acc[2] = {ZERO4, ZERO4};
    const int g = lane >> 4, q = (lane & 15) >> 2, p = lane & 3;
    bf16x8 hf[4], wf[4][2];
#pragma unroll
    for (int i = 0; i < 4; ++i) {
        const int s = wid * 4 + i, cc = 4 * s + g;
        hf[i] = ldsr128(img + cc * 256 + (((lane & 15) ^ (cc & 15)) << 4));
#pragma unroll
        for (int nf = 0; nf < 2; ++nf) wf[i][nf] = ldstr2(lds + (32 * s + 8 * g + q) * 64 + ((nf ^ (g & 1)) << 5) + 8 * p, 256);
    }
    LAS float* red = (LAS float*)(lds + 147456);
    const char* hb = ws + WS_H1 + ((size_t)rb * 128 + 16 * wid) * 256;
    if (isv) {
#pragma unroll
        for (int i = 0; i < 4; ++i) { acc[0] = MFMA16(hf[i], wf[i][0], acc[0]); acc[1] = MFMA16(hf[i], wf[i][1], acc[1]); }
        st16(hb, lane * 16 + 1024, hf[1]); st16(hb, lane * 16 + 3072, hf[3]);
#pragma unroll
        for (int nf = 0; nf < 2; ++nf) *(LAS f32x4*)(red + wid * 512 + (nf * 16 + (lane & 15)) * 16 + 4 * g) = acc[nf];
    } else {
#pragma unroll
        for (int i = 0; i < 4; ++i) { acc[0] = MFMA16(wf[i][0], hf[i], acc[0]); acc[1] = MFMA16(wf[i][1], hf[i], acc[1]); }
        st16(hb, lane * 16, hf[0]); st16(hb, lane * 16 + 2048, hf[2]);
#pragma unroll
        for (int nf = 0; nf < 2; ++nf) *(LAS f32x4*)(red + wid * 512 + (lane & 15) * 32 + nf * 16 + 4 * g) = acc[nf];
    }
    BAR_L();
    STAMP(3);
    if (wid == 0) {
        const int o0 = isv ? (lane & 31) * 16 + 4 * (lane >> 5) : (lane & 15) * 32 + g * 8, o1 = o0 + (isv ? 8 : 4);
        f32x4 a = ZERO4, b = ZERO4;
#pragma unroll
        for (int w = 0; w < 8; ++w) { a += *(const LAS f32x4*)(red + w * 512 + o0); b += *(const LAS f32x4*)(red + w * 512 + o1); }
        const u32x4 v = {cvtpk(a[0], a[1]), cvtpk(a[2], a[3]), cvtpk(b[0], b[1]), cvtpk(b[2], b[3])};
        st16(ws + WS_KV + (size_t)(rb >> 1) * 4096, isv ? 2048 + (rb & 1) * 1024 + lane * 16 : (g >> 1) * 1024 + ((g & 1) * 32 + (rb & 1) * 16 + (lane & 15)) * 16, v);
        float n2 = 0.f, mx = 0.f;
#pragma unroll
        for (int j = 0; j < 4; ++j) { n2 += bflo(v[j]) * bflo(v[j]) + bfhi(v[j]) * bfhi(v[j]); mx = fmaxf(mx, fmaxf(fabsf(bflo(v[j])), fabsf(bfhi(v[j])))); }
        if (!isv) mx = rows_sum(n2);
        if (!(n2 - n2 == 0.f)) mx = INFINITY;
        mx = wmax(mx);
        if (lane == 0) st4(ws + WS_KN, (unsigned)(isv * 128 + rb) * 4u, mx);
    }
    WAITV_BAR(0);
    STAMP(4); STAMP_FLUSH(1);
}
template <class Next>
__device__ __forceinline__ void ln1kv_produce(const Ctx& cx, const u16* __restrict__ wq, const u16* __restrict__ wk, const u16* __restrict__ wv, const u16* __restrict__ c, const u16* __restrict__ wmod, Next next_pre) {
    char* ws = cx.ws; const int pw = cx.w, lane = lane_id();
    int rbq, hp; tile128x64(cx.bid, rbq, hp);
    if (cx.bid == 0 && pw >= 2) { if ((pw - 2) * 64 + lane < C_NUM) st4(ws + WS_CNT, (unsigned)((pw - 2) * 64 + lane) * 256u, 0.f); WAIT_V(0); }
    if (pw < 2) { __builtin_amdgcn_s_sleep(8); pack_load((const char*)wq + (size_t)(rbq * 2 + pw) * 65536 + hp * 128, 2048, cx.lds0 + 106496 + pw * 4096, lane); }
    BARRIER();
    {
        const char* w = (const char*)(((cx.bid >> 3) & 1) ? wv : wk) + pw * 16384;
        const unsigned voff = (lane >> 2) * 64 + (((lane & 3) ^ (((lane >> 5) & 1) << 1)) << 4);
#pragma unroll
        for (int i = 0; i < 16; ++i) dma16(w + i * 1024, voff, cx.lds0 + pw * 16384 + i * 1024);
    }
    const int k0 = (cx.bid >> 5) * 128 + pw * 32, col2 = 1024 + (cx.bid & 31) * 64;
    u32x4 cw;
    pack_load((const char*)wmod + (size_t)k0 * 12288 + (2048 + col2) * 2, 12288, cx.lds0 + 126976 + pw * 4096, lane);
    LD16(cw, c, (unsigned)(k0 + (lane >> 4) * 8) * 2u);
    if (pw < 2) {
        WAIT_V(21);
#pragma unroll
        for (int nf = 0; nf < 4; ++nf) st16(ws + WS_WQ + (size_t)(hp * 32 + rbq * 2 + pw) * 4096, lane * 16 + nf * 1024, pack_frag(cx.lds + 106496 + pw * 4096, nf, lane));
    }
    WAIT_V(0);
    mod_unit(cx.lds + 126976 + pw * 4096, silu_frag(cw), cx.lds + 143360 + pw * 256, lane);
    BAR_L();
    if (pw == 0) signal(ws, C_WQ + hp, lane);
    BARRIER();
    BARRIER();
    await<4>(ws, C_WQ + hp, 1, 16, lane);
    next_pre();
    BARRIER();
    if (pw == 0) {
        signal(ws, C_H1 + (cx.bid >> 4), lane);
        const LAS float* r = (const LAS float*)(cx.lds + 143360) + lane;
        st4(ws + WS_MP2, (unsigned)((cx.bid >> 5) * 4096 + col2 + lane) * 4u, (r[0] + r[64]) + (r[128] + r[192]));
    }
}

__device__ __forceinline__ void g64_pre(const Ctx& cx, const char* Wp, int lane) {
#pragma unroll
    for (int t = 0; t < 5; ++t) { dmac(Wp + cx.w * 2048 + t * 8192, lane * 16, cx.lds0 + 16384 + cx.w * 2048 + t * 24576); dmac(Wp + cx.w * 2048 + t * 8192 + 1024, lane * 16, cx.lds0 + 16384 + cx.w * 2048 + t * 24576 + 1024); }
}
template <int NTAIL, class First, class Tail>
__device__ __forceinline__ void g64_produce(const Ctx& cx, const char* Ap, const char* Wp, int lane, First first, Tail tail) {
    const char* Aw = Ap + (size_t)(2 * cx.w) * 32768; const char* Ww = Wp + cx.w * 2048;
    const unsigned voff = lane * 16, dA = cx.lds0 + cx.w * 4096, dW = cx.lds0 + 16384 + cx.w * 2048;
#define G64_A(t, so) do { const char* a_ = Aw + (size_t)(t) * 2048; dmac(a_, voff, dA + (so)); dmac(a_ + 32768, voff, dA + (so) + 2048); dmac(a_ + 1024, voff, dA + (so) + 1024); dmac(a_ + 33792, voff, dA + (so) + 3072); } while (0)
#define G64_STAGE(t, so) do { const char* w_ = Ww + (size_t)(t) * 8192; dmac(w_, voff, dW + (so)); dmac(w_ + 1024, voff, dW + (so) + 1024); G64_A(t, so); } while (0)
#pragma unroll
    for (int t = 0; t < 5; ++t) G64_A(t, t * 24576);
    WAITV_BAR(16);
    first();
    WAITV_BAR(12); G64_STAGE(5, 5 * 24576);
    WAITV_BAR(14); G64_STAGE(6, 0);
    WAITV_BAR(16); G64_STAGE(7, 24576);
    int sf = 2 * 24576;
#pragma unroll 1
    for (int t = 3; t < 11; ++t) {
        WAITV_BAR(18);
        G64_STAGE(t + 5, sf);
        sf = sf == 5 * 24576 ? 0 : sf + 24576;
    }
    WAITV_BAR(18);
    WAITV_BAR(12);
    tail();
    asm volatile("s_waitcnt vmcnt(%0)\n\ts_barrier" ::"i"(6 + NTAIL) : "memory");
    asm volatile("s_waitcnt vmcnt(%0) lgkmcnt(0)\n\ts_barrier" ::"i"(NTAIL) : "memory");
#undef G64_STAGE
#undef G64_A
}
template <class Mid, class Late, class St>
__device__ __forceinline__ void g64_consume(f32x4 (&acc)[2][2], const Ctx& cx, size_t woff, unsigned& tch, int lane, Mid mid, Late late, St stamp) {
    const lds_ptr pa = cx.lds + (cx.w & 3) * 4096 + lane * 16, pw = cx.lds + 16384 + (cx.w >> 2) * 2048 + lane * 16;
#pragma unroll
    for (int m = 0; m < 2; ++m) { acc[m][0] = ZERO4; acc[m][1] = ZERO4; }
    bf16x8 aA[4], wA[4], aB[4], wB[4];
    if (MEGA) TOUCH(tch, cx.ws, (unsigned)woff + ((cx.bid >> 3) & 3) * 32768u + ((cx.w & 3) * 64 + lane) * 128u);
    INPUT_BARRIER();
    BARRIER(); SBAR();
    stamp();
#pragma unroll
    for (int ks = 0; ks < 2; ++ks) { wA[2 * ks] = ldsr128(pw + ks * 4096); aA[2 * ks] = ldsr128(pa + ks * 1024); wA[2 * ks + 1] = ldsr128(pw + ks * 4096 + 1024); aA[2 * ks + 1] = ldsr128(pa + 2048 + ks * 1024); }
    SBAR();
    int sn = 24576;
#define G64_STEP(AC, WC, AN, WN, DO_NEXT, HOOK) do { \
        if (DO_NEXT) BARRIER(); SBAR(); HOOK; \
        _Pragma("unroll") for (int i = 0; i < 4; ++i) { const int ks = i >> 1, m = i & 1; \
            if (DO_NEXT) { WN[i] = ldsr128(pw + sn + ks * 4096 + m * 1024); AN[i] = ldsr128(pa + sn + m * 2048 + ks * 1024); } \
            acc[m][0] = MFMA16(WC[2 * ks], AC[i], acc[m][0]); acc[m][1] = MFMA16(WC[2 * ks + 1], AC[i], acc[m][1]); PIN(acc[m][0]); PIN(acc[m][1]); \
            SBAR(); } \
        sn = sn == 5 * 24576 ? 0 : sn + 24576; } while (0)
#define G64_PAIR() do { G64_STEP(aA, wA, aB, wB, true, (void)0); G64_STEP(aB, wB, aA, wA, true, (void)0); } while (0)
#pragma unroll 1
    for (int t = 0; t < 6; t += 2) G64_PAIR();
    mid(6);
#pragma unroll 1
    for (int t = 6; t < 10; t += 2) G64_PAIR();
    mid(10);
    G64_PAIR();
    mid(12);
    G64_PAIR();
    G64_STEP(aA, wA, aB, wB, true, late());
    G64_STEP(aB, wB, aA, wA, false, (void)0);
#undef G64_PAIR
#undef G64_STEP
}

__device__ __forceinline__ void attn_pre(const Ctx& cx) { int rb, hp; tile128x64(cx.bid, rb, hp); g64_pre(cx, cx.ws + WS_WQ + (size_t)hp * 131072, lane_id()); }
template <class Next>
__device__ __forceinline__ void attn_produce(const Ctx& cx, const u16* __restrict__ wo, const u16* __restrict__ wg, const u16* __restrict__ wu, const u16* __restrict__ wd, const u16* __restrict__ c, const u16* __restrict__ wmod, Next next_pre) {
    char* ws = cx.ws; const int pw = cx.w, lane = lane_id();
    int rb, hp; tile128x64(cx.bid, rb, hp);
    const char* kvg = ws + WS_KV + pw * 8192;
    const unsigned voff = lane * 16, kvd = cx.lds0 + pw * 8192;
#define KV_STAGE(st, so) do { _Pragma("unroll") for (int i_ = 0; i_ < 8; ++i_) dmac(kvg + (st) * 32768 + i_ * 1024, voff, kvd + (so) + i_ * 1024); } while (0)
    STAMP_DECL; STAMP(0);
    await_input(ws, pw, C_H1 + rb, 16, lane);
    STAMP(1);
    if (pw == 0) signal(ws, C_SIDE + (cx.bid & 15), lane);
    g64_produce<16>(cx, ws + WS_H1 + (size_t)rb * 8 * 32768, ws + WS_WQ + (size_t)hp * 131072, lane, [&]() { STAMP(2); },
                    [&]() { KV_STAGE(0, 98304); KV_STAGE(1, 131072); });
    WAITV_BAR(8);
    const lds_ptr area = cx.lds + pw * 16384; const unsigned area0 = cx.lds0 + pw * 16384;
    const int k0 = (cx.bid >> 5) * 128 + pw * 32, col2 = (cx.bid & 15) * 64 + ((cx.bid >> 4) & 1) * 3072;
    const int gw = cx.bid * 4 + pw, isgu = gw < 896, mat = gw >= 448, k8 = (gw - mat * 448) / 56, cg = gw - mat * 448 - k8 * 56;
    u32x4 cw; f32x4 vp[8];
    pack_load((const char*)wmod + (size_t)k0 * 12288 + (2048 + col2) * 2, 12288, area0, lane);
    LD16(cw, c, (unsigned)(k0 + (lane >> 4) * 8) * 2u);
    auto unit_load = [&](int j) {
        const unsigned dst = area0 + 8192 + (j & 1) * 4096;
        if (!isgu) { const int u = (gw - 896) * 4 + (j & 3); pack_load((const char*)wo + (size_t)(u >> 4) * 65536 + (u & 15) * 128, 2048, dst, lane); }
        else if (j < 4) pack_load((const char*)(mat ? wu : wg) + (size_t)(k8 * 4 + j) * 32 * 7168 + cg * 128, 7168, dst, lane);
        else { const int u = (j - 4) * 896 + gw; pack_load((const char*)wd + (size_t)(u >> 4) * 65536 + (u & 15) * 128, 2048, dst, lane); }
    };
    f32x4 ut[4] = {ZERO4, ZERO4, ZERO4, ZERO4};
    unsigned guo[4];
#pragma unroll
    for (int nf = 0; nf < 4; ++nf) {
        const int cj = cg * 8 + 2 * nf + ((lane >> 3) & 1), ct = cj / 14, r = cj - ct * 14, wn = r >= 7, c = r - wn * 7;
        const int fr = c < 6 ? mat * 3 + (c >> 1) : 6, hf = c < 6 ? (c & 1) : mat;
        guo[nf] = (unsigned)((ct * 32 * 14 + wn * 7 + fr) * 1024 + (lane >> 4) * 256 + (hf * 8 + (lane & 7)) * 16);
        PIN(guo[nf]);
    }
    auto unit_store = [&](int j) {
        const lds_ptr src = area + 8192 + (j & 1) * 4096;
        bf16x8 f[4];
#pragma unroll
        for (int nf = 0; nf < 4; ++nf) f[nf] = pack_frag(src, nf, lane);
        if (!isgu || j >= 4) {
            const int u = isgu ? (j - 4) * 896 + gw : (gw - 896) * 4 + (j & 3);
            const char* d = isgu ? ws + WS_WD + (size_t)((((u & 15) >> 1) * 112 + (u >> 4)) * 2 + (u & 1)) * 4096 : ws + WS_WO + (size_t)((u & 15) * 32 + (u >> 4)) * 4096;
#pragma unroll
            for (int nf = 0; nf < 4; ++nf) st16(d, voff + nf * 1024, f[nf]);
        } else {
            bf16x8 vf = ldsr128(area + 4096 + (lane & 1) * 256 + j * 64 + (lane >> 4) * 16);
#pragma unroll
            for (int nf = 0; nf < 4; ++nf) {
                st16(ws + WS_WGU + (size_t)(k8 * 4 + j) * 14336, guo[nf], f[nf]);
                ut[nf] = MFMA16(f[nf], vf, ut[nf]);
            }
        }
    };
    {
        const unsigned vo = (unsigned)((lane < 32 ? 2048 : 1024) + (k8 & 7) * 128 + (lane & 31) * 4) * 4u;
#pragma unroll
        for (int i = 0; i < 8; ++i) LD16C(vp[i], ws + WS_MP2, vo + i * 16384);
    }
#define DBA(i) do {} while (0)
#define DBB(i) do {} while (0)
    unit_load(0);
    STAMP(3);
    WAITV_BAR(17);
    KV_STAGE(2, 65536);
    WAIT_V(8);
    mod_unit(area, silu_frag(cw), area + 5120, lane);
    {
        f32x4 v = rbf4(sum8(vp));
        if (lane < 32) v = rbf4(v + 1.f);
        *(LAS u32x2*)(area + 4096 + lane * 8) = (u32x2){cvtpk(v[0], v[1]), cvtpk(v[2], v[3])};
    }
    unit_store(0); unit_load(1);
    STAMP(4);
    WAITVL_BAR(8);
    KV_STAGE(3, 98304);
    if (pw == 0) {
        const LAS float* r = (const LAS float*)(cx.lds + 5120) + lane;
        st4(ws + WS_MP2, (unsigned)((cx.bid >> 5) * 4096 + col2 + lane) * 4u, (r[0] + r[4096]) + (r[8192] + r[12288]));
    }
    WAIT_V(8); unit_store(1); unit_load(2);
    STAMP(5);
    DBA(0); WAITVL_BAR(8); DBB(0); KV_STAGE(4, 131072); WAIT_V(8); unit_store(2); unit_load(3);
    DBA(1); WAITVL_BAR(8); DBB(1); KV_STAGE(5, 65536); WAIT_V(8); unit_store(3); unit_load(4);
    if (isgu && (lane & 15) < 2) {
#pragma unroll
        for (int nf = 0; nf < 4; ++nf) st16(ws + WS_UT, (unsigned)((k8 * 4 + mat * 2 + (lane & 15)) * F + cg * 64 + nf * 16 + (lane >> 4) * 4) * 4u, ut[nf]);
    }
    DBA(2); WAITVL_BAR(8); DBB(2); KV_STAGE(6, 98304); WAIT_V(8); unit_store(4); unit_load(5);
    DBA(3); WAITVL_BAR(8); DBB(3); KV_STAGE(7, 131072); WAIT_V(8); unit_store(5);
    {
        const u32x4 e = {~0u, ~0u, ~0u, ~0u};
#pragma unroll
        for (int i = 0; i < 4; ++i) st16(ws + WS_XCH + (size_t)gw * 4096, voff + i * 1024, e);
    }
    WAITVL_BAR(0);
    if (pw == 0) signal(ws, C_SIDE + (cx.bid & 15), lane);
#undef KV_STAGE
    if (MEGA && pw == 0) { __builtin_amdgcn_s_sleep(40); await<20>(ws, C_SIDE, 16, 32, lane); }
    BARRIER();
    STAMP(6);
    next_pre();
    STAMP(7);
    BARRIER();
    if (pw == 0) signal(ws, C_O + rb, lane);
    PSTAMP_FLUSH(10);
}
__device__ __forceinline__ void attn_consume(const Ctx& cx) {
    char* ws = cx.ws; const lds_ptr lds = cx.lds; const int wid = cx.w, lane = lane_id();
    STAMP_DECL; STAMP(0);
    f32x4 qa[2][2]; f32x2 kn2, vm2; float knm, vmm;
    unsigned cv;
    int rb, hp; tile128x64(cx.bid, rb, hp);
    unsigned tch = 0u;
    g64_consume(qa, cx, WS_WQ + (size_t)hp * 131072, tch, lane, [&](int t) {
            if (MEGA) {
                if (t == 6) asm volatile("s_nop 4\n\tglobal_load_dword %0, %1, %2 sc1" : "=v"(cv) : "v"(wid == 0 ? (lane & 31) * 256 : 0), "s"(ws + WS_CNT) : "memory");
                if (t == 10) { asm volatile("s_waitcnt vmcnt(0)" : "+v"(cv) :: "memory"); if (wid == 0 && !__all(cv >= 16u)) await<6>(ws, C_H1, 32, 16, lane); }
            }
            if (t == 12) asm volatile("s_nop 4\n\tglobal_load_dwordx2 %0, %2, %3 sc1\n\tglobal_load_dwordx2 %1, %2, %3 offset:512 sc1" : "=&v"(kn2), "=&v"(vm2) : "v"(lane * 8), "s"(ws + WS_KN) : "memory"); },
        [&]() { asm volatile("s_waitcnt vmcnt(0)" : "+v"(kn2), "+v"(vm2), "+v"(tch) :: "memory"); knm = wmax(fmaxf(kn2[0], kn2[1])); vmm = wmax(fmaxf(vm2[0], vm2[1])); PIN(knm); PIN(vmm); SBAR(); },
        [&]() { STAMP(5); });
    BAR_L();
    STAMP(2);
    constexpr float QS = 0.17677669529663687f * 1.4426950408889634f;
    u32x4 qw0, qw1;
    qw0 = swap16(cvtpk(qa[0][0][0] * QS, qa[0][0][1] * QS), cvtpk(qa[0][0][2] * QS, qa[0][0][3] * QS), cvtpk(qa[1][0][0] * QS, qa[1][0][1] * QS), cvtpk(qa[1][0][2] * QS, qa[1][0][3] * QS));
    qw1 = swap16(cvtpk(qa[0][1][0] * QS, qa[0][1][1] * QS), cvtpk(qa[0][1][2] * QS, qa[0][1][3] * QS), cvtpk(qa[1][1][0] * QS, qa[1][1][1] * QS), cvtpk(qa[1][1][2] * QS, qa[1][1][3] * QS));
    float qn = 0.f;
#pragma unroll
    for (int j = 0; j < 4; ++j) { qn += bflo(qw0[j]) * bflo(qw0[j]) + bfhi(qw0[j]) * bfhi(qw0[j]) + bflo(qw1[j]) * bflo(qw1[j]) + bfhi(qw1[j]) * bfhi(qw1[j]); }
    qn = half_sum(qn);
    const bool fast = __all((qn * knm <= 88.f * 88.f) && (vmm <= 16777216.f) && (vmm >= 9.5367431640625e-7f));
    const bf16x8 q0 = __builtin_bit_cast(bf16x8, qw0), q1 = __builtin_bit_cast(bf16x8, qw1);
    f32x16 o = {};
    float linv;
    const lds_ptr pl = lds + lane * 16;
#define EX(x) __builtin_amdgcn_exp2f(x)
#define BF8(x) __builtin_bit_cast(bf16x8, x)
    if (fast) {
        const short ov = ((lane & 15) == ((lane >> 4) & 1)) ? (short)0x3f80 : (short)0;
        const bf16x8 sel = {ov, ov, ov, ov, ov, ov, ov, ov};
        f32x4 ls = ZERO4;
        const f32x16 zero = {};
        f32x16 sA, sB;
        bf16x8 k0, k1, v0, v1;
        u32x4 p0, p1 = {0u, 0u, 0u, 0u};
        {
            const lds_ptr pb = pl + 98304;
            k0 = ldsr128(pb); k1 = ldsr128(pb + 1024);
            sA = MFMA32(k0, q0, zero); sA = MFMA32(k1, q1, sA);
            k0 = ldsr128(pb + 4096); k1 = ldsr128(pb + 4096 + 1024); v0 = ldsr128(pb + 2048); v1 = v0;
        }
#define ATT_TILE(SC, SN, PK, PV0, PV1) do { \
            SN = MFMA32(k0, q0, zero); PIN(SN); SBAR(); k0 = ldsr128(PK); \
            SC[0] = EX(SC[0]); SC[1] = EX(SC[1]); SC[2] = EX(SC[2]); SC[3] = EX(SC[3]); PIN(SC); SBAR(); \
            o = MFMA32(v1, BF8(p1), o); PIN(o); SBAR(); v1 = ldsr128(PV1); \
            SC[4] = EX(SC[4]); SC[5] = EX(SC[5]); SC[6] = EX(SC[6]); PIN(SC); SBAR(); \
            ls = MFMA16(sel, BF8(p1), ls); PIN(ls); SBAR(); SC[7] = EX(SC[7]); p0[0] = cvtpk(SC[0], SC[1]); p0[1] = cvtpk(SC[2], SC[3]); PIN(SC); PIN(p0); SBAR(); \
            SN = MFMA32(k1, q1, SN); PIN(SN); SBAR(); k1 = ldsr128((PK) + 1024); \
            p0[2] = cvtpk(SC[4], SC[5]); p0[3] = cvtpk(SC[6], SC[7]); PIN(p0); \
            SC[8] = EX(SC[8]); SC[9] = EX(SC[9]); SC[10] = EX(SC[10]); PIN(SC); SBAR(); \
            o = MFMA32(v0, BF8(p0), o); PIN(o); SBAR(); v0 = ldsr128(PV0); \
            SC[11] = EX(SC[11]); SC[12] = EX(SC[12]); SC[13] = EX(SC[13]); PIN(SC); SBAR(); \
            ls = MFMA16(sel, BF8(p0), ls); PIN(ls); SBAR(); \
            SC[14] = EX(SC[14]); SC[15] = EX(SC[15]); \
            p1[0] = cvtpk(SC[8], SC[9]); p1[1] = cvtpk(SC[10], SC[11]); p1[2] = cvtpk(SC[12], SC[13]); p1[3] = cvtpk(SC[14], SC[15]); PIN(p1); SBAR(); } while (0)
        int so = 98304;
        STAMP(1);
#pragma unroll 1
        for (int st = 0; st < 8; ++st) {
            const int sn = so == 131072 ? 65536 : so + 32768;
            const lds_ptr pc = pl + so, pn = pl + sn;
            __builtin_amdgcn_s_setprio(1);
            ATT_TILE(sA, sB, pc + 2 * 4096, pc + 1 * 4096 + 2048, pc + 0 * 4096 + 3072);
            ATT_TILE(sB, sA, pc + 3 * 4096, pc + 2 * 4096 + 2048, pc + 1 * 4096 + 3072);
            ATT_TILE(sA, sB, pc + 4 * 4096, pc + 3 * 4096 + 2048, pc + 2 * 4096 + 3072);
            ATT_TILE(sB, sA, pc + 5 * 4096, pc + 4 * 4096 + 2048, pc + 3 * 4096 + 3072);
            __builtin_amdgcn_s_setprio(0);
            ATT_TILE(sA, sB, pc + 6 * 4096, pc + 5 * 4096 + 2048, pc + 4 * 4096 + 3072);
            ATT_TILE(sB, sA, pc + 7 * 4096, pc + 6 * 4096 + 2048, pc + 5 * 4096 + 3072);
            if (st < 7) BAR_L();
            if (st == 1) STAMP(6);
            if (st == 4) STAMP(7);
            if (st < 7) __builtin_amdgcn_s_setprio(2);
            SBAR();
            ATT_TILE(sA, sB, pn, pc + 7 * 4096 + 2048, pc + 6 * 4096 + 3072);
            ATT_TILE(sB, sA, pn + 4096, pn + 2048, pc + 7 * 4096 + 3072);
            so = sn;
        }
        o = MFMA32(v1, BF8(p1), o);
        ls = MFMA16(sel, BF8(p1), ls);
#undef ATT_TILE
        const float la = __shfl(ls[0], lane & 15), lb = __shfl(ls[1], lane & 15);
        linv = 1.f / ((lane & 16) ? lb : la);
    } else {
        float m = -INFINITY, l = 0.f;
        const f32x16 zero = {};
        int so = 98304;
#pragma unroll 1
        for (int st = 0; st < 8; ++st) {
            const lds_ptr pb = pl + so;
#pragma unroll 1
            for (int t = 0; t < 8; ++t) {
                if (t == 6 && st < 7) BAR_L();
                const bf16x8 k0 = ldsr128(pb + t * 4096), k1 = ldsr128(pb + t * 4096 + 1024), v0 = ldsr128(pb + t * 4096 + 2048), v1 = ldsr128(pb + t * 4096 + 3072);
                f32x16 s = MFMA32(k0, q0, zero);
                s = MFMA32(k1, q1, s);
                float tm = s[0];
#pragma unroll
                for (int j = 1; j < 16; ++j) tm = fmaxf(tm, s[j]);
                tm = fmaxf(tm, __shfl_xor(tm, 32));
                const float mn = fmaxf(m, tm), al = EX(m - mn);
                m = mn; l *= al;
#pragma unroll
                for (int j = 0; j < 16; ++j) o[j] *= al;
                u32x4 p0, p1;
#pragma unroll
                for (int j = 0; j < 4; ++j) {
                    p0[j] = cvtpk(EX(s[2 * j] - mn), EX(s[2 * j + 1] - mn));
                    p1[j] = cvtpk(EX(s[8 + 2 * j] - mn), EX(s[8 + 2 * j + 1] - mn));
                    l += (bflo(p0[j]) + bfhi(p0[j])) + (bflo(p1[j]) + bfhi(p1[j]));
                }
                o = MFMA32(v0, BF8(p0), o);
                o = MFMA32(v1, BF8(p1), o);
            }
            so = so == 131072 ? 65536 : so + 32768;
        }
        linv = 1.f / half_sum(l);
    }
#undef EX
#undef BF8
    STAMP(3);
    {
        const char* ob = ws + WS_O + ((size_t)(rb * 8 + (wid & 3) * 2) * 128 + (hp * 2 + (wid >> 2)) * 4) * 256;
        const unsigned vo = (unsigned)(((lane >> 4) & 1) * 32768 + (lane >> 5) * 256 + (lane & 15) * 16);
#pragma unroll
        for (int gp = 0; gp < 2; ++gp) {
            const auto r0 = __builtin_amdgcn_permlane32_swap(cvtpk(o[8 * gp] * linv, o[8 * gp + 1] * linv), cvtpk(o[8 * gp + 4] * linv, o[8 * gp + 5] * linv), false, false);
            const auto r1 = __builtin_amdgcn_permlane32_swap(cvtpk(o[8 * gp + 2] * linv, o[8 * gp + 3] * linv), cvtpk(o[8 * gp + 6] * linv, o[8 * gp + 7] * linv), false, false);
            st16(ob, vo + gp * 512, ((u32x4){r0[0], r1[0], r0[1], r1[1]}));
        }
    }
    BAR_L();
    WAITV_BAR(0);
    STAMP(4); STAMP_FLUSH(2);
}

struct OPre { f32x4 p[8]; };
__device__ __forceinline__ void oproj_pre(const Ctx& cx, OPre& o) {
    const int lane = lane_id(); int rb, ct; tile128x64(cx.bid, rb, ct);
    const unsigned vo = (unsigned)(((lane >> 4) & 1) * 2048 + ct * 64 + (lane & 15) * 4) * 4u;
#pragma unroll
    for (int i = 0; i < 8; ++i) LD16C(o.p[i], cx.ws + WS_MP2, vo + i * 16384);
    g64_pre(cx, cx.ws + WS_WO + (size_t)ct * 131072, lane);
}
template <class Next>
__device__ __forceinline__ void oproj_produce(const Ctx& cx, OPre& o, const u16* __restrict__ x, Next next_pre) {
    char* ws = cx.ws; const int pw = cx.w, lane = lane_id();
    int rb, ct; tile128x64(cx.bid, rb, ct);
    STAMP_DECL; STAMP(0);
    await_input(ws, pw, C_O + rb, 16, lane);
    STAMP(1);
    f32x4 tv;
    g64_produce<4>(cx, ws + WS_O + (size_t)rb * 8 * 32768, ws + WS_WO + (size_t)ct * 131072, lane,
        [&]() { STAMP(2); tv = rbf4(sum8(o.p)); if (lane & 16) tv = rbf4(tv + 1.f); PIN(tv); },
        [&]() {
            const unsigned voffX = (unsigned)((rb * 128 + pw * 32 + (lane >> 3)) * 2048 + ct * 128 + (((lane & 7) ^ ((lane >> 3) & 7)) << 4));
#pragma unroll
            for (int i = 0; i < 4; ++i) dma16((const char*)x + i * 8 * 2048, voffX, cx.lds0 + 147456 + pw * 4096 + i * 1024);
            if (pw == 0 && lane < 32) *(LAS f32x4*)(cx.lds + 98304 + lane * 16) = tv; });
    WAITV_BAR(0);
    BARRIER();
    next_pre();
    BARRIER();
    if (pw == 0) signal(ws, C_Y + (rb >> 1), lane);
    PSTAMP_FLUSH(11);
}
__device__ __forceinline__ void oproj_consume(const Ctx& cx) {
    char* ws = cx.ws; const lds_ptr lds = cx.lds; const int wid = cx.w, lane = lane_id();
    STAMP_DECL; STAMP(0);
    f32x4 acc[2][2];
    int rb, ct; tile128x64(cx.bid, rb, ct);
    unsigned tch = 0u;
    g64_consume(acc, cx, WS_WO + (size_t)ct * 131072, tch, lane, [](int) {}, []() {}, [&]() { STAMP(4); });
    asm volatile("s_waitcnt vmcnt(0) lgkmcnt(0)\n\ts_barrier" : "+v"(tch) :: "memory");
    STAMP(1);
    const int tb = wid & 3, nh = wid >> 2, g = lane >> 4, gc = ((g & 1) << 1) | (g >> 1);
    f32x4 ga[2], cb[2]; u32x4 x1v[2];
#pragma unroll
    for (int nf = 0; nf < 2; ++nf) { ga[nf] = *(const LAS f32x4*)(lds + 98304 + (nh * 8 + nf * 4 + g) * 16); cb[nf] = *(const LAS f32x4*)(lds + 98304 + 256 + (nh * 8 + nf * 4 + g) * 16); }
#pragma unroll
    for (int m = 0; m < 2; ++m) {
        const int rl = tb * 32 + m * 16 + (lane & 15);
        unsigned u[2][2], y[2][2]; float v[8];
#pragma unroll
        for (int nf = 0; nf < 2; ++nf) {
            const u32x2 xv = *(const LAS u32x2*)(lds + 147456 + rl * 128 + (((nh * 4 + nf * 2 + (g >> 1)) ^ (rl & 7)) << 4) + 8 * (g & 1));
            u[nf][0] = cvtpk(bflo(xv[0]) + ga[nf][0] * acc[m][nf][0], bfhi(xv[0]) + ga[nf][1] * acc[m][nf][1]);
            u[nf][1] = cvtpk(bflo(xv[1]) + ga[nf][2] * acc[m][nf][2], bfhi(xv[1]) + ga[nf][3] * acc[m][nf][3]);
            v[4 * nf] = bflo(u[nf][0]); v[4 * nf + 1] = bfhi(u[nf][0]); v[4 * nf + 2] = bflo(u[nf][1]); v[4 * nf + 3] = bfhi(u[nf][1]);
            y[nf][0] = cvtpk(v[4 * nf] * cb[nf][0], v[4 * nf + 1] * cb[nf][1]); y[nf][1] = cvtpk(v[4 * nf + 2] * cb[nf][2], v[4 * nf + 3] * cb[nf][3]);
        }
        st16(ws + WS_Y + ((size_t)(rb * 8 + tb * 2 + m) * 128 + ct * 8 + nh * 4) * 256, (unsigned)(gc * 256 + (lane & 15) * 16), swap16(y[0][0], y[0][1], y[1][0], y[1][1]));
        x1v[m] = swap16(u[0][0], u[0][1], u[1][0], u[1][1]);
        const float mean = rows_sum(((v[0] + v[1]) + (v[2] + v[3])) + ((v[4] + v[5]) + (v[6] + v[7]))) * (1.f / 32.f);
        float q = 0.f;
#pragma unroll
        for (int j = 0; j < 8; ++j) q += (v[j] - mean) * (v[j] - mean);
        q = rows_sum(q);
        if (g == 0) *(LAS f32x2*)(lds + 99328 + (rl * 2 + nh) * 8) = (f32x2){mean, q};
    }
    BAR_L();
    if (wid < 2) {
        const int rl = wid * 64 + lane;
        const f32x4 s = *(const LAS f32x4*)(lds + 99328 + rl * 16);
        const float dm = s[0] - s[2];
        st8(ws + WS_ST2 + (size_t)(ct * 2048 + rb * 128) * 8, (unsigned)rl * 8u, ((f32x2){0.5f * (s[0] + s[2]), s[1] + s[3] + 16.f * dm * dm}));
    }
    WAITV_BAR(0);
#pragma unroll
    for (int m = 0; m < 2; ++m) st16(ws + WS_X1 + ((size_t)(rb * 8 + tb * 2 + m) * 128 + ct * 8 + nh * 4) * 256, (unsigned)(gc * 256 + (lane & 15) * 16), x1v[m]);
    STAMP(2); STAMP_FLUSH(3);
}

constexpr int GSL = 30720, G_ROW = 5 * GSL + 1024, G_COL = G_ROW + 2048;
__device__ __forceinline__ void tile_glu(int bid, int& rp, int& ct) { const int xcd = bid & 7, idx = bid >> 3; rp = (xcd & 1) * 4 + (idx & 3); ct = (xcd >> 1) * 8 + (idx >> 2); }
#define dmax dmac
#define GLU_W(t, so) do { const char* w_ = Wb + (size_t)(t) * 14336; const unsigned d2_ = pw < 3 ? dW + (so) + 2048 : cx.lds0 + 5 * GSL; \
        dmax(w_, voff, dW + (so)); dmax(w_ + 1024, voff, dW + (so) + 1024); dmax(w_, voff2, d2_); dmax(w_ + 1024, voff2, pw < 3 ? d2_ + 1024 : d2_); } while (0)
__device__ __forceinline__ void glu_pre(const Ctx& cx) {
    const int pw = cx.w, lane = lane_id(); int rp, ct; tile_glu(cx.bid, rp, ct);
    const char* Wb = cx.ws + WS_WGU + (size_t)ct * 32 * 14336 + pw * 4096;
    const unsigned voff = lane * 16, dW = cx.lds0 + 16384 + pw * 4096;
#pragma unroll
    for (int t = 0; t < 4; ++t) {
        dmac(Wb + t * 14336, voff, dW + t * GSL); dmac(Wb + t * 14336 + 1024, voff, dW + t * GSL + 1024);
        if (pw < 3) { dmac(Wb + t * 14336 + 2048, voff, dW + t * GSL + 2048); dmac(Wb + t * 14336 + 3072, voff, dW + t * GSL + 3072); }
    }
}
template <class Next>
__device__ __forceinline__ void glu_produce(const Ctx& cx, Next next_pre) {
    char* ws = cx.ws; const int pw = cx.w, lane = lane_id(); int rp, ct; tile_glu(cx.bid, rp, ct);
    const char* Ab = ws + WS_Y + (size_t)(rp * 16 + 4 * pw) * 32768;
    const char* Wb = ws + WS_WGU + (size_t)ct * 32 * 14336 + pw * 4096;
    const unsigned voff = lane * 16, voff2 = pw < 3 ? lane * 16 + 2048 : 0u, dA = cx.lds0 + pw * 4096, dW = cx.lds0 + 16384 + pw * 4096;
#define GLU_A(t, so) do { const char* a_ = Ab + (size_t)(t) * 1024; dmax(a_, voff, dA + (so)); dmax(a_ + 32768, voff, dA + (so) + 1024); dmax(a_ + 65536, voff, dA + (so) + 2048); dmax(a_ + 98304, voff, dA + (so) + 3072); } while (0)
    STAMP_DECL; STAMP(0);
    await_input(ws, pw, C_Y + rp, 32, lane);
    STAMP(1);
    GLU_A(0, 0); GLU_A(1, GSL); GLU_A(2, 2 * GSL); GLU_A(3, 3 * GSL);
    STAMP(2);
    WAITV_BAR(12);
    STAMP(3);
    WAITV_BAR(8); GLU_W(4, 4 * GSL); GLU_A(4, 4 * GSL);
    WAITV_BAR(12); GLU_W(5, 0); GLU_A(5, 0);
    int sf = GSL;
#define GLU_NEXT(t, VM) do { WAITV_BAR(VM); GLU_W((t) + 4, sf); GLU_A((t) + 4, sf); sf = sf == 4 * GSL ? 0 : sf + GSL; } while (0)
#pragma unroll 1
    for (int t = 2; t < 10; ++t) GLU_NEXT(t, 16);
    {
        f32x2 sp[16]; f32x4 cp[8];
        const char* sb_ = ws + WS_ST2 + (size_t)(rp * 256 + pw * 64) * 8;
#pragma unroll
        for (int i = 0; i < 8; ++i) LD8C(sp[i], sb_, (unsigned)(lane * 8 + i * 16384));
        GLU_NEXT(10, 24);
        if (pw == 0) { int rbo, cto; tile128x64(cx.bid, rbo, cto); signal(ws, C_X1 + rbo, lane); }
#pragma unroll
        for (int i = 8; i < 16; ++i) LD8C(sp[i], sb_, (unsigned)(lane * 8 + i * 16384));
        GLU_NEXT(11, 32);
        int i4 = pw * 64 + lane; i4 = i4 < 112 ? i4 : 0;
        const unsigned vo = (unsigned)((i4 / 28) * F + ct * 112 + (i4 % 28) * 4) * 4u;
#pragma unroll
        for (int i = 0; i < 8; ++i) LD16C(cp[i], ws + WS_UT, vo + i * (4 * F * 4));
        GLU_NEXT(12, 40); GLU_NEXT(13, 32); GLU_NEXT(14, 24); GLU_NEXT(15, 16);
        __builtin_amdgcn_s_setprio(0);
        float mean = 0.f, q = 0.f;
#pragma unroll
        for (int i = 0; i < 16; ++i) { PIN(sp[i]); mean += sp[i][0]; q += sp[i][1]; }
        mean *= 1.f / 16.f; PIN(mean); PIN(q);
        GLU_NEXT(16, 16);
        float dq = 0.f;
#pragma unroll
        for (int i = 0; i < 16; ++i) dq += (sp[i][0] - mean) * (sp[i][0] - mean);
        PIN(dq);
        GLU_NEXT(17, 16);
        const float rstd = 1.f / sqrtf((q + 64.f * dq) * (1.f / 1024.f) + 1e-5f);
        *(LAS f32x2*)(cx.lds + G_ROW + (pw * 64 + lane) * 8) = (f32x2){rstd, -mean * rstd};
        GLU_NEXT(18, 16);
        const f32x4 c = sum8(cp);
        if (pw * 64 + lane < 112) *(LAS f32x4*)(cx.lds + G_COL + (pw * 64 + lane) * 16) = c;
        __builtin_amdgcn_s_setprio(3);
        GLU_NEXT(19, 16);
    }
#pragma unroll 1
    for (int t = 20; t < 28; ++t) GLU_NEXT(t, 16);
    WAITV_BAR(16); WAITV_BAR(8); WAITVL_BAR(0);
#undef GLU_NEXT
#undef GLU_A
    BARRIER();
    next_pre();
    BARRIER();
    if (pw == 0) signal(ws, C_HH + rp * 2 + (ct >> 4), lane);
    PSTAMP_FLUSH(13);
}
#undef GLU_W
__device__ __forceinline__ void glu_consume(const Ctx& cx) {
    char* ws = cx.ws; const lds_ptr lds = cx.lds; const int wm = cx.w & 3, wn = cx.w >> 2;
    STAMP_DECL; STAMP(0);
    f32x4 acc[4][7];
    {
        const int lane = lane_id();
        const lds_ptr pa = lds + wm * 4096 + lane * 16, pw = lds + 16384 + wn * 7168 + lane * 16;
        bf16x8 af[4], wf[4];
#pragma unroll
        for (int n = 0; n < 7; ++n)
#pragma unroll
            for (int m = 0; m < 4; ++m) acc[m][n] = ZERO4;
        INPUT_BARRIER();
        BARRIER();
        STAMP(1);
#pragma unroll
        for (int m = 0; m < 4; ++m) { af[m] = ldsr128(pa + m * 1024); wf[m] = ldsr128(pw + m * 1024); }
        int sc = 0, sn = GSL;
#define LAD(p) __builtin_amdgcn_s_setprio(p)
#define DBG_PRE(U) do {} while (0)
#define DBG_POST(U) do {} while (0)
#define GW(U, n) wf[(7 * (U) + (n)) & 3]
#define GLU_STEP(U, DO_NEXT) do { \
            DBG_PRE(U); if ((U) == 0) WAIT_V(0); if (DO_NEXT) BARRIER(); DBG_POST(U); LAD(1); SBAR(); \
            const lds_ptr pwc = pw + sc, pwn = pw + sn, pan = pa + sn; \
            _Pragma("unroll") for (int m = 0; m < 4; ++m) { MFMA16_ACC(acc[m][0], GW(U, 0), af[m]); MFMA16_ACC(acc[m][1], GW(U, 1), af[m]); } \
            SBAR(); GW(U, 4) = ldsr128(pwc + 4096); GW(U, 5) = ldsr128(pwc + 5120); SBAR(); \
            _Pragma("unroll") for (int m = 0; m < 4; ++m) MFMA16_ACC(acc[m][2], GW(U, 2), af[m]); \
            SBAR(); GW(U, 6) = ldsr128(pwc + 6144); SBAR(); \
            _Pragma("unroll") for (int m = 0; m < 4; ++m) MFMA16_ACC(acc[m][3], GW(U, 3), af[m]); \
            SBAR(); if (DO_NEXT) GW(U, 7) = ldsr128(pwn); SBAR(); \
            _Pragma("unroll") for (int m = 0; m < 4; ++m) MFMA16_ACC(acc[m][4], GW(U, 4), af[m]); \
            SBAR(); if (DO_NEXT) GW(U, 8) = ldsr128(pwn + 1024); SBAR(); \
            _Pragma("unroll") for (int m = 0; m < 4; ++m) { MFMA16_ACC(acc[m][5], GW(U, 5), af[m]); MFMA16_ACC(acc[m][6], GW(U, 6), af[m]); \
                SBAR(); if (DO_NEXT) af[m] = ldsr128(pan + m * 1024); if (m == X_GDROP) LAD(0); SBAR(); } \
            if (DO_NEXT) { GW(U, 9) = ldsr128(pwn + 2048); GW(U, 10) = ldsr128(pwn + 3072); } SBAR(); \
            sc = sn; sn = sn == 4 * GSL ? 0 : sn + GSL; } while (0)
#pragma unroll 1
        for (int t = 0; t < 28; t += 4) { if (t == 4) STAMP(5); if (t == 16) STAMP(6); if (t == 24) STAMP(7); GLU_STEP(0, true); GLU_STEP(1, true); GLU_STEP(2, true); GLU_STEP(3, true); }
        GLU_STEP(0, true); GLU_STEP(1, true); GLU_STEP(2, true); GLU_STEP(3, false);
#undef GLU_STEP
#undef GW
    }
    asm volatile("s_nop 7\n\ts_nop 7" : "+v"(acc[0][6]), "+v"(acc[1][6]), "+v"(acc[2][6]), "+v"(acc[3][6]), "+v"(acc[0][5]), "+v"(acc[1][5]), "+v"(acc[2][5]), "+v"(acc[3][5]) :: "memory");
    SBAR();
    STAMP(2);
    const int lane = lane_id(), g = lane >> 4, gc = ((g & 1) << 1) | (g >> 1);
    int rp, ct; tile_glu(cx.bid, rp, ct);
    const char* hb = ws + WS_HH + ((size_t)(rp * 16 + wm * 4) * (F / 8) + ct * 14 + wn * 7) * 256;
    const unsigned vo = (lane & 15) * 16;
    constexpr unsigned RB = (F / 8) * 256;
    f32x2 rw[4];
#pragma unroll
    for (int m = 0; m < 4; ++m) rw[m] = *(const LAS f32x2*)(lds + G_ROW + (wm * 64 + m * 16 + (lane & 15)) * 8);
#define GLU_OUT(m, f, ug, tg, uu, tu, d0, d1) do { const f32x4 gt = acc[m][f] * rw[m][0] + (ug * rw[m][1] + tg), up = acc[m][3 + (f)] * rw[m][0] + (uu * rw[m][1] + tu); \
        d0 = cvtpk(fmaxf(gt[0], 0.f) * up[0], fmaxf(gt[1], 0.f) * up[1]); d1 = cvtpk(fmaxf(gt[2], 0.f) * up[2], fmaxf(gt[3], 0.f) * up[3]); } while (0)
    {
        const lds_ptr pc = lds + G_COL + (wn * 14 + g) * 16;
        f32x4 ct_[3][4];
#pragma unroll
        for (int f = 0; f < 2; ++f)
#pragma unroll
            for (int k = 0; k < 4; ++k) ct_[f][k] = *(const LAS f32x4*)(pc + f * 64 + k * 448);
#pragma unroll
        for (int m = 0; m < 4; ++m) {
            unsigned a0, a1, b0, b1;
            GLU_OUT(m, 0, ct_[0][0], ct_[0][1], ct_[0][2], ct_[0][3], a0, a1); GLU_OUT(m, 1, ct_[1][0], ct_[1][1], ct_[1][2], ct_[1][3], b0, b1);
            st16(hb, vo + m * RB + gc * 256, swap16(a0, a1, b0, b1));
            if (m == 1) { _Pragma("unroll") for (int k = 0; k < 4; ++k) ct_[2][k] = *(const LAS f32x4*)(pc + 128 + k * 448); }
            SBAR();
        }
#pragma unroll
        for (int mp = 0; mp < 2; ++mp) {
            unsigned a0, a1, b0, b1;
            GLU_OUT(2 * mp, 2, ct_[2][0], ct_[2][1], ct_[2][2], ct_[2][3], a0, a1); GLU_OUT(2 * mp + 1, 2, ct_[2][0], ct_[2][1], ct_[2][2], ct_[2][3], b0, b1);
            st16(hb, vo + (2 * mp + (g & 1)) * RB + (4 + (g >> 1)) * 256, swap16(a0, a1, b0, b1));
            SBAR();
        }
    }
#undef GLU_OUT
    {
        const lds_ptr pc = lds + G_COL + (g >> 1) * 896 + (wn * 14 + 12 + (g & 1)) * 16;
        const f32x4 uc = *(const LAS f32x4*)pc, tc = *(const LAS f32x4*)(pc + 448);
        unsigned ux[2][2];
#pragma unroll
        for (int mp = 0; mp < 2; ++mp) {
            const f32x4 a = acc[2 * mp][6] * rw[2 * mp][0] + (uc * rw[2 * mp][1] + tc), b = acc[2 * mp + 1][6] * rw[2 * mp + 1][0] + (uc * rw[2 * mp + 1][1] + tc);
            float hv[4];
#pragma unroll
            for (int r = 0; r < 4; ++r) {
                const auto x = __builtin_amdgcn_permlane32_swap(__float_as_uint(a[r]), __float_as_uint(b[r]), false, false);
                hv[r] = fmaxf(__uint_as_float(x[0]), 0.f) * __uint_as_float(x[1]);
            }
            ux[mp][0] = cvtpk(hv[0], hv[1]); ux[mp][1] = cvtpk(hv[2], hv[3]);
        }
        st16(hb, vo + gc * RB + 6 * 256, swap16(ux[0][0], ux[0][1], ux[1][0], ux[1][1]));
    }
    STAMP(3);
    BARRIER();
    WAITV_BAR(0);
    STAMP(4); STAMP_FLUSH(5);
}

__device__ __forceinline__ void tile_down(int bid, int& rb, int& ct, int& kh) { const int xcd = bid & 7, idx = bid >> 3; kh = xcd & 1; rb = (xcd >> 1) * 4 + (idx & 3); ct = idx >> 2; }
#define DN_W(t, so) do { const char* w_ = Wb + (size_t)(t) * 16384; dmac(w_, voff, dW + (so)); dmac(w_ + 1024, voff, dW + (so) + 1024); dmac(w_ + 2048, voff, dW + (so) + 2048); dmac(w_ + 3072, voff, dW + (so) + 3072); } while (0)
__device__ __forceinline__ void down_pre(const Ctx& cx, OPre& o) {
    const int pw = cx.w, lane = lane_id(); int rb, ct, kh; tile_down(cx.bid, rb, ct, kh);
    if (pw == 0) await<4>(cx.ws, C_X1 + rb, 1, 16, lane);
    const unsigned vo = (unsigned)(3072 + ct * 128 + (lane & 31) * 4) * 4u;
#pragma unroll
    for (int i = 0; i < 8; ++i) LD16C(o.p[i], cx.ws + WS_MP2, vo + i * 16384);
    const char* Wb = cx.ws + WS_WD + (size_t)(ct * 112 + kh * 56) * 8192 + pw * 4096;
    const unsigned voff = lane * 16, dW = cx.lds0 + 16384 + pw * 4096;
#pragma unroll
    for (int t = 0; t < 4; ++t) DN_W(t, t * 32768);
}
__device__ __forceinline__ void down_produce(const Ctx& cx, OPre& o) {
    char* ws = cx.ws; const int pw = cx.w, lane = lane_id(); int rb, ct, kh; tile_down(cx.bid, rb, ct, kh);
    const char* Ab = ws + WS_HH + ((size_t)(rb * 8 + 2 * pw) * (F / 8) + kh * 224) * 256;
    const char* Wb = ws + WS_WD + (size_t)(ct * 112 + kh * 56) * 8192 + pw * 4096;
    const unsigned voff = lane * 16, dA = cx.lds0 + pw * 4096, dW = cx.lds0 + 16384 + pw * 4096;
#define DN_A(t, so) do { const char* a_ = Ab + (size_t)(t) * 2048; dmac(a_, voff, dA + (so)); dmac(a_ + 1024, voff, dA + (so) + 1024); \
        dmac(a_ + (F / 8) * 256, voff, dA + (so) + 2048); dmac(a_ + (F / 8) * 256 + 1024, voff, dA + (so) + 3072); } while (0)
    await_input(ws, pw, C_HH + (rb >> 1) * 2 + kh, 16, lane);
    DN_A(0, 0); DN_A(1, 32768); DN_A(2, 65536); DN_A(3, 98304);
    WAITV_BAR(12);
    WAITV_BAR(8); DN_W(4, 131072); DN_A(4, 131072);
    f32x4 tv = rbf4(sum8(o.p)); PIN(tv);
    WAITV_BAR(12); DN_W(5, 0); DN_A(5, 0);
    int sf = 32768;
#pragma unroll 1
    for (int t = 2; t < 24; ++t) {
        WAITV_BAR(16);
        DN_W(t + 4, sf); DN_A(t + 4, sf);
        sf = sf == 131072 ? 0 : sf + 32768;
    }
    WAITV_BAR(16);
    {
        const char* xb = ws + WS_X1 + ((size_t)(rb * 8 + (pw >> 1) * 4 + kh * 2 + (pw & 1)) * 128 + ct * 16) * 256;
#pragma unroll
        for (int i = 0; i < 4; ++i) dmac(xb + i * 1024, voff, cx.lds0 + 98304 + pw * 4096 + i * 1024);
        if (pw == 0 && lane < 32) *(LAS f32x4*)(cx.lds + 114688 + lane * 16) = tv;
    }
    WAITV_BAR(12); WAITV_BAR(4); WAITVL_BAR(0);
#undef DN_A
}
#undef DN_W
__device__ __forceinline__ void down_consume(const Ctx& cx, u16* __restrict__ out) {
    char* ws = cx.ws; const lds_ptr lds = cx.lds; const int wid = cx.w, lane = lane_id(), wm = wid >> 2, wn = wid & 3, g = lane >> 4;
    STAMP_DECL; STAMP(0);
    const lds_ptr pa = lds + wm * 8192 + lane * 16, pw = lds + 16384 + wn * 2048 + lane * 16;
    f32x4 acc[4][2];
#pragma unroll
    for (int m = 0; m < 4; ++m) { acc[m][0] = ZERO4; acc[m][1] = ZERO4; }
    bf16x8 aA[8], wA[4], aB[8], wB[4];
    INPUT_BARRIER();
    BARRIER(); SBAR();
    STAMP(1);
#pragma unroll
    for (int ks = 0; ks < 2; ++ks) {
        wA[2 * ks] = ldsr128(pw + ks * 8192); wA[2 * ks + 1] = ldsr128(pw + ks * 8192 + 1024);
#pragma unroll
        for (int m = 0; m < 4; ++m) aA[4 * ks + m] = ldsr128(pa + m * 2048 + ks * 1024);
    }
    SBAR();
    int sn = 32768;
#define DBH_PRE() do {} while (0)
#define DBH_POST() do {} while (0)
#define DN_STEP(AC, WC, AN, WN, DO_NEXT) do { \
        DBH_PRE(); if (DO_NEXT) BARRIER(); DBH_POST(); LAD(1); SBAR(); \
        const lds_ptr pan = pa + sn, pwn = pw + sn; \
        _Pragma("unroll") for (int i = 0; i < 8; ++i) { const int ks = i >> 2, m = i & 3; \
            MFMA16_ACC(acc[m][0], WC[2 * ks], AC[i]); MFMA16_ACC(acc[m][1], WC[2 * ks + 1], AC[i]); SBAR(); \
            if (DO_NEXT) { if (m < 2) WN[2 * ks + m] = ldsr128(pwn + ks * 8192 + m * 1024); AN[i] = ldsr128(pan + m * 2048 + ks * 1024); } \
            if (i == X_HDROP) LAD(0); \
            SBAR(); } \
        sn = sn == 131072 ? 0 : sn + 32768; } while (0)
#pragma unroll 1
    for (int t = 0; t < 26; t += 2) {
        if (t == 4) STAMP(6); if (t == 14) STAMP(7);
        DN_STEP(aA, wA, aB, wB, true); DN_STEP(aB, wB, aA, wA, true);
    }
    DN_STEP(aA, wA, aB, wB, true); DN_STEP(aB, wB, aA, wA, false);
#undef DN_STEP
    asm volatile("s_nop 7\n\ts_nop 7" : "+v"(acc[0][0]), "+v"(acc[0][1]), "+v"(acc[1][0]), "+v"(acc[1][1]), "+v"(acc[2][0]), "+v"(acc[2][1]), "+v"(acc[3][0]), "+v"(acc[3][1]));
    SBAR();
    STAMP(2);
    int rb, ct, kh; tile_down(cx.bid, rb, ct, kh);
    f32x4 keep[2][2];
#pragma unroll
    for (int mm = 0; mm < 2; ++mm) {
        const f32x4 g0 = kh ? acc[mm][0] : acc[2 + mm][0], g1 = kh ? acc[mm][1] : acc[2 + mm][1];
        st16(ws + WS_XCH + ((size_t)cx.bid * 8 + wid) * 2048, lane * 16 + mm * 1024, ((u32x4){cvtpk(g0[0], g0[1]), cvtpk(g0[2], g0[3]), cvtpk(g1[0], g1[1]), cvtpk(g1[2], g1[3])}));
        keep[mm][0] = kh ? acc[2 + mm][0] : acc[mm][0]; keep[mm][1] = kh ? acc[2 + mm][1] : acc[mm][1];
    }
    BARRIER();
    u32x2 xr[2][2]; f32x4 gb[2];
#pragma unroll
    for (int nf = 0; nf < 2; ++nf) {
        gb[nf] = *(const LAS f32x4*)(lds + 114688 + (wn * 8 + nf * 4 + g) * 16);
#pragma unroll
        for (int mm = 0; mm < 2; ++mm) xr[mm][nf] = *(const LAS u32x2*)(lds + 98304 + ((wm * 2 + mm) * 16 + wn * 4 + nf * 2 + (g >> 1)) * 256 + (lane & 15) * 16 + 8 * (g & 1));
    }
    STAMP(5);
    u32x4 got[2];
    {
        const char* xi = ws + WS_XCH + ((size_t)(cx.bid ^ 1) * 8 + wid) * 2048;
        for (unsigned spins = 0; spins < (1u << 15); ++spins) {
            asm volatile("s_nop 4\n\tglobal_load_dwordx4 %0, %2, %3 sc1\n\tglobal_load_dwordx4 %1, %2, %3 offset:1024 sc1\n\ts_waitcnt vmcnt(0)" : "=&v"(got[0]), "=&v"(got[1]) : "v"(lane * 16), "s"(xi) : "memory");
            const bool ok = got[0][0] != ~0u && got[0][1] != ~0u && got[0][2] != ~0u && got[0][3] != ~0u && got[1][0] != ~0u && got[1][1] != ~0u && got[1][2] != ~0u && got[1][3] != ~0u;
            if (__all(ok)) break;
            __builtin_amdgcn_s_sleep(2);
        }
    }
    STAMP(3);
#pragma unroll
    for (int mm = 0; mm < 2; ++mm) {
        unsigned u[2][2];
#pragma unroll
        for (int nf = 0; nf < 2; ++nf) {
            const f32x4 gg = gb[nf], k = keep[mm][nf];
            u[nf][0] = cvtpk(bflo(xr[mm][nf][0]) + gg[0] * (k[0] + bflo(got[mm][2 * nf])), bfhi(xr[mm][nf][0]) + gg[1] * (k[1] + bfhi(got[mm][2 * nf])));
            u[nf][1] = cvtpk(bflo(xr[mm][nf][1]) + gg[2] * (k[2] + bflo(got[mm][2 * nf + 1])), bfhi(xr[mm][nf][1]) + gg[3] * (k[3] + bfhi(got[mm][2 * nf + 1])));
        }
        st16(out + (size_t)(rb * 128 + wm * 64 + (2 * kh + mm) * 16) * D + ct * 128 + wn * 32, (unsigned)((lane & 15) * 2048 + (((g & 1) << 1) | (g >> 1)) * 16), swap16(u[0][0], u[0][1], u[1][0], u[1][1]));
    }
    STAMP(4); STAMP_FLUSH(6);
}

#define CTX_INIT extern __shared__ __attribute__((aligned(16))) char smem[]; \
    const int wid_ = uni(threadIdx.x >> 6); const bool prod = wid_ >= 8; \
    const Ctx cx = {ws, (lds_ptr)smem, uni((unsigned)(uintptr_t)(lds_ptr)smem), (int)blockIdx.x, prod ? wid_ - 8 : wid_}; \
    if (prod) __builtin_amdgcn_s_setprio(3)
__global__ __launch_bounds__(768, 3) void k_main(const u16* __restrict__ x, const u16* __restrict__ wq, const u16* __restrict__ wk, const u16* __restrict__ wv, const u16* __restrict__ wo,
                                                 const u16* __restrict__ wg, const u16* __restrict__ wu, const u16* __restrict__ wd, const u16* __restrict__ c, const u16* __restrict__ wmod,
                                                 char* __restrict__ ws, u16* __restrict__ out) {
    CTX_INIT;
    if (prod) {
        OPre o;
        ln1kv_produce(cx, wq, wk, wv, c, wmod, [&]() { attn_pre(cx); });
        attn_produce(cx, wo, wg, wu, wd, c, wmod, [&]() { oproj_pre(cx, o); });
        oproj_produce(cx, o, x, [&]() { glu_pre(cx); });
        glu_produce(cx, [&]() { down_pre(cx, o); });
        down_produce(cx, o);
    } else {
        ln1kv_consume(cx, x, c, wmod);
        attn_consume(cx);
        oproj_consume(cx);
        glu_consume(cx);
        down_consume(cx, out);
    }
}

extern "C" void kernel_launch(void* const* d_in, const int* in_sizes, int n_in, void* d_out, int out_size, void* d_ws, size_t ws_size, hipStream_t stream) {
    constexpr int LDS = 163840;
    (void)hipFuncSetAttribute((const void*)k_main, hipFuncAttributeMaxDynamicSharedMemorySize, LDS);
    k_main<<<256, 768, LDS, stream>>>((const u16*)d_in[0], (const u16*)d_in[1], (const u16*)d_in[2], (const u16*)d_in[3], (const u16*)d_in[4], (const u16*)d_in[6], (const u16*)d_in[7],
                                      (const u16*)d_in[8], (const u16*)d_in[9], (const u16*)d_in[10], (char*)d_ws, (u16*)d_out);
}
```

```cpp
#include <hip/hip_runtime.h>
#include <hip/hip_bf16.h>

#define X_SLEEP 2
#define X_GDROP 1
#define X_HDROP 5
#define X_S1 18
#define X_S2 8
#define MEGA 1
#define LAS __attribute__((address_space(3)))
#define GAS __attribute__((address_space(1)))
typedef unsigned short u16;
typedef short bf16x8 __attribute__((ext_vector_type(8)));
typedef short s16x4 __attribute__((ext_vector_type(4)));
typedef float f32x2 __attribute__((ext_vector_type(2)));
typedef float f32x4 __attribute__((ext_vector_type(4)));
typedef float f32x16 __attribute__((ext_vector_type(16)));
typedef unsigned u32x4 __attribute__((ext_vector_type(4)));
typedef unsigned u32x2 __attribute__((ext_vector_type(2)));
typedef __bf16 bf16x2v __attribute__((ext_vector_type(2)));
typedef LAS char* lds_ptr;

constexpr int D = 1024, F = 3584;
constexpr size_t WS_CNT = 0;
constexpr int C_KV = 0, C_SIDE = 16, C_O = 32, C_Y = 48, C_HH = 56, C_WQ = 72, C_H1 = 104, C_ST = 120, C_NUM = 128;
constexpr size_t WS_EPOCH = 98304 + 4096;
constexpr size_t WS_MP1 = 32768;
constexpr size_t WS_MP2 = 786432;
constexpr size_t WS_KN = 98304;
constexpr size_t WS_KV = 262144;
constexpr size_t WS_ST2 = 524288;
constexpr size_t WS_UT = 1u << 20;
constexpr size_t WS_H1 = 2u << 20;
constexpr size_t WS_O = WS_H1 + (4u << 20);
constexpr size_t WS_X1 = WS_O + (4u << 20);
constexpr size_t WS_Y = WS_X1 + (4u << 20);
constexpr size_t WS_HH = WS_Y + (4u << 20);
constexpr size_t WS_XCH = WS_HH + (16u << 20);
constexpr size_t WS_WQ = WS_XCH + (4u << 20);
constexpr size_t WS_WO = WS_WQ + (2u << 20);
constexpr size_t WS_WGU = WS_WO + (2u << 20);
constexpr size_t WS_WD = WS_WGU + (16u << 20);

__device__ __forceinline__ float bf2f(u16 b) { return __uint_as_float(((unsigned)b) << 16); }
__device__ __forceinline__ float bflo(unsigned w) { return __uint_as_float(w << 16); }
__device__ __forceinline__ float bfhi(unsigned w) { return __uint_as_float(w & 0xffff0000u); }
__device__ __forceinline__ unsigned cvtpk(float lo, float hi) { return __builtin_bit_cast(unsigned, __builtin_convertvector((f32x2){lo, hi}, bf16x2v)); }
__device__ __forceinline__ float rbf(float x) { return bflo(cvtpk(x, x)); }
__device__ __forceinline__ f32x4 rbf4(f32x4 s) { return (f32x4){rbf(s[0]), rbf(s[1]), rbf(s[2]), rbf(s[3])}; }

__device__ __forceinline__ void dma16(const void* gbase, unsigned voff, unsigned ldsaddr) {
    asm volatile("s_mov_b32 m0, %0\n\ts_nop 3\n\tglobal_load_lds_dwordx4 %1, %2" ::"s"(ldsaddr), "v"(voff), "s"(gbase) : "memory");
}
__device__ __forceinline__ void dmac(const void* gbase, unsigned voff, unsigned ldsaddr) {
    asm volatile("s_mov_b32 m0, %0\n\ts_nop 3\n\tglobal_load_lds_dwordx4 %1, %2 sc1" ::"s"(ldsaddr), "v"(voff), "s"(gbase) : "memory");
}
#define LD16(dst, base, voff) asm volatile("s_nop 4\n\tglobal_load_dwordx4 %0, %1, %2" : "=v"(dst) : "v"(voff), "s"(base) : "memory")
#define LD16C(dst, base, voff) asm volatile("s_nop 4\n\tglobal_load_dwordx4 %0, %1, %2 sc1" : "=v"(dst) : "v"(voff), "s"(base) : "memory")
#define LD8C(dst, base, voff) asm volatile("s_nop 4\n\tglobal_load_dwordx2 %0, %1, %2 sc1" : "=v"(dst) : "v"(voff), "s"(base) : "memory")
#define TOUCH(reg, base, voff) asm volatile("s_nop 4\n\tglobal_load_dword %0, %1, %2 sc1" : "+v"(reg) : "v"(voff), "s"(base) : "memory")
template <class T> __device__ __forceinline__ void st16(const void* base, unsigned voff, T v) {
    asm volatile("s_nop 4\n\tglobal_store_dwordx4 %0, %1, %2 sc1\n\ts_nop 1" ::"v"(voff), "v"(v), "s"(base) : "memory");
}
template <class T> __device__ __forceinline__ void st8(const void* base, unsigned voff, T v) { asm volatile("s_nop 4\n\tglobal_store_dwordx2 %0, %1, %2 sc1" ::"v"(voff), "v"(v), "s"(base) : "memory"); }
__device__ __forceinline__ void st4(const void* base, unsigned voff, float v) { asm volatile("s_nop 4\n\tglobal_store_dword %0, %1, %2 sc1" ::"v"(voff), "v"(v), "s"(base) : "memory"); }
#define WAIT_V(n) asm volatile("s_waitcnt vmcnt(" #n ")" ::: "memory")
#define BARRIER() asm volatile("s_barrier" ::: "memory")
#define BAR_L() asm volatile("s_waitcnt lgkmcnt(0)\n\ts_barrier" ::: "memory")
#define WAITV_BAR(n) asm volatile("s_waitcnt vmcnt(" #n ")\n\ts_barrier" ::: "memory")
#define WAITVL_BAR(n) asm volatile("s_waitcnt vmcnt(" #n ") lgkmcnt(0)\n\ts_barrier" ::: "memory")
#define PIN(x) asm volatile("" : "+v"(x))
#define SBAR() __builtin_amdgcn_sched_barrier(0)
#define MFMA32(a, b, c) __builtin_amdgcn_mfma_f32_32x32x16_bf16(a, b, c, 0, 0, 0)
#define MFMA16(a, b, c) __builtin_amdgcn_mfma_f32_16x16x32_bf16(a, b, c, 0, 0, 0)
#define MFMA16_ACC(c, a, b) asm volatile("v_mfma_f32_16x16x32_bf16 %0, %1, %2, %0" : "+v"(c) : "v"(a), "v"(b))
#define ZERO4 ((f32x4){0.f, 0.f, 0.f, 0.f})
__device__ __forceinline__ bf16x8 ldsr128(lds_ptr p) { return *(const LAS bf16x8*)p; }
__device__ __forceinline__ s16x4 ldstr(lds_ptr p) { return __builtin_amdgcn_ds_read_tr16_b64_v4i16((LAS s16x4*)p); }
__device__ __forceinline__ bf16x8 ldstr2(lds_ptr p, int d) { s16x4 a = ldstr(p), b = ldstr(p + d); return (bf16x8){a[0], a[1], a[2], a[3], b[0], b[1], b[2], b[3]}; }
__device__ __forceinline__ unsigned uni(unsigned x) { return (unsigned)__builtin_amdgcn_readfirstlane((int)x); }
__device__ __forceinline__ int lane_id() { int l = __builtin_amdgcn_mbcnt_hi(~0u, __builtin_amdgcn_mbcnt_lo(~0u, 0u)); PIN(l); return l; }
__device__ __forceinline__ u32x4 swap16(unsigned a0, unsigned a1, unsigned b0, unsigned b1) {
    const auto r0 = __builtin_amdgcn_permlane16_swap(a0, b0, false, false), r1 = __builtin_amdgcn_permlane16_swap(a1, b1, false, false);
    return (u32x4){r0[0], r1[0], r0[1], r1[1]};
}

#define STAMP_DECL do {} while (0)
#define STAMP(i) do {} while (0)
#define STAMP_FLUSH(kid) do {} while (0)
#define PSTAMP_FLUSH(kid) do {} while (0)

template <int CTRL> __device__ __forceinline__ float dppf(float v) { return __builtin_bit_cast(float, __builtin_amdgcn_mov_dpp(__builtin_bit_cast(int, v), CTRL, 0xf, 0xf, true)); }
__device__ __forceinline__ float half_sum(float v) { const auto r = __builtin_amdgcn_permlane32_swap(__float_as_uint(v), __float_as_uint(v), false, false); return __uint_as_float(r[0]) + __uint_as_float(r[1]); }
__device__ __forceinline__ float rows_sum(float v) {
    const auto r = __builtin_amdgcn_permlane16_swap(__float_as_uint(v), __float_as_uint(v), false, false); return half_sum(__uint_as_float(r[0]) + __uint_as_float(r[1]));
}
__device__ __forceinline__ float rows_max(float v) {
    auto r = __builtin_amdgcn_permlane16_swap(__float_as_uint(v), __float_as_uint(v), false, false); v = fmaxf(__uint_as_float(r[0]), __uint_as_float(r[1]));
    r = __builtin_amdgcn_permlane32_swap(__float_as_uint(v), __float_as_uint(v), false, false); return fmaxf(__uint_as_float(r[0]), __uint_as_float(r[1]));
}
__device__ __forceinline__ float row16_sum(float v) { v += dppf<0xB1>(v); v += dppf<0x4E>(v); v += dppf<0x141>(v); v += dppf<0x140>(v); return v; }
__device__ __forceinline__ float row16_max(float v) { v = fmaxf(v, dppf<0xB1>(v)); v = fmaxf(v, dppf<0x4E>(v)); v = fmaxf(v, dppf<0x141>(v)); v = fmaxf(v, dppf<0x140>(v)); return v; }
__device__ __forceinline__ float wsum(float v) { return rows_sum(row16_sum(v)); }
__device__ __forceinline__ float wmax(float v) { return rows_max(row16_max(v)); }
__device__ __forceinline__ f32x4 sum8(f32x4 (&p)[8]) {
    asm volatile("" : "+v"(p[0]), "+v"(p[1]), "+v"(p[2]), "+v"(p[3]), "+v"(p[4]), "+v"(p[5]), "+v"(p[6]), "+v"(p[7]));
    return ((p[0] + p[1]) + (p[2] + p[3])) + ((p[4] + p[5]) + (p[6] + p[7]));
}

__device__ __forceinline__ void signal(char* ws, int idx, int lane) {
    if (MEGA) { if (lane == 0) __hip_atomic_fetch_add((GAS unsigned*)(ws + WS_CNT) + idx * 64, 1u, __ATOMIC_RELAXED, __HIP_MEMORY_SCOPE_AGENT); }
}
template <int SLEEP>
__device__ __forceinline__ void await(char* ws, int idx, int n, unsigned need, int lane) {
    if (MEGA) {
        GAS unsigned* p = (GAS unsigned*)(ws + WS_CNT) + (idx + (lane < n ? lane : 0)) * 64;
        for (unsigned spins = 0; spins < (1u << 15); ++spins) {
            if (__all(__hip_atomic_load(p, __ATOMIC_RELAXED, __HIP_MEMORY_SCOPE_AGENT) >= need)) break;
            __builtin_amdgcn_s_sleep(SLEEP);
        }
        asm volatile("s_waitcnt vmcnt(0)" ::: "memory");
    }
}
__device__ __forceinline__ void await_input(char* ws, int pw, int idx, unsigned need, int lane) { if (MEGA) { if (pw == 0) await<X_SLEEP>(ws, idx, 1, need, lane); BARRIER(); } }
#define INPUT_BARRIER() do { if (MEGA) BARRIER(); } while (0)

__device__ __forceinline__ void pack_load(const char* src  , unsigned pitch, unsigned ldsaddr, int lane) {
    const unsigned vo = (unsigned)(lane >> 3) * pitch, sw = (lane & 7) ^ (((lane >> 4) & 1) << 1);
#pragma unroll
    for (int i = 0; i < 4; ++i) dma16(src + (size_t)i * 8 * pitch, vo + ((sw ^ ((i & 1) << 2)) << 4), ldsaddr + i * 1024);
}
__device__ __forceinline__ bf16x8 pack_frag(lds_ptr area, int nf, int lane) {
    const int g = lane >> 4, q = (lane & 15) >> 2;
    return ldstr2(area + (8 * g + q) * 128 + ((nf ^ (((q >> 1) & 1) | ((g & 1) << 1))) << 5) + (lane & 3) * 8, 512);
}
__device__ __forceinline__ void mod_unit(lds_ptr area, bf16x8 sc, lds_ptr red, int lane) {
    f32x4 d[4];
#pragma unroll
    for (int nf = 0; nf < 4; ++nf) d[nf] = MFMA16(pack_frag(area, nf, lane), sc, ZERO4);
    if ((lane & 15) == 0) {
#pragma unroll
        for (int nf = 0; nf < 4; ++nf) *(LAS f32x4*)(red + nf * 64 + (lane >> 4) * 16) = d[nf];
    }
}

__device__ __forceinline__ float silu_bf(float cv) { return rbf(cv / (1.f + __expf(-cv))); }
__device__ __forceinline__ bf16x8 silu_frag(u32x4 cw) {
    PIN(cw);
    u32x4 r;
#pragma unroll
    for (int j = 0; j < 4; ++j) r[j] = cvtpk(silu_bf(bflo(cw[j])), silu_bf(bfhi(cw[j])));
    return __builtin_bit_cast(bf16x8, r);
}

__device__ __forceinline__ void tile128x64(int bid, int& rb, int& ct) { const int xcd = bid & 7, idx = bid >> 3; rb = (xcd >> 1) * 4 + (idx & 3); ct = (xcd & 1) * 8 + (idx >> 2); }
struct Ctx { char* ws; lds_ptr lds; unsigned lds0; int bid, w; };

__device__ __forceinline__ void ln1kv_consume(const Ctx& cx, const u16* __restrict__ x, const u16* __restrict__ c, const u16* __restrict__ wmod) {
    char* ws = cx.ws; const lds_ptr lds = cx.lds; const int wid = cx.w, lane = lane_id(), tid = wid * 64 + lane;
    STAMP_DECL; STAMP(0);
    const int rb = (cx.bid >> 4) * 8 + (cx.bid & 7), isv = (cx.bid >> 3) & 1;
    const int idx = cx.bid >> 3, cs = ((((idx & 3) << 3) | (cx.bid & 7)) << 1) | ((idx >> 2) & 1), kq = idx >> 3, kr = kq * 256 + (tid >> 2);
    const u32x4 w0 = *(const u32x4*)(wmod + (size_t)kr * 6144 + cs * 32 + (tid & 3) * 8), w1 = *(const u32x4*)(wmod + (size_t)(kr + 128) * 6144 + cs * 32 + (tid & 3) * 8);
    const u16 c0 = c[kr], c1 = c[kr + 128];
    unsigned long long tag = (__hip_atomic_load((GAS unsigned long long*)(ws + WS_EPOCH), __ATOMIC_RELAXED, __HIP_MEMORY_SCOPE_AGENT) + 1ull) << 24;
    u32x4 xv[2][2];
#pragma unroll
    for (int rr = 0; rr < 2; ++rr)
#pragma unroll
        for (int hh = 0; hh < 2; ++hh) xv[rr][hh] = *(const u32x4*)(x + (size_t)(rb * 16 + 2 * wid + rr) * D + hh * 512 + lane * 8);
    {
        const float s0 = silu_bf(bf2f(c0)), s1 = silu_bf(bf2f(c1));
        float a[8];
#pragma unroll
        for (int j = 0; j < 4; ++j) { a[2 * j] = s0 * bflo(w0[j]) + s1 * bflo(w1[j]); a[2 * j + 1] = s0 * bfhi(w0[j]) + s1 * bfhi(w1[j]); }
#pragma unroll
        for (int j = 0; j < 8; ++j) { a[j] += dppf<0x124>(a[j]); a[j] += dppf<0x128>(a[j]); a[j] = rows_sum(a[j]); }
        PIN(tag);
        if (lane < 4) { *(LAS f32x4*)(lds + 114688 + wid * 128 + lane * 32) = (f32x4){a[0], a[1], a[2], a[3]}; *(LAS f32x4*)(lds + 114688 + wid * 128 + lane * 32 + 16) = (f32x4){a[4], a[5], a[6], a[7]}; }
    }
    BAR_L();
    STAMP(6);
    if (wid == 0 && lane < 32) {
        const LAS float* r = (const LAS float*)(lds + 114688) + lane;
        const unsigned pv = __float_as_uint(((r[0] + r[32]) + (r[64] + r[96])) + ((r[128] + r[160]) + (r[192] + r[224])));
        __hip_atomic_store((GAS unsigned long long*)(ws + WS_MP1) + (kq * 2048 + ((cs * 32 + 1024) & 2047) + lane), tag | ((pv + 0x80u) >> 8), __ATOMIC_RELAXED, __HIP_MEMORY_SCOPE_AGENT);
    }
    STAMP(5);
    float xn[2][16];
#pragma unroll
    for (int rr = 0; rr < 2; ++rr) {
        float* v = xn[rr];
#pragma unroll
        for (int hh = 0; hh < 2; ++hh)
#pragma unroll
            for (int j = 0; j < 4; ++j) { v[hh * 8 + 2 * j] = bflo(xv[rr][hh][j]); v[hh * 8 + 2 * j + 1] = bfhi(xv[rr][hh][j]); }
        float sm = 0.f;
#pragma unroll
        for (int j = 0; j < 16; ++j) sm += v[j];
        const float mean = wsum(sm) * (1.f / 1024.f);
        float q = 0.f;
#pragma unroll
        for (int j = 0; j < 16; ++j) { v[j] -= mean; q += v[j] * v[j]; }
        const float rstd = 1.f / sqrtf(wsum(q) * (1.f / 1024.f) + 1e-5f);
#pragma unroll
        for (int j = 0; j < 16; ++j) { v[j] *= rstd; PIN(v[j]); }
    }
    LAS float* tab = (LAS float*)(lds + 98304);
    {
        u32x4 g[4][2];
        const unsigned tl = (unsigned)tag, th = (unsigned)(tag >> 32);
        __builtin_amdgcn_s_sleep(X_S1);
        for (unsigned spins = 0; spins < (1u << 15); ++spins) {
            asm volatile("s_nop 4\n\tglobal_load_dwordx4 %0, %8, %12 sc1\n\tglobal_load_dwordx4 %1, %8, %13 sc1\n\tglobal_load_dwordx4 %2, %9, %12 sc1\n\tglobal_load_dwordx4 %3, %9, %13 sc1\n\t"
                         "global_load_dwordx4 %4, %10, %12 sc1\n\tglobal_load_dwordx4 %5, %10, %13 sc1\n\tglobal_load_dwordx4 %6, %11, %12 sc1\n\tglobal_load_dwordx4 %7, %11, %13 sc1\n\ts_waitcnt vmcnt(0)"
                         : "=&v"(g[0][0]), "=&v"(g[0][1]), "=&v"(g[1][0]), "=&v"(g[1][1]), "=&v"(g[2][0]), "=&v"(g[2][1]), "=&v"(g[3][0]), "=&v"(g[3][1])
                         : "v"(tid * 16), "v"(tid * 16 + 16384), "v"(tid * 16 + 32768), "v"(tid * 16 + 49152), "s"(ws + WS_MP1), "s"(ws + WS_MP1 + 8192) : "memory");
            unsigned bad = 0u;
#pragma unroll
            for (int k = 0; k < 4; ++k)
                bad |= ((g[k][0][0] ^ tl) | (g[k][0][2] ^ tl) | (g[k][1][0] ^ tl) | (g[k][1][2] ^ tl)) >> 24 | (g[k][0][1] ^ th) | (g[k][0][3] ^ th) | (g[k][1][1] ^ th) | (g[k][1][3] ^ th);
            if (__all(bad == 0u)) break;
            __builtin_amdgcn_s_sleep(X_S2);
        }
        f32x4 v;
#pragma unroll
        for (int j = 0; j < 4; ++j)
            v[j] = (__uint_as_float(g[0][j >> 1][(j & 1) * 2] << 8) + __uint_as_float(g[1][j >> 1][(j & 1) * 2] << 8)) + (__uint_as_float(g[2][j >> 1][(j & 1) * 2] << 8) + __uint_as_float(g[3][j >> 1][(j & 1) * 2] << 8));
        v = rbf4(v);
        *(LAS f32x2*)(tab + tid * 2) = (f32x2){rbf(1.f + v[0]), rbf(1.f + v[1])};
        *(LAS f32x2*)(tab + 1024 + tid * 2) = (f32x2){v[2], v[3]};
    }
    BAR_L();
    if (cx.bid == 0 && tid == 0) __hip_atomic_store((GAS unsigned long long*)(ws + WS_EPOCH), tag >> 24, __ATOMIC_RELAXED, __HIP_MEMORY_SCOPE_AGENT);
    STAMP(1);
    const lds_ptr img = lds + 65536;
#pragma unroll
    for (int hh = 0; hh < 2; ++hh) {
        const int c = hh * 64 + lane;
        const f32x4 g0 = *(const LAS f32x4*)(tab + c * 8), g1 = *(const LAS f32x4*)(tab + c * 8 + 4);
        const f32x4 b0 = *(const LAS f32x4*)(tab + 1024 + c * 8), b1 = *(const LAS f32x4*)(tab + 1024 + c * 8 + 4);
#pragma unroll
        for (int rr = 0; rr < 2; ++rr) {
            float h[8];
#pragma unroll
            for (int j = 0; j < 4; ++j) { h[j] = xn[rr][hh * 8 + j] * g0[j] + b0[j]; h[4 + j] = xn[rr][hh * 8 + 4 + j] * g1[j] + b1[j]; }
            *(LAS u32x4*)(img + c * 256 + (((2 * wid + rr) ^ (c & 15)) << 4)) = (u32x4){cvtpk(h[0], h[1]), cvtpk(h[2], h[3]), cvtpk(h[4], h[5]), cvtpk(h[6], h[7])};
        }
    }
    BAR_L();
    STAMP(2);
    f32x4 acc[2] = {ZERO4, ZERO4};
    const int g = lane >> 4, q = (lane & 15) >> 2, p = lane & 3;
    bf16x8 hf[4], wf[4][2];
#pragma unroll
    for (int i = 0; i < 4; ++i) {
        const int s = wid * 4 + i, cc = 4 * s + g;
        hf[i] = ldsr128(img + cc * 256 + (((lane & 15) ^ (cc & 15)) << 4));
#pragma unroll
        for (int nf = 0; nf < 2; ++nf) wf[i][nf] = ldstr2(lds + (32 * s + 8 * g + q) * 64 + ((nf ^ (g & 1)) << 5) + 8 * p, 256);
    }
    LAS float* red = (LAS float*)(lds + 147456);
    const char* hb = ws + WS_H1 + ((size_t)rb * 128 + 16 * wid) * 256;
    if (isv) {
#pragma unroll
        for (int i = 0; i < 4; ++i) { acc[0] = MFMA16(hf[i], wf[i][0], acc[0]); acc[1] = MFMA16(hf[i], wf[i][1], acc[1]); }
        st16(hb, lane * 16 + 1024, hf[1]); st16(hb, lane * 16 + 3072, hf[3]);
#pragma unroll
        for (int nf = 0; nf < 2; ++nf) *(LAS f32x4*)(red + wid * 512 + (nf * 16 + (lane & 15)) * 16 + 4 * g) = acc[nf];
    } else {
#pragma unroll
        for (int i = 0; i < 4; ++i) { acc[0] = MFMA16(wf[i][0], hf[i], acc[0]); acc[1] = MFMA16(wf[i][1], hf[i], acc[1]); }
        st16(hb, lane * 16, hf[0]); st16(hb, lane * 16 + 2048, hf[2]);
#pragma unroll
        for (int nf = 0; nf < 2; ++nf) *(LAS f32x4*)(red + wid * 512 + (lane & 15) * 32 + nf * 16 + 4 * g) = acc[nf];
    }
    WAITVL_BAR(0);
    STAMP(3);
    if (wid == 0) {
        const int o0 = isv ? (lane & 31) * 16 + 4 * (lane >> 5) : (lane & 15) * 32 + g * 8, o1 = o0 + (isv ? 8 : 4);
        f32x4 a = ZERO4, b = ZERO4;
#pragma unroll
        for (int w = 0; w < 8; ++w) { a += *(const LAS f32x4*)(red + w * 512 + o0); b += *(const LAS f32x4*)(red + w * 512 + o1); }
        const u32x4 v = {cvtpk(a[0], a[1]), cvtpk(a[2], a[3]), cvtpk(b[0], b[1]), cvtpk(b[2], b[3])};
        st16(ws + WS_KV + (size_t)(rb >> 1) * 4096, isv ? 2048 + (rb & 1) * 1024 + lane * 16 : (g >> 1) * 1024 + ((g & 1) * 32 + (rb & 1) * 16 + (lane & 15)) * 16, v);
        float n2 = 0.f, mx = 0.f;
#pragma unroll
        for (int j = 0; j < 4; ++j) { n2 += bflo(v[j]) * bflo(v[j]) + bfhi(v[j]) * bfhi(v[j]); mx = fmaxf(mx, fmaxf(fabsf(bflo(v[j])), fabsf(bfhi(v[j])))); }
        if (!isv) mx = rows_sum(n2);
        if (!(n2 - n2 == 0.f)) mx = INFINITY;
        mx = wmax(mx);
        if (lane == 0) st4(ws + WS_KN, (unsigned)(isv * 128 + rb) * 4u, mx);
    }
    WAITV_BAR(0);
    STAMP(4); STAMP_FLUSH(1);
}
template <class Next>
__device__ __forceinline__ void ln1kv_produce(const Ctx& cx, const u16* __restrict__ wq, const u16* __restrict__ wk, const u16* __restrict__ wv, const u16* __restrict__ c, const u16* __restrict__ wmod, Next next_pre) {
    char* ws = cx.ws; const int pw = cx.w, lane = lane_id();
    int rbq, hp; tile128x64(cx.bid, rbq, hp);
    if (cx.bid == 0 && pw >= 2) { if ((pw - 2) * 64 + lane < C_NUM) st4(ws + WS_CNT, (unsigned)((pw - 2) * 64 + lane) * 256u, 0.f); WAIT_V(0); }
    if (pw < 2) { __builtin_amdgcn_s_sleep(8); pack_load((const char*)wq + (size_t)(rbq * 2 + pw) * 65536 + hp * 128, 2048, cx.lds0 + 106496 + pw * 4096, lane); }
    BARRIER();
    {
        const char* w = (const char*)(((cx.bid >> 3) & 1) ? wv : wk) + pw * 16384;
        const unsigned voff = (lane >> 2) * 64 + (((lane & 3) ^ (((lane >> 5) & 1) << 1)) << 4);
#pragma unroll
        for (int i = 0; i < 16; ++i) dma16(w + i * 1024, voff, cx.lds0 + pw * 16384 + i * 1024);
    }
    const int k0 = (cx.bid >> 5) * 128 + pw * 32, col2 = 1024 + (cx.bid & 31) * 64;
    u32x4 cw;
    pack_load((const char*)wmod + (size_t)k0 * 12288 + (2048 + col2) * 2, 12288, cx.lds0 + 126976 + pw * 4096, lane);
    LD16(cw, c, (unsigned)(k0 + (lane >> 4) * 8) * 2u);
    if (pw < 2) {
        WAIT_V(21);
#pragma unroll
        for (int nf = 0; nf < 4; ++nf) st16(ws + WS_WQ + (size_t)(hp * 32 + rbq * 2 + pw) * 4096, lane * 16 + nf * 1024, pack_frag(cx.lds + 106496 + pw * 4096, nf, lane));
    }
    WAIT_V(0);
    mod_unit(cx.lds + 126976 + pw * 4096, silu_frag(cw), cx.lds + 143360 + pw * 256, lane);
    BAR_L();
    if (pw == 0) signal(ws, C_WQ + hp, lane);
    BARRIER();
    BARRIER();
    if (pw == 0) {
        signal(ws, C_H1 + (cx.bid >> 4), lane);
        const LAS float* r = (const LAS float*)(cx.lds + 143360) + lane;
        st4(ws + WS_MP2, (unsigned)((cx.bid >> 5) * 4096 + col2 + lane) * 4u, (r[0] + r[64]) + (r[128] + r[192]));
    }
    await<4>(ws, C_WQ + hp, 1, 16, lane);
    next_pre();
}

__device__ __forceinline__ void g64_pre(const Ctx& cx, const char* Wp, int lane) {
#pragma unroll
    for (int t = 0; t < 5; ++t) { dmac(Wp + cx.w * 2048 + t * 8192, lane * 16, cx.lds0 + 16384 + cx.w * 2048 + t * 24576); dmac(Wp + cx.w * 2048 + t * 8192 + 1024, lane * 16, cx.lds0 + 16384 + cx.w * 2048 + t * 24576 + 1024); }
}
template <int NTAIL, class Issued, class First, class Tail>
__device__ __forceinline__ void g64_produce(const Ctx& cx, const char* Ap, const char* Wp, int lane, Issued issued, First first, Tail tail) {
    const char* Aw = Ap + (size_t)(2 * cx.w) * 32768; const char* Ww = Wp + cx.w * 2048;
    const unsigned voff = lane * 16, dA = cx.lds0 + cx.w * 4096, dW = cx.lds0 + 16384 + cx.w * 2048;
#define G64_A(t, so) do { const char* a_ = Aw + (size_t)(t) * 2048; dmac(a_, voff, dA + (so)); dmac(a_ + 32768, voff, dA + (so) + 2048); dmac(a_ + 1024, voff, dA + (so) + 1024); dmac(a_ + 33792, voff, dA + (so) + 3072); } while (0)
#define G64_STAGE(t, so) do { const char* w_ = Ww + (size_t)(t) * 8192; dmac(w_, voff, dW + (so)); dmac(w_ + 1024, voff, dW + (so) + 1024); G64_A(t, so); } while (0)
#pragma unroll
    for (int t = 0; t < 5; ++t) G64_A(t, t * 24576);
    issued();
    WAITV_BAR(16);
    first();
    WAITV_BAR(12); G64_STAGE(5, 5 * 24576);
    WAITV_BAR(14); G64_STAGE(6, 0);
    WAITV_BAR(16); G64_STAGE(7, 24576);
    int sf = 2 * 24576;
#pragma unroll 1
    for (int t = 3; t < 11; ++t) {
        WAITV_BAR(18);
        G64_STAGE(t + 5, sf);
        sf = sf == 5 * 24576 ? 0 : sf + 24576;
    }
    WAITV_BAR(18);
    WAITV_BAR(12);
    tail();
    asm volatile("s_waitcnt vmcnt(%0)\n\ts_barrier" ::"i"(6 + NTAIL) : "memory");
    asm volatile("s_waitcnt vmcnt(%0) lgkmcnt(0)\n\ts_barrier" ::"i"(NTAIL) : "memory");
#undef G64_STAGE
#undef G64_A
}
template <bool INBAR, class Mid, class Late, class St>
__device__ __forceinline__ void g64_consume(f32x4 (&acc)[2][2], const Ctx& cx, size_t woff, unsigned& tch, int lane, Mid mid, Late late, St stamp) {
    const lds_ptr pa = cx.lds + (cx.w & 3) * 4096 + lane * 16, pw = cx.lds + 16384 + (cx.w >> 2) * 2048 + lane * 16;
#pragma unroll
    for (int m = 0; m < 2; ++m) { acc[m][0] = ZERO4; acc[m][1] = ZERO4; }
    bf16x8 aA[4], wA[4], aB[4], wB[4];
    if (MEGA) TOUCH(tch, cx.ws, (unsigned)woff + ((cx.bid >> 3) & 3) * 32768u + ((cx.w & 3) * 64 + lane) * 128u);
    if (INBAR) INPUT_BARRIER();
    BARRIER(); SBAR();
    stamp();
#pragma unroll
    for (int ks = 0; ks < 2; ++ks) { wA[2 * ks] = ldsr128(pw + ks * 4096); aA[2 * ks] = ldsr128(pa + ks * 1024); wA[2 * ks + 1] = ldsr128(pw + ks * 4096 + 1024); aA[2 * ks + 1] = ldsr128(pa + 2048 + ks * 1024); }
    SBAR();
    int sn = 24576;
#define G64_STEP(AC, WC, AN, WN, DO_NEXT, HOOK) do { \
        if (DO_NEXT) BARRIER(); SBAR(); HOOK; \
        _Pragma("unroll") for (int i = 0; i < 4; ++i) { const int ks = i >> 1, m = i & 1; \
            if (DO_NEXT) { WN[i] = ldsr128(pw + sn + ks * 4096 + m * 1024); AN[i] = ldsr128(pa + sn + m * 2048 + ks * 1024); } \
            acc[m][0] = MFMA16(WC[2 * ks], AC[i], acc[m][0]); acc[m][1] = MFMA16(WC[2 * ks + 1], AC[i], acc[m][1]); PIN(acc[m][0]); PIN(acc[m][1]); \
            SBAR(); } \
        sn = sn == 5 * 24576 ? 0 : sn + 24576; } while (0)
#define G64_PAIR() do { G64_STEP(aA, wA, aB, wB, true, (void)0); G64_STEP(aB, wB, aA, wA, true, (void)0); } while (0)
#pragma unroll 1
    for (int t = 0; t < 6; t += 2) G64_PAIR();
    mid(6);
#pragma unroll 1
    for (int t = 6; t < 10; t += 2) G64_PAIR();
    mid(10);
    G64_PAIR();
    mid(12);
    G64_PAIR();
    G64_STEP(aA, wA, aB, wB, true, late());
    G64_STEP(aB, wB, aA, wA, false, (void)0);
#undef G64_PAIR
#undef G64_STEP
}

__device__ __forceinline__ void attn_pre(const Ctx& cx) { int rb, hp; tile128x64(cx.bid, rb, hp); g64_pre(cx, cx.ws + WS_WQ + (size_t)hp * 131072, lane_id()); }
template <class Next>
__device__ __forceinline__ void attn_produce(const Ctx& cx, const u16* __restrict__ wo, const u16* __restrict__ wg, const u16* __restrict__ wu, const u16* __restrict__ wd, const u16* __restrict__ c, const u16* __restrict__ wmod, Next next_pre) {
    char* ws = cx.ws; const int pw = cx.w, lane = lane_id();
    int rb, hp; tile128x64(cx.bid, rb, hp);
    const char* kvg = ws + WS_KV + pw * 8192;
    const unsigned voff = lane * 16, kvd = cx.lds0 + pw * 8192;
#define KV_STAGE(st, so) do { _Pragma("unroll") for (int i_ = 0; i_ < 8; ++i_) dmac(kvg + (st) * 32768 + i_ * 1024, voff, kvd + (so) + i_ * 1024); } while (0)
    STAMP_DECL; STAMP(0);
    await<2>(ws, C_H1 + rb, 1, 16, lane);
    STAMP(1);
    if (pw == 0) signal(ws, C_SIDE + (cx.bid & 15), lane);
    g64_produce<16>(cx, ws + WS_H1 + (size_t)rb * 8 * 32768, ws + WS_WQ + (size_t)hp * 131072, lane,
                    [&]() { BARRIER(); if (pw == 0) signal(ws, C_KV + (cx.bid & 15), lane); },
                    [&]() { STAMP(2); },
                    [&]() { KV_STAGE(0, 98304); KV_STAGE(1, 131072); });
    WAITV_BAR(8);
    const lds_ptr area = cx.lds + pw * 16384; const unsigned area0 = cx.lds0 + pw * 16384;
    const int k0 = (cx.bid >> 5) * 128 + pw * 32, col2 = (cx.bid & 15) * 64 + ((cx.bid >> 4) & 1) * 3072;
    const int gw = cx.bid * 4 + pw, isgu = gw < 896, mat = gw >= 448, k8 = (gw - mat * 448) / 56, cg = gw - mat * 448 - k8 * 56;
    u32x4 cw; f32x4 vp[8];
    pack_load((const char*)wmod + (size_t)k0 * 12288 + (2048 + col2) * 2, 12288, area0, lane);
    LD16(cw, c, (unsigned)(k0 + (lane >> 4) * 8) * 2u);
    auto unit_load = [&](int j) {
        const unsigned dst = area0 + 8192 + (j & 1) * 4096;
        if (!isgu) { const int u = (gw - 896) * 4 + (j & 3); pack_load((const char*)wo + (size_t)(u >> 4) * 65536 + (u & 15) * 128, 2048, dst, lane); }
        else if (j < 4) pack_load((const char*)(mat ? wu : wg) + (size_t)(k8 * 4 + j) * 32 * 7168 + cg * 128, 7168, dst, lane);
        else { const int u = (j - 4) * 896 + gw; pack_load((const char*)wd + (size_t)(u >> 4) * 65536 + (u & 15) * 128, 2048, dst, lane); }
    };
    f32x4 ut[4] = {ZERO4, ZERO4, ZERO4, ZERO4};
    unsigned guo[4];
#pragma unroll
    for (int nf = 0; nf < 4; ++nf) {
        const int cj = cg * 8 + 2 * nf + ((lane >> 3) & 1), ct = cj / 14, r = cj - ct * 14, wn = r >= 7, c = r - wn * 7;
        const int fr = c < 6 ? mat * 3 + (c >> 1) : 6, hf = c < 6 ? (c & 1) : mat;
        guo[nf] = (unsigned)((ct * 32 * 14 + wn * 7 + fr) * 1024 + (lane >> 4) * 256 + (hf * 8 + (lane & 7)) * 16);
        PIN(guo[nf]);
    }
    auto unit_store = [&](int j) {
        const lds_ptr src = area + 8192 + (j & 1) * 4096;
        bf16x8 f[4];
#pragma unroll
        for (int nf = 0; nf < 4; ++nf) f[nf] = pack_frag(src, nf, lane);
        if (!isgu || j >= 4) {
            const int u = isgu ? (j - 4) * 896 + gw : (gw - 896) * 4 + (j & 3);
            const char* d = isgu ? ws + WS_WD + (size_t)((((u & 15) >> 1) * 112 + (u >> 4)) * 2 + (u & 1)) * 4096 : ws + WS_WO + (size_t)((u & 15) * 32 + (u >> 4)) * 4096;
#pragma unroll
            for (int nf = 0; nf < 4; ++nf) st16(d, voff + nf * 1024, f[nf]);
        } else {
            bf16x8 vf = ldsr128(area + 4096 + (lane & 1) * 256 + j * 64 + (lane >> 4) * 16);
#pragma unroll
            for (int nf = 0; nf < 4; ++nf) {
                st16(ws + WS_WGU + (size_t)(k8 * 4 + j) * 14336, guo[nf], f[nf]);
                ut[nf] = MFMA16(f[nf], vf, ut[nf]);
            }
        }
    };
    {
        const unsigned vo = (unsigned)((lane < 32 ? 2048 : 1024) + (k8 & 7) * 128 + (lane & 31) * 4) * 4u;
#pragma unroll
        for (int i = 0; i < 8; ++i) LD16C(vp[i], ws + WS_MP2, vo + i * 16384);
    }
#define DBA(i) do {} while (0)
#define DBB(i) do {} while (0)
    unit_load(0);
    STAMP(3);
    WAITV_BAR(17);
    KV_STAGE(2, 65536);
    WAIT_V(8);
    mod_unit(area, silu_frag(cw), area + 5120, lane);
    {
        f32x4 v = rbf4(sum8(vp));
        if (lane < 32) v = rbf4(v + 1.f);
        *(LAS u32x2*)(area + 4096 + lane * 8) = (u32x2){cvtpk(v[0], v[1]), cvtpk(v[2], v[3])};
    }
    unit_store(0); unit_load(1);
    STAMP(4);
    WAITVL_BAR(8);
    KV_STAGE(3, 98304);
    if (pw == 0) {
        const LAS float* r = (const LAS float*)(cx.lds + 5120) + lane;
        st4(ws + WS_MP2, (unsigned)((cx.bid >> 5) * 4096 + col2 + lane) * 4u, (r[0] + r[4096]) + (r[8192] + r[12288]));
    }
    WAIT_V(8); unit_store(1); unit_load(2);
    STAMP(5);
    DBA(0); WAITVL_BAR(8); DBB(0); KV_STAGE(4, 131072); WAIT_V(8); unit_store(2); unit_load(3);
    DBA(1); WAITVL_BAR(8); DBB(1); KV_STAGE(5, 65536); WAIT_V(8); unit_store(3); unit_load(4);
    if (isgu && (lane & 15) < 2) {
#pragma unroll
        for (int nf = 0; nf < 4; ++nf) st16(ws + WS_UT, (unsigned)((k8 * 4 + mat * 2 + (lane & 15)) * F + cg * 64 + nf * 16 + (lane >> 4) * 4) * 4u, ut[nf]);
    }
    DBA(2); WAITVL_BAR(8); DBB(2); KV_STAGE(6, 98304); WAIT_V(8); unit_store(4); unit_load(5);
    DBA(3); WAITVL_BAR(8); DBB(3); KV_STAGE(7, 131072); WAIT_V(8); unit_store(5);
    {
        const u32x4 e = {~0u, ~0u, ~0u, ~0u};
#pragma unroll
        for (int i = 0; i < 4; ++i) st16(ws + WS_XCH + (size_t)gw * 4096, voff + i * 1024, e);
    }
    WAITVL_BAR(0);
    if (pw == 0) signal(ws, C_SIDE + (cx.bid & 15), lane);
#undef KV_STAGE
    if (MEGA && pw == 0) { __builtin_amdgcn_s_sleep(40); await<20>(ws, C_SIDE, 16, 32, lane); }
    BARRIER();
    STAMP(6);
    next_pre();
    STAMP(7);
    BARRIER();
    if (pw == 0) signal(ws, C_O + rb, lane);
    PSTAMP_FLUSH(10);
}
__device__ __forceinline__ void attn_consume(const Ctx& cx) {
    char* ws = cx.ws; const lds_ptr lds = cx.lds; const int wid = cx.w, lane = lane_id();
    STAMP_DECL; STAMP(0);
    f32x4 qa[2][2]; f32x2 kn2, vm2; float knm, vmm;
    unsigned cv;
    int rb, hp; tile128x64(cx.bid, rb, hp);
    unsigned tch = 0u;
    g64_consume<false>(qa, cx, WS_WQ + (size_t)hp * 131072, tch, lane, [&](int t) {
            if (MEGA) {
                if (t == 6) asm volatile("s_nop 4\n\tglobal_load_dword %0, %1, %2 sc1" : "=v"(cv) : "v"(wid == 0 ? (lane & 31) * 256 : 0), "s"(ws + WS_CNT) : "memory");
                if (t == 10) { asm volatile("s_waitcnt vmcnt(0)" : "+v"(cv) :: "memory"); if (wid == 0 && !__all(cv >= 16u)) await<6>(ws, C_KV, 32, 16, lane); }
            }
            if (t == 12) asm volatile("s_nop 4\n\tglobal_load_dwordx2 %0, %2, %3 sc1\n\tglobal_load_dwordx2 %1, %2, %3 offset:512 sc1" : "=&v"(kn2), "=&v"(vm2) : "v"(lane * 8), "s"(ws + WS_KN) : "memory"); },
        [&]() { asm volatile("s_waitcnt vmcnt(0)" : "+v"(kn2), "+v"(vm2), "+v"(tch) :: "memory"); knm = wmax(fmaxf(kn2[0], kn2[1])); vmm = wmax(fmaxf(vm2[0], vm2[1])); PIN(knm); PIN(vmm); SBAR(); },
        [&]() { STAMP(5); });
    BAR_L();
    STAMP(2);
    const lds_ptr pl = lds + lane * 16;
    bf16x8 k0 = ldsr128(pl + 98304), k1 = ldsr128(pl + 98304 + 1024), v0 = ldsr128(pl + 98304 + 2048);
    constexpr float QS = 0.17677669529663687f * 1.4426950408889634f;
    u32x4 qw0, qw1;
    qw0 = swap16(cvtpk(qa[0][0][0] * QS, qa[0][0][1] * QS), cvtpk(qa[0][0][2] * QS, qa[0][0][3] * QS), cvtpk(qa[1][0][0] * QS, qa[1][0][1] * QS), cvtpk(qa[1][0][2] * QS, qa[1][0][3] * QS));
    qw1 = swap16(cvtpk(qa[0][1][0] * QS, qa[0][1][1] * QS), cvtpk(qa[0][1][2] * QS, qa[0][1][3] * QS), cvtpk(qa[1][1][0] * QS, qa[1][1][1] * QS), cvtpk(qa[1][1][2] * QS, qa[1][1][3] * QS));
    const bf16x8 q0 = __builtin_bit_cast(bf16x8, qw0), q1 = __builtin_bit_cast(bf16x8, qw1);
    f32x16 s0 = MFMA32(k0, q0, ((f32x16){})); s0 = MFMA32(k1, q1, s0);
    PIN(s0);
    float qn = 0.f;
#pragma unroll
    for (int j = 0; j < 4; ++j) { qn += bflo(qw0[j]) * bflo(qw0[j]) + bfhi(qw0[j]) * bfhi(qw0[j]) + bflo(qw1[j]) * bflo(qw1[j]) + bfhi(qw1[j]) * bfhi(qw1[j]); }
    qn = half_sum(qn);
    const bool fast = __all((qn * knm <= 88.f * 88.f) && (vmm <= 16777216.f) && (vmm >= 9.5367431640625e-7f));
    f32x16 o = {};
    float linv;
#define EX(x) __builtin_amdgcn_exp2f(x)
#define BF8(x) __builtin_bit_cast(bf16x8, x)
    if (fast) {
        const short ov = ((lane & 15) == ((lane >> 4) & 1)) ? (short)0x3f80 : (short)0;
        const bf16x8 sel = {ov, ov, ov, ov, ov, ov, ov, ov};
        f32x4 ls = ZERO4;
        const f32x16 zero = {};
        f32x16 sA, sB;
        bf16x8 v1 = v0;
        u32x4 p0, p1 = {0u, 0u, 0u, 0u};
        sA = s0;
        k0 = ldsr128(pl + 98304 + 4096); k1 = ldsr128(pl + 98304 + 4096 + 1024);
#define ATT_TILE(SC, SN, PK, PV0, PV1) do { \
            SN = MFMA32(k0, q0, zero); PIN(SN); SBAR(); k0 = ldsr128(PK); \
            SC[0] = EX(SC[0]); SC[1] = EX(SC[1]); SC[2] = EX(SC[2]); SC[3] = EX(SC[3]); PIN(SC); SBAR(); \
            o = MFMA32(v1, BF8(p1), o); PIN(o); SBAR(); v1 = ldsr128(PV1); \
            SC[4] = EX(SC[4]); SC[5] = EX(SC[5]); SC[6] = EX(SC[6]); PIN(SC); SBAR(); \
            ls = MFMA16(sel, BF8(p1), ls); PIN(ls); SBAR(); SC[7] = EX(SC[7]); p0[0] = cvtpk(SC[0], SC[1]); p0[1] = cvtpk(SC[2], SC[3]); PIN(SC); PIN(p0); SBAR(); \
            SN = MFMA32(k1, q1, SN); PIN(SN); SBAR(); k1 = ldsr128((PK) + 1024); \
            p0[2] = cvtpk(SC[4], SC[5]); p0[3] = cvtpk(SC[6], SC[7]); PIN(p0); \
            SC[8] = EX(SC[8]); SC[9] = EX(SC[9]); SC[10] = EX(SC[10]); PIN(SC); SBAR(); \
            o = MFMA32(v0, BF8(p0), o); PIN(o); SBAR(); v0 = ldsr128(PV0); \
            SC[11] = EX(SC[11]); SC[12] = EX(SC[12]); SC[13] = EX(SC[13]); PIN(SC); SBAR(); \
            ls = MFMA16(sel, BF8(p0), ls); PIN(ls); SBAR(); \
            SC[14] = EX(SC[14]); SC[15] = EX(SC[15]); \
            p1[0] = cvtpk(SC[8], SC[9]); p1[1] = cvtpk(SC[10], SC[11]); p1[2] = cvtpk(SC[12], SC[13]); p1[3] = cvtpk(SC[14], SC[15]); PIN(p1); SBAR(); } while (0)
        int so = 98304;
        STAMP(1);
#pragma unroll 1
        for (int st = 0; st < 8; ++st) {
            const int sn = so == 131072 ? 65536 : so + 32768;
            const lds_ptr pc = pl + so, pn = pl + sn;
            __builtin_amdgcn_s_setprio(1);
            ATT_TILE(sA, sB, pc + 2 * 4096, pc + 1 * 4096 + 2048, pc + 0 * 4096 + 3072);
            ATT_TILE(sB, sA, pc + 3 * 4096, pc + 2 * 4096 + 2048, pc + 1 * 4096 + 3072);
            ATT_TILE(sA, sB, pc + 4 * 4096, pc + 3 * 4096 + 2048, pc + 2 * 4096 + 3072);
            ATT_TILE(sB, sA, pc + 5 * 4096, pc + 4 * 4096 + 2048, pc + 3 * 4096 + 3072);
            __builtin_amdgcn_s_setprio(0);
            ATT_TILE(sA, sB, pc + 6 * 4096, pc + 5 * 4096 + 2048, pc + 4 * 4096 + 3072);
            ATT_TILE(sB, sA, pc + 7 * 4096, pc + 6 * 4096 + 2048, pc + 5 * 4096 + 3072);
            if (st < 7) BAR_L();
            if (st < 7) __builtin_amdgcn_s_setprio(2);
            SBAR();
            ATT_TILE(sA, sB, pn, pc + 7 * 4096 + 2048, pc + 6 * 4096 + 3072);
            ATT_TILE(sB, sA, pn + 4096, pn + 2048, pc + 7 * 4096 + 3072);
            so = sn;
        }
        o = MFMA32(v1, BF8(p1), o);
        ls = MFMA16(sel, BF8(p1), ls);
#undef ATT_TILE
        const float la = __shfl(ls[0], lane & 15), lb = __shfl(ls[1], lane & 15);
        linv = 1.f / ((lane & 16) ? lb : la);
    } else {
        float m = -INFINITY, l = 0.f;
        const f32x16 zero = {};
        int so = 98304;
#pragma unroll 1
        for (int st = 0; st < 8; ++st) {
            const lds_ptr pb = pl + so;
#pragma unroll 1
            for (int t = 0; t < 8; ++t) {
                if (t == 6 && st < 7) BAR_L();
                k0 = ldsr128(pb + t * 4096); k1 = ldsr128(pb + t * 4096 + 1024); v0 = ldsr128(pb + t * 4096 + 2048); const bf16x8 v1 = ldsr128(pb + t * 4096 + 3072);
                f32x16 s = MFMA32(k0, q0, zero);
                s = MFMA32(k1, q1, s);
                float tm = s[0];
#pragma unroll
                for (int j = 1; j < 16; ++j) tm = fmaxf(tm, s[j]);
                tm = fmaxf(tm, __shfl_xor(tm, 32));
                const float mn = fmaxf(m, tm), al = EX(m - mn);
                m = mn; l *= al;
#pragma unroll
                for (int j = 0; j < 16; ++j) o[j] *= al;
                u32x4 p0, p1;
#pragma unroll
                for (int j = 0; j < 4; ++j) {
                    p0[j] = cvtpk(EX(s[2 * j] - mn), EX(s[2 * j + 1] - mn));
                    p1[j] = cvtpk(EX(s[8 + 2 * j] - mn), EX(s[8 + 2 * j + 1] - mn));
                    l += (bflo(p0[j]) + bfhi(p0[j])) + (bflo(p1[j]) + bfhi(p1[j]));
                }
                o = MFMA32(v0, BF8(p0), o);
                o = MFMA32(v1, BF8(p1), o);
            }
            so = so == 131072 ? 65536 : so + 32768;
        }
        linv = 1.f / half_sum(l);
    }
#undef EX
#undef BF8
    STAMP(3);
    {
        const char* ob = ws + WS_O + ((size_t)(rb * 8 + (wid & 3) * 2) * 128 + (hp * 2 + (wid >> 2)) * 4) * 256;
        const unsigned vo = (unsigned)(((lane >> 4) & 1) * 32768 + (lane >> 5) * 256 + (lane & 15) * 16);
#pragma unroll
        for (int gp = 0; gp < 2; ++gp) {
            const auto r0 = __builtin_amdgcn_permlane32_swap(cvtpk(o[8 * gp] * linv, o[8 * gp + 1] * linv), cvtpk(o[8 * gp + 4] * linv, o[8 * gp + 5] * linv), false, false);
            const auto r1 = __builtin_amdgcn_permlane32_swap(cvtpk(o[8 * gp + 2] * linv, o[8 * gp + 3] * linv), cvtpk(o[8 * gp + 6] * linv, o[8 * gp + 7] * linv), false, false);
            st16(ob, vo + gp * 512, ((u32x4){r0[0], r1[0], r0[1], r1[1]}));
        }
    }
    BAR_L();
    WAITV_BAR(0);
    STAMP(4); STAMP_FLUSH(2);
}

struct OPre { f32x4 p[8]; };
__device__ __forceinline__ void oproj_pre(const Ctx& cx, OPre& o) {
    const int lane = lane_id(); int rb, ct; tile128x64(cx.bid, rb, ct);
    const unsigned vo = (unsigned)(((lane >> 4) & 1) * 2048 + ct * 64 + (lane & 15) * 4) * 4u;
#pragma unroll
    for (int i = 0; i < 8; ++i) LD16C(o.p[i], cx.ws + WS_MP2, vo + i * 16384);
    g64_pre(cx, cx.ws + WS_WO + (size_t)ct * 131072, lane);
}
template <class Next>
__device__ __forceinline__ void oproj_produce(const Ctx& cx, OPre& o, const u16* __restrict__ x, Next next_pre) {
    char* ws = cx.ws; const int pw = cx.w, lane = lane_id();
    int rb, ct; tile128x64(cx.bid, rb, ct);
    STAMP_DECL; STAMP(0);
    await_input(ws, pw, C_O + rb, 16, lane);
    STAMP(1);
    f32x4 tv;
    g64_produce<4>(cx, ws + WS_O + (size_t)rb * 8 * 32768, ws + WS_WO + (size_t)ct * 131072, lane, []() {},
        [&]() { STAMP(2); tv = rbf4(sum8(o.p)); if (lane & 16) tv = rbf4(tv + 1.f); PIN(tv); },
        [&]() {
            const unsigned voffX = (unsigned)((rb * 128 + pw * 32 + (lane >> 3)) * 2048 + ct * 128 + (((lane & 7) ^ ((lane >> 3) & 7)) << 4));
#pragma unroll
            for (int i = 0; i < 4; ++i) dma16((const char*)x + i * 8 * 2048, voffX, cx.lds0 + 147456 + pw * 4096 + i * 1024);
            if (pw == 0 && lane < 32) *(LAS f32x4*)(cx.lds + 98304 + lane * 16) = tv; });
    WAITV_BAR(0);
    BARRIER();
    next_pre();
    BARRIER();
    if (pw == 0) signal(ws, C_Y + (rb >> 1), lane);
    PSTAMP_FLUSH(11);
}
__device__ __forceinline__ void oproj_consume(const Ctx& cx) {
    char* ws = cx.ws; const lds_ptr lds = cx.lds; const int wid = cx.w, lane = lane_id();
    STAMP_DECL; STAMP(0);
    f32x4 acc[2][2];
    int rb, ct; tile128x64(cx.bid, rb, ct);
    unsigned tch = 0u;
    g64_consume<true>(acc, cx, WS_WO + (size_t)ct * 131072, tch, lane, [](int) {}, []() {}, [&]() { STAMP(4); });
    asm volatile("s_waitcnt vmcnt(0) lgkmcnt(0)\n\ts_barrier" : "+v"(tch) :: "memory");
    STAMP(1);
    const int tb = wid & 3, nh = wid >> 2, g = lane >> 4, gc = ((g & 1) << 1) | (g >> 1);
    f32x4 ga[2], cb[2]; u32x4 x1v[2];
#pragma unroll
    for (int nf = 0; nf < 2; ++nf) { ga[nf] = *(const LAS f32x4*)(lds + 98304 + (nh * 8 + nf * 4 + g) * 16); cb[nf] = *(const LAS f32x4*)(lds + 98304 + 256 + (nh * 8 + nf * 4 + g) * 16); }
    float v[2][8];
#pragma unroll
    for (int m = 0; m < 2; ++m) {
        const int rl = tb * 32 + m * 16 + (lane & 15);
        unsigned u[2][2], y[2][2];
#pragma unroll
        for (int nf = 0; nf < 2; ++nf) {
            const u32x2 xv = *(const LAS u32x2*)(lds + 147456 + rl * 128 + (((nh * 4 + nf * 2 + (g >> 1)) ^ (rl & 7)) << 4) + 8 * (g & 1));
            u[nf][0] = cvtpk(bflo(xv[0]) + ga[nf][0] * acc[m][nf][0], bfhi(xv[0]) + ga[nf][1] * acc[m][nf][1]);
            u[nf][1] = cvtpk(bflo(xv[1]) + ga[nf][2] * acc[m][nf][2], bfhi(xv[1]) + ga[nf][3] * acc[m][nf][3]);
            v[m][4 * nf] = bflo(u[nf][0]); v[m][4 * nf + 1] = bfhi(u[nf][0]); v[m][4 * nf + 2] = bflo(u[nf][1]); v[m][4 * nf + 3] = bfhi(u[nf][1]);
            y[nf][0] = cvtpk(v[m][4 * nf] * cb[nf][0], v[m][4 * nf + 1] * cb[nf][1]); y[nf][1] = cvtpk(v[m][4 * nf + 2] * cb[nf][2], v[m][4 * nf + 3] * cb[nf][3]);
        }
        st16(ws + WS_Y + ((size_t)(rb * 8 + tb * 2 + m) * 128 + ct * 8 + nh * 4) * 256, (unsigned)(gc * 256 + (lane & 15) * 16), swap16(y[0][0], y[0][1], y[1][0], y[1][1]));
        x1v[m] = swap16(u[0][0], u[0][1], u[1][0], u[1][1]);
    }
    BAR_L();
#pragma unroll
    for (int m = 0; m < 2; ++m) {
        const float mean = rows_sum(((v[m][0] + v[m][1]) + (v[m][2] + v[m][3])) + ((v[m][4] + v[m][5]) + (v[m][6] + v[m][7]))) * (1.f / 32.f);
        float q = 0.f;
#pragma unroll
        for (int j = 0; j < 8; ++j) q += (v[m][j] - mean) * (v[m][j] - mean);
        q = rows_sum(q);
        if (g == 0) *(LAS f32x2*)(lds + 99328 + ((tb * 32 + m * 16 + (lane & 15)) * 2 + nh) * 8) = (f32x2){mean, q};
    }
    WAITVL_BAR(0);
    if (wid < 2) {
        const int rl = wid * 64 + lane;
        const f32x4 s = *(const LAS f32x4*)(lds + 99328 + rl * 16);
        const float dm = s[0] - s[2];
        st8(ws + WS_ST2 + (size_t)(ct * 2048 + rb * 128) * 8, (unsigned)rl * 8u, ((f32x2){0.5f * (s[0] + s[2]), s[1] + s[3] + 16.f * dm * dm}));
    }
#pragma unroll
    for (int m = 0; m < 2; ++m) st16(ws + WS_X1 + ((size_t)(rb * 8 + tb * 2 + m) * 128 + ct * 8 + nh * 4) * 256, (unsigned)(gc * 256 + (lane & 15) * 16), x1v[m]);
    STAMP(2); STAMP_FLUSH(3);
}

constexpr int GSL = 30720, G_ROW = 5 * GSL + 1024, G_COL = G_ROW + 2048;
__device__ __forceinline__ void tile_glu(int bid, int& rp, int& ct) { const int xcd = bid & 7, idx = bid >> 3; rp = (xcd & 1) * 4 + (idx & 3); ct = (xcd >> 1) * 8 + (idx >> 2); }
#define dmax dmac
#define GLU_W(t, so) do { const char* w_ = Wb + (size_t)(t) * 14336; const unsigned d2_ = pw < 3 ? dW + (so) + 2048 : cx.lds0 + 5 * GSL; \
        dmax(w_, voff, dW + (so)); dmax(w_ + 1024, voff, dW + (so) + 1024); dmax(w_, voff2, d2_); dmax(w_ + 1024, voff2, pw < 3 ? d2_ + 1024 : d2_); } while (0)
__device__ __forceinline__ void glu_pre(const Ctx& cx) {
    const int pw = cx.w, lane = lane_id(); int rp, ct; tile_glu(cx.bid, rp, ct);
    const char* Wb = cx.ws + WS_WGU + (size_t)ct * 32 * 14336 + pw * 4096;
    const unsigned voff = lane * 16, dW = cx.lds0 + 16384 + pw * 4096;
#pragma unroll
    for (int t = 0; t < 4; ++t) {
        dmac(Wb + t * 14336, voff, dW + t * GSL); dmac(Wb + t * 14336 + 1024, voff, dW + t * GSL + 1024);
        if (pw < 3) { dmac(Wb + t * 14336 + 2048, voff, dW + t * GSL + 2048); dmac(Wb + t * 14336 + 3072, voff, dW + t * GSL + 3072); }
    }
}
template <class Next>
__device__ __forceinline__ void glu_produce(const Ctx& cx, Next next_pre) {
    char* ws = cx.ws; const int pw = cx.w, lane = lane_id(); int rp, ct; tile_glu(cx.bid, rp, ct);
    const char* Ab = ws + WS_Y + (size_t)(rp * 16 + 4 * pw) * 32768;
    const char* Wb = ws + WS_WGU + (size_t)ct * 32 * 14336 + pw * 4096;
    const unsigned voff = lane * 16, voff2 = pw < 3 ? lane * 16 + 2048 : 0u, dA = cx.lds0 + pw * 4096, dW = cx.lds0 + 16384 + pw * 4096;
#define GLU_A(t, so) do { const char* a_ = Ab + (size_t)(t) * 1024; dmax(a_, voff, dA + (so)); dmax(a_ + 32768, voff, dA + (so) + 1024); dmax(a_ + 65536, voff, dA + (so) + 2048); dmax(a_ + 98304, voff, dA + (so) + 3072); } while (0)
    STAMP_DECL; STAMP(0);
    await_input(ws, pw, C_Y + rp, 32, lane);
    STAMP(1);
    GLU_A(0, 0); GLU_A(1, GSL); GLU_A(2, 2 * GSL); GLU_A(3, 3 * GSL);
    STAMP(2);
    WAITV_BAR(12);
    STAMP(3);
    if (pw == 0) { int rbo, cto; tile128x64(cx.bid, rbo, cto); signal(ws, C_ST + (rbo >> 1), lane); }
    WAITV_BAR(8); GLU_W(4, 4 * GSL); GLU_A(4, 4 * GSL);
    WAITV_BAR(12); GLU_W(5, 0); GLU_A(5, 0);
    int sf = GSL;
#define GLU_NEXT(t, VM) do { WAITV_BAR(VM); GLU_W((t) + 4, sf); GLU_A((t) + 4, sf); sf = sf == 4 * GSL ? 0 : sf + GSL; } while (0)
#pragma unroll 1
    for (int t = 2; t < 6; ++t) GLU_NEXT(t, 16);
    {
        unsigned cv;
        asm volatile("s_nop 4\n\tglobal_load_dword %0, %1, %2 sc1" : "=v"(cv) : "v"((C_ST + rp) * 256), "s"(ws + WS_CNT) : "memory");
        GLU_NEXT(6, 17); GLU_NEXT(7, 17); GLU_NEXT(8, 17); GLU_NEXT(9, 16);
        PIN(cv);
        if (!__all(cv >= 32u)) await<4>(ws, C_ST + rp, 1, 32, lane);
    }
    {
        f32x2 sp[16]; f32x4 cp[8];
        const char* sb_ = ws + WS_ST2 + (size_t)(rp * 256 + pw * 64) * 8;
#pragma unroll
        for (int i = 0; i < 8; ++i) LD8C(sp[i], sb_, (unsigned)(lane * 8 + i * 16384));
        GLU_NEXT(10, 24);
#pragma unroll
        for (int i = 8; i < 16; ++i) LD8C(sp[i], sb_, (unsigned)(lane * 8 + i * 16384));
        GLU_NEXT(11, 32);
        int i4 = pw * 64 + lane; i4 = i4 < 112 ? i4 : 0;
        const unsigned vo = (unsigned)((i4 / 28) * F + ct * 112 + (i4 % 28) * 4) * 4u;
#pragma unroll
        for (int i = 0; i < 8; ++i) LD16C(cp[i], ws + WS_UT, vo + i * (4 * F * 4));
        GLU_NEXT(12, 40); GLU_NEXT(13, 32); GLU_NEXT(14, 24); GLU_NEXT(15, 16);
        __builtin_amdgcn_s_setprio(0);
        float mean = 0.f, q = 0.f;
#pragma unroll
        for (int i = 0; i < 16; ++i) { PIN(sp[i]); mean += sp[i][0]; q += sp[i][1]; }
        mean *= 1.f / 16.f; PIN(mean); PIN(q);
        GLU_NEXT(16, 16);
        float dq = 0.f;
#pragma unroll
        for (int i = 0; i < 16; ++i) dq += (sp[i][0] - mean) * (sp[i][0] - mean);
        PIN(dq);
        GLU_NEXT(17, 16);
        const float rstd = 1.f / sqrtf((q + 64.f * dq) * (1.f / 1024.f) + 1e-5f);
        *(LAS f32x2*)(cx.lds + G_ROW + (pw * 64 + lane) * 8) = (f32x2){rstd, -mean * rstd};
        GLU_NEXT(18, 16);
        const f32x4 c = sum8(cp);
        if (pw * 64 + lane < 112) *(LAS f32x4*)(cx.lds + G_COL + (pw * 64 + lane) * 16) = c;
        __builtin_amdgcn_s_setprio(3);
        GLU_NEXT(19, 16);
    }
#pragma unroll 1
    for (int t = 20; t < 28; ++t) GLU_NEXT(t, 16);
    WAITV_BAR(16); WAITV_BAR(8); WAITVL_BAR(0);
#undef GLU_NEXT
#undef GLU_A
    BARRIER();
    next_pre();
    BARRIER();
    if (pw == 0) signal(ws, C_HH + rp * 2 + (ct >> 4), lane);
    PSTAMP_FLUSH(13);
}
#undef GLU_W
__device__ __forceinline__ void glu_consume(const Ctx& cx) {
    char* ws = cx.ws; const lds_ptr lds = cx.lds; const int wm = cx.w & 3, wn = cx.w >> 2;
    STAMP_DECL; STAMP(0);
    f32x4 acc[4][7];
    {
        const int lane = lane_id();
        const lds_ptr pa = lds + wm * 4096 + lane * 16, pw = lds + 16384 + wn * 7168 + lane * 16;
        bf16x8 af[4], wf[4];
#pragma unroll
        for (int n = 0; n < 7; ++n)
#pragma unroll
            for (int m = 0; m < 4; ++m) acc[m][n] = ZERO4;
        INPUT_BARRIER();
        WAITV_BAR(0);
        STAMP(1);
#pragma unroll
        for (int m = 0; m < 4; ++m) { af[m] = ldsr128(pa + m * 1024); wf[m] = ldsr128(pw + m * 1024); }
        int sc = 0, sn = GSL;
#define LAD(p) __builtin_amdgcn_s_setprio(p)
#define DBG_PRE(U) do {} while (0)
#define DBG_POST(U) do {} while (0)
#define GW(U, n) wf[(7 * (U) + (n)) & 3]
#define GLU_STEP(U, DO_NEXT) do { \
            DBG_PRE(U); if (DO_NEXT) BARRIER(); DBG_POST(U); LAD(1); SBAR(); \
            const lds_ptr pwc = pw + sc, pwn = pw + sn, pan = pa + sn; \
            _Pragma("unroll") for (int m = 0; m < 4; ++m) { MFMA16_ACC(acc[m][0], GW(U, 0), af[m]); MFMA16_ACC(acc[m][1], GW(U, 1), af[m]); } \
            SBAR(); GW(U, 4) = ldsr128(pwc + 4096); GW(U, 5) = ldsr128(pwc + 5120); SBAR(); \
            _Pragma("unroll") for (int m = 0; m < 4; ++m) MFMA16_ACC(acc[m][2], GW(U, 2), af[m]); \
            SBAR(); GW(U, 6) = ldsr128(pwc + 6144); SBAR(); \
            _Pragma("unroll") for (int m = 0; m < 4; ++m) MFMA16_ACC(acc[m][3], GW(U, 3), af[m]); \
            SBAR(); if (DO_NEXT) GW(U, 7) = ldsr128(pwn); SBAR(); \
            _Pragma("unroll") for (int m = 0; m < 4; ++m) MFMA16_ACC(acc[m][4], GW(U, 4), af[m]); \
            SBAR(); if (DO_NEXT) GW(U, 8) = ldsr128(pwn + 1024); SBAR(); \
            _Pragma("unroll") for (int m = 0; m < 4; ++m) { MFMA16_ACC(acc[m][5], GW(U, 5), af[m]); MFMA16_ACC(acc[m][6], GW(U, 6), af[m]); \
                SBAR(); if (DO_NEXT) af[m] = ldsr128(pan + m * 1024); if (m == X_GDROP) LAD(0); SBAR(); } \
            if (DO_NEXT) { GW(U, 9) = ldsr128(pwn + 2048); GW(U, 10) = ldsr128(pwn + 3072); } SBAR(); \
            sc = sn; sn = sn == 4 * GSL ? 0 : sn + GSL; } while (0)
#pragma unroll 1
        for (int t = 0; t < 28; t += 4) { GLU_STEP(0, true); GLU_STEP(1, true); GLU_STEP(2, true); GLU_STEP(3, true); }
        GLU_STEP(0, true); GLU_STEP(1, true); GLU_STEP(2, true); GLU_STEP(3, false);
#undef GLU_STEP
#undef GW
    }
    asm volatile("s_nop 7\n\ts_nop 7" : "+v"(acc[0][6]), "+v"(acc[1][6]), "+v"(acc[2][6]), "+v"(acc[3][6]), "+v"(acc[0][5]), "+v"(acc[1][5]), "+v"(acc[2][5]), "+v"(acc[3][5]) :: "memory");
    SBAR();
    STAMP(2);
    const int lane = lane_id(), g = lane >> 4, gc = ((g & 1) << 1) | (g >> 1);
    int rp, ct; tile_glu(cx.bid, rp, ct);
    const char* hb = ws + WS_HH + ((size_t)(rp * 16 + wm * 4) * (F / 8) + ct * 14 + wn * 7) * 256;
    const unsigned vo = (lane & 15) * 16;
    constexpr unsigned RB = (F / 8) * 256;
    f32x2 rw[4];
#pragma unroll
    for (int m = 0; m < 4; ++m) rw[m] = *(const LAS f32x2*)(lds + G_ROW + (wm * 64 + m * 16 + (lane & 15)) * 8);
#define GLU_OUT(m, f, ug, tg, uu, tu, d0, d1) do { const f32x4 gt = acc[m][f] * rw[m][0] + (ug * rw[m][1] + tg), up = acc[m][3 + (f)] * rw[m][0] + (uu * rw[m][1] + tu); \
        d0 = cvtpk(fmaxf(gt[0], 0.f) * up[0], fmaxf(gt[1], 0.f) * up[1]); d1 = cvtpk(fmaxf(gt[2], 0.f) * up[2], fmaxf(gt[3], 0.f) * up[3]); } while (0)
    {
        const lds_ptr pc = lds + G_COL + (wn * 14 + g) * 16;
        f32x4 ct_[3][4];
#pragma unroll
        for (int f = 0; f < 2; ++f)
#pragma unroll
            for (int k = 0; k < 4; ++k) ct_[f][k] = *(const LAS f32x4*)(pc + f * 64 + k * 448);
#pragma unroll
        for (int m = 0; m < 4; ++m) {
            unsigned a0, a1, b0, b1;
            GLU_OUT(m, 0, ct_[0][0], ct_[0][1], ct_[0][2], ct_[0][3], a0, a1); GLU_OUT(m, 1, ct_[1][0], ct_[1][1], ct_[1][2], ct_[1][3], b0, b1);
            st16(hb, vo + m * RB + gc * 256, swap16(a0, a1, b0, b1));
            if (m == 1) { _Pragma("unroll") for (int k = 0; k < 4; ++k) ct_[2][k] = *(const LAS f32x4*)(pc + 128 + k * 448); }
            SBAR();
        }
#pragma unroll
        for (int mp = 0; mp < 2; ++mp) {
            unsigned a0, a1, b0, b1;
            GLU_OUT(2 * mp, 2, ct_[2][0], ct_[2][1], ct_[2][2], ct_[2][3], a0, a1); GLU_OUT(2 * mp + 1, 2, ct_[2][0], ct_[2][1], ct_[2][2], ct_[2][3], b0, b1);
            st16(hb, vo + (2 * mp + (g & 1)) * RB + (4 + (g >> 1)) * 256, swap16(a0, a1, b0, b1));
            SBAR();
        }
    }
#undef GLU_OUT
    {
        const lds_ptr pc = lds + G_COL + (g >> 1) * 896 + (wn * 14 + 12 + (g & 1)) * 16;
        const f32x4 uc = *(const LAS f32x4*)pc, tc = *(const LAS f32x4*)(pc + 448);
        unsigned ux[2][2];
#pragma unroll
        for (int mp = 0; mp < 2; ++mp) {
            const f32x4 a = acc[2 * mp][6] * rw[2 * mp][0] + (uc * rw[2 * mp][1] + tc), b = acc[2 * mp + 1][6] * rw[2 * mp + 1][0] + (uc * rw[2 * mp + 1][1] + tc);
            float hv[4];
#pragma unroll
            for (int r = 0; r < 4; ++r) {
                const auto x = __builtin_amdgcn_permlane32_swap(__float_as_uint(a[r]), __float_as_uint(b[r]), false, false);
                hv[r] = fmaxf(__uint_as_float(x[0]), 0.f) * __uint_as_float(x[1]);
            }
            ux[mp][0] = cvtpk(hv[0], hv[1]); ux[mp][1] = cvtpk(hv[2], hv[3]);
        }
        st16(hb, vo + gc * RB + 6 * 256, swap16(ux[0][0], ux[0][1], ux[1][0], ux[1][1]));
    }
    STAMP(3);
    BARRIER();
    WAITV_BAR(0);
    STAMP(4); STAMP_FLUSH(5);
}

__device__ __forceinline__ void tile_down(int bid, int& rb, int& ct, int& kh) { const int xcd = bid & 7, idx = bid >> 3; kh = xcd & 1; rb = (xcd >> 1) * 4 + (idx & 3); ct = idx >> 2; }
#define DN_W(t, so) do { const char* w_ = Wb + (size_t)(t) * 16384; dmax(w_, voff, dW + (so)); dmax(w_ + 1024, voff, dW + (so) + 1024); dmax(w_ + 2048, voff, dW + (so) + 2048); dmax(w_ + 3072, voff, dW + (so) + 3072); } while (0)
__device__ __forceinline__ void down_pre(const Ctx& cx, OPre& o) {
    const int pw = cx.w, lane = lane_id(); int rb, ct, kh; tile_down(cx.bid, rb, ct, kh);
    if (pw == 0) await<4>(cx.ws, C_ST + (rb >> 1), 1, 32, lane);
    const unsigned vo = (unsigned)(3072 + ct * 128 + (lane & 31) * 4) * 4u;
#pragma unroll
    for (int i = 0; i < 8; ++i) LD16C(o.p[i], cx.ws + WS_MP2, vo + i * 16384);
    const char* Wb = cx.ws + WS_WD + (size_t)(ct * 112 + kh * 56) * 8192 + pw * 4096;
    const unsigned voff = lane * 16, dW = cx.lds0 + 16384 + pw * 4096;
#pragma unroll
    for (int t = 0; t < 4; ++t) DN_W(t, t * 32768);
}
__device__ __forceinline__ void down_produce(const Ctx& cx, OPre& o) {
    char* ws = cx.ws; const int pw = cx.w, lane = lane_id(); int rb, ct, kh; tile_down(cx.bid, rb, ct, kh);
    const char* Ab = ws + WS_HH + ((size_t)(rb * 8 + 2 * pw) * (F / 8) + kh * 224) * 256;
    const char* Wb = ws + WS_WD + (size_t)(ct * 112 + kh * 56) * 8192 + pw * 4096;
    const unsigned voff = lane * 16, dA = cx.lds0 + pw * 4096, dW = cx.lds0 + 16384 + pw * 4096;
#define DN_A(t, so) do { const char* a_ = Ab + (size_t)(t) * 2048; dmax(a_, voff, dA + (so)); dmax(a_ + 1024, voff, dA + (so) + 1024); \
        dmax(a_ + (F / 8) * 256, voff, dA + (so) + 2048); dmax(a_ + (F / 8) * 256 + 1024, voff, dA + (so) + 3072); } while (0)
    await_input(ws, pw, C_HH + (rb >> 1) * 2 + kh, 16, lane);
    DN_A(0, 0); DN_A(1, 32768); DN_A(2, 65536); DN_A(3, 98304);
    WAITV_BAR(12);
    WAITV_BAR(8); DN_W(4, 131072); DN_A(4, 131072);
    f32x4 tv = rbf4(sum8(o.p)); PIN(tv);
    WAITV_BAR(12); DN_W(5, 0); DN_A(5, 0);
    int sf = 32768;
#pragma unroll 1
    for (int t = 2; t < 24; ++t) {
        WAITV_BAR(16);
        DN_W(t + 4, sf); DN_A(t + 4, sf);
        sf = sf == 131072 ? 0 : sf + 32768;
    }
    WAITV_BAR(16);
    {
        const char* xb = ws + WS_X1 + ((size_t)(rb * 8 + (pw >> 1) * 4 + kh * 2 + (pw & 1)) * 128 + ct * 16) * 256;
#pragma unroll
        for (int i = 0; i < 4; ++i) dmac(xb + i * 1024, voff, cx.lds0 + 98304 + pw * 4096 + i * 1024);
        if (pw == 0 && lane < 32) *(LAS f32x4*)(cx.lds + 114688 + lane * 16) = tv;
    }
    WAITV_BAR(12); WAITV_BAR(4); WAITVL_BAR(0);
#undef DN_A
}
#undef DN_W
__device__ __forceinline__ void down_consume(const Ctx& cx, u16* __restrict__ out) {
    char* ws = cx.ws; const lds_ptr lds = cx.lds; const int wid = cx.w, lane = lane_id(), wm = wid >> 2, wn = wid & 3, g = lane >> 4;
    STAMP_DECL; STAMP(0);
    const lds_ptr pa = lds + wm * 8192 + lane * 16, pw = lds + 16384 + wn * 2048 + lane * 16;
    f32x4 acc[4][2];
#pragma unroll
    for (int m = 0; m < 4; ++m) { acc[m][0] = ZERO4; acc[m][1] = ZERO4; }
    bf16x8 aA[8], wA[4], aB[8], wB[4];
    INPUT_BARRIER();
    BARRIER(); SBAR();
    STAMP(1);
#pragma unroll
    for (int ks = 0; ks < 2; ++ks) {
        wA[2 * ks] = ldsr128(pw + ks * 8192); wA[2 * ks + 1] = ldsr128(pw + ks * 8192 + 1024);
#pragma unroll
        for (int m = 0; m < 4; ++m) aA[4 * ks + m] = ldsr128(pa + m * 2048 + ks * 1024);
    }
    SBAR();
    int sn = 32768;
#define DN_STEP(AC, WC, AN, WN, DO_NEXT) do { \
        if (DO_NEXT) BARRIER(); LAD(1); SBAR(); \
        const lds_ptr pan = pa + sn, pwn = pw + sn; \
        _Pragma("unroll") for (int i = 0; i < 8; ++i) { const int ks = i >> 2, m = i & 3; \
            MFMA16_ACC(acc[m][0], WC[2 * ks], AC[i]); MFMA16_ACC(acc[m][1], WC[2 * ks + 1], AC[i]); SBAR(); \
            if (DO_NEXT) { if (m < 2) WN[2 * ks + m] = ldsr128(pwn + ks * 8192 + m * 1024); AN[i] = ldsr128(pan + m * 2048 + ks * 1024); } \
            if (i == X_HDROP) LAD(0); \
            SBAR(); } \
        sn = sn == 131072 ? 0 : sn + 32768; } while (0)
#pragma unroll 1
    for (int t = 0; t < 26; t += 2) {
        DN_STEP(aA, wA, aB, wB, true); DN_STEP(aB, wB, aA, wA, true);
    }
    DN_STEP(aA, wA, aB, wB, true); DN_STEP(aB, wB, aA, wA, false);
#undef DN_STEP
    asm volatile("s_nop 7\n\ts_nop 7" : "+v"(acc[0][0]), "+v"(acc[0][1]), "+v"(acc[1][0]), "+v"(acc[1][1]), "+v"(acc[2][0]), "+v"(acc[2][1]), "+v"(acc[3][0]), "+v"(acc[3][1]));
    SBAR();
    STAMP(2);
    int rb, ct, kh; tile_down(cx.bid, rb, ct, kh);
    f32x4 keep[2][2];
#pragma unroll
    for (int mm = 0; mm < 2; ++mm) {
        const f32x4 g0 = kh ? acc[mm][0] : acc[2 + mm][0], g1 = kh ? acc[mm][1] : acc[2 + mm][1];
        st16(ws + WS_XCH + ((size_t)cx.bid * 8 + wid) * 2048, lane * 16 + mm * 1024, ((u32x4){cvtpk(g0[0], g0[1]), cvtpk(g0[2], g0[3]), cvtpk(g1[0], g1[1]), cvtpk(g1[2], g1[3])}));
        keep[mm][0] = kh ? acc[2 + mm][0] : acc[mm][0]; keep[mm][1] = kh ? acc[2 + mm][1] : acc[mm][1];
    }
    BARRIER();
    u32x2 xr[2][2]; f32x4 gb[2];
#pragma unroll
    for (int nf = 0; nf < 2; ++nf) {
        gb[nf] = *(const LAS f32x4*)(lds + 114688 + (wn * 8 + nf * 4 + g) * 16);
#pragma unroll
        for (int mm = 0; mm < 2; ++mm) xr[mm][nf] = *(const LAS u32x2*)(lds + 98304 + ((wm * 2 + mm) * 16 + wn * 4 + nf * 2 + (g >> 1)) * 256 + (lane & 15) * 16 + 8 * (g & 1));
    }
    STAMP(5);
    u32x4 got[2];
    {
        const char* xi = ws + WS_XCH + ((size_t)(cx.bid ^ 1) * 8 + wid) * 2048;
        for (unsigned spins = 0; spins < (1u << 15); ++spins) {
            asm volatile("s_nop 4\n\tglobal_load_dwordx4 %0, %2, %3 sc1\n\tglobal_load_dwordx4 %1, %2, %3 offset:1024 sc1\n\ts_waitcnt vmcnt(0)" : "=&v"(got[0]), "=&v"(got[1]) : "v"(lane * 16), "s"(xi) : "memory");
            const bool ok = got[0][0] != ~0u && got[0][1] != ~0u && got[0][2] != ~0u && got[0][3] != ~0u && got[1][0] != ~0u && got[1][1] != ~0u && got[1][2] != ~0u && got[1][3] != ~0u;
            if (__all(ok)) break;
            __builtin_amdgcn_s_sleep(2);
        }
    }
    STAMP(3);
#pragma unroll
    for (int mm = 0; mm < 2; ++mm) {
        unsigned u[2][2];
#pragma unroll
        for (int nf = 0; nf < 2; ++nf) {
            const f32x4 gg = gb[nf], k = keep[mm][nf];
            u[nf][0] = cvtpk(bflo(xr[mm][nf][0]) + gg[0] * (k[0] + bflo(got[mm][2 * nf])), bfhi(xr[mm][nf][0]) + gg[1] * (k[1] + bfhi(got[mm][2 * nf])));
            u[nf][1] = cvtpk(bflo(xr[mm][nf][1]) + gg[2] * (k[2] + bflo(got[mm][2 * nf + 1])), bfhi(xr[mm][nf][1]) + gg[3] * (k[3] + bfhi(got[mm][2 * nf + 1])));
        }
        st16(out + (size_t)(rb * 128 + wm * 64 + (2 * kh + mm) * 16) * D + ct * 128 + wn * 32, (unsigned)((lane & 15) * 2048 + (((g & 1) << 1) | (g >> 1)) * 16), swap16(u[0][0], u[0][1], u[1][0], u[1][1]));
    }
    STAMP(4); STAMP_FLUSH(6);
}

#define CTX_INIT extern __shared__ __attribute__((aligned(16))) char smem[]; \
    const int wid_ = uni(threadIdx.x >> 6); const bool prod = wid_ >= 8; \
    const Ctx cx = {ws, (lds_ptr)smem, uni((unsigned)(uintptr_t)(lds_ptr)smem), (int)blockIdx.x, prod ? wid_ - 8 : wid_}; \
    if (prod) __builtin_amdgcn_s_setprio(3)
__global__ __launch_bounds__(768, 3) void k_main(const u16* __restrict__ x, const u16* __restrict__ wq, const u16* __restrict__ wk, const u16* __restrict__ wv, const u16* __restrict__ wo,
                                                 const u16* __restrict__ wg, const u16* __restrict__ wu, const u16* __restrict__ wd, const u16* __restrict__ c, const u16* __restrict__ wmod,
                                                 char* __restrict__ ws, u16* __restrict__ out) {
    CTX_INIT;
    if (prod) {
        OPre o;
        ln1kv_produce(cx, wq, wk, wv, c, wmod, [&]() { attn_pre(cx); });
        attn_produce(cx, wo, wg, wu, wd, c, wmod, [&]() { oproj_pre(cx, o); });
        oproj_produce(cx, o, x, [&]() { glu_pre(cx); });
        glu_produce(cx, [&]() { down_pre(cx, o); });
        down_produce(cx, o);
    } else {
        ln1kv_consume(cx, x, c, wmod);
        attn_consume(cx);
        oproj_consume(cx);
        glu_consume(cx);
        down_consume(cx, out);
    }
}

extern "C" void kernel_launch(void* const* d_in, const int* in_sizes, int n_in, void* d_out, int out_size, void* d_ws, size_t ws_size, hipStream_t stream) {
    constexpr int LDS = 163840;
    (void)hipFuncSetAttribute((const void*)k_main, hipFuncAttributeMaxDynamicSharedMemorySize, LDS);
    k_main<<<256, 768, LDS, stream>>>((const u16*)d_in[0], (const u16*)d_in[1], (const u16*)d_in[2], (const u16*)d_in[3], (const u16*)d_in[4], (const u16*)d_in[6], (const u16*)d_in[7],
                                      (const u16*)d_in[8], (const u16*)d_in[9], (const u16*)d_in[10], (char*)d_ws, (u16*)d_out);
}
```

```cpp
#include <hip/hip_runtime.h>
#include <hip/hip_bf16.h>

#define X_SLEEP 2
#define X_GDROP 1
#define X_HDROP 5
#define X_S1 18
#define X_S2 8
#define X_XS 2
#define X_S3 30
#define MEGA 1
#define LAS __attribute__((address_space(3)))
#define GAS __attribute__((address_space(1)))
typedef unsigned short u16;
typedef short bf16x8 __attribute__((ext_vector_type(8)));
typedef short s16x4 __attribute__((ext_vector_type(4)));
typedef float f32x2 __attribute__((ext_vector_type(2)));
typedef float f32x4 __attribute__((ext_vector_type(4)));
typedef float f32x16 __attribute__((ext_vector_type(16)));
typedef unsigned u32x4 __attribute__((ext_vector_type(4)));
typedef unsigned u32x2 __attribute__((ext_vector_type(2)));
typedef __bf16 bf16x2v __attribute__((ext_vector_type(2)));
typedef LAS char* lds_ptr;

constexpr int D = 1024, F = 3584;
constexpr size_t WS_CNT = 0;
constexpr int C_KV = 0, C_SIDE = 16, C_ST = 32, C_EARLY = 40, C_NUM = 56;
constexpr size_t WS_FLG = 1u << 20;
constexpr int F_H1 = 0, F_O = 256, F_Y = 512, F_HH = 768, F_WQ = 1024;
constexpr size_t WS_EPOCH = 98304 + 4096;
constexpr size_t WS_MP1 = 32768;
constexpr size_t WS_MP2 = 786432;
constexpr size_t WS_KN = 98304;
constexpr size_t WS_KV = 262144;
constexpr size_t WS_ST2 = 524288;
constexpr size_t WS_UT = (1u << 20) + 262144;
constexpr size_t WS_H1 = 2u << 20;
constexpr size_t WS_O = WS_H1 + (4u << 20);
constexpr size_t WS_X1 = WS_O + (4u << 20);
constexpr size_t WS_Y = WS_X1 + (4u << 20);
constexpr size_t WS_HH = WS_Y + (4u << 20);
constexpr size_t WS_XCH = WS_HH + (16u << 20);
constexpr size_t WS_WQ = WS_XCH + (4u << 20);
constexpr size_t WS_WO = WS_WQ + (2u << 20);
constexpr size_t WS_WGU = WS_WO + (2u << 20);
constexpr size_t WS_WD = WS_WGU + (16u << 20);

__device__ __forceinline__ float bf2f(u16 b) { return __uint_as_float(((unsigned)b) << 16); }
__device__ __forceinline__ float bflo(unsigned w) { return __uint_as_float(w << 16); }
__device__ __forceinline__ float bfhi(unsigned w) { return __uint_as_float(w & 0xffff0000u); }
__device__ __forceinline__ unsigned cvtpk(float lo, float hi) { return __builtin_bit_cast(unsigned, __builtin_convertvector((f32x2){lo, hi}, bf16x2v)); }
__device__ __forceinline__ float rbf(float x) { return bflo(cvtpk(x, x)); }
__device__ __forceinline__ f32x4 rbf4(f32x4 s) { return (f32x4){rbf(s[0]), rbf(s[1]), rbf(s[2]), rbf(s[3])}; }

__device__ __forceinline__ void dma16(const void* gbase, unsigned voff, unsigned ldsaddr) {
    asm volatile("s_mov_b32 m0, %0\n\ts_nop 3\n\tglobal_load_lds_dwordx4 %1, %2" ::"s"(ldsaddr), "v"(voff), "s"(gbase) : "memory");
}
__device__ __forceinline__ void dmac(const void* gbase, unsigned voff, unsigned ldsaddr) {
    asm volatile("s_mov_b32 m0, %0\n\ts_nop 3\n\tglobal_load_lds_dwordx4 %1, %2 sc1" ::"s"(ldsaddr), "v"(voff), "s"(gbase) : "memory");
}
#define LD16(dst, base, voff) asm volatile("s_nop 4\n\tglobal_load_dwordx4 %0, %1, %2" : "=v"(dst) : "v"(voff), "s"(base) : "memory")
#define LD16C(dst, base, voff) asm volatile("s_nop 4\n\tglobal_load_dwordx4 %0, %1, %2 sc1" : "=v"(dst) : "v"(voff), "s"(base) : "memory")
#define LD8C(dst, base, voff) asm volatile("s_nop 4\n\tglobal_load_dwordx2 %0, %1, %2 sc1" : "=v"(dst) : "v"(voff), "s"(base) : "memory")
#define TOUCH(reg, base, voff) asm volatile("s_nop 4\n\tglobal_load_dword %0, %1, %2 sc1" : "+v"(reg) : "v"(voff), "s"(base) : "memory")
template <class T> __device__ __forceinline__ void st16(const void* base, unsigned voff, T v) {
    asm volatile("s_nop 4\n\tglobal_store_dwordx4 %0, %1, %2 sc1\n\ts_nop 1" ::"v"(voff), "v"(v), "s"(base) : "memory");
}
template <class T> __device__ __forceinline__ void st8(const void* base, unsigned voff, T v) { asm volatile("s_nop 4\n\tglobal_store_dwordx2 %0, %1, %2 sc1" ::"v"(voff), "v"(v), "s"(base) : "memory"); }
__device__ __forceinline__ void st4(const void* base, unsigned voff, float v) { asm volatile("s_nop 4\n\tglobal_store_dword %0, %1, %2 sc1" ::"v"(voff), "v"(v), "s"(base) : "memory"); }
#define WAIT_V(n) asm volatile("s_waitcnt vmcnt(" #n ")" ::: "memory")
#define BARRIER() asm volatile("s_barrier" ::: "memory")
#define BAR_L() asm volatile("s_waitcnt lgkmcnt(0)\n\ts_barrier" ::: "memory")
#define WAITV_BAR(n) asm volatile("s_waitcnt vmcnt(" #n ")\n\ts_barrier" ::: "memory")
#define WAITVL_BAR(n) asm volatile("s_waitcnt vmcnt(" #n ") lgkmcnt(0)\n\ts_barrier" ::: "memory")
#define PIN(x) asm volatile("" : "+v"(x))
#define SBAR() __builtin_amdgcn_sched_barrier(0)
#define MFMA32(a, b, c) __builtin_amdgcn_mfma_f32_32x32x16_bf16(a, b, c, 0, 0, 0)
#define MFMA16(a, b, c) __builtin_amdgcn_mfma_f32_16x16x32_bf16(a, b, c, 0, 0, 0)
#define MFMA16_ACC(c, a, b) asm volatile("v_mfma_f32_16x16x32_bf16 %0, %1, %2, %0" : "+v"(c) : "v"(a), "v"(b))
#define MFMA32_ACC(c, a, b) asm volatile("v_mfma_f32_32x32x16_bf16 %0, %1, %2, %0" : "+v"(c) : "v"(a), "v"(b))
#define ZERO4 ((f32x4){0.f, 0.f, 0.f, 0.f})
__device__ __forceinline__ bf16x8 ldsr128(lds_ptr p) { return *(const LAS bf16x8*)p; }
__device__ __forceinline__ s16x4 ldstr(lds_ptr p) { return __builtin_amdgcn_ds_read_tr16_b64_v4i16((LAS s16x4*)p); }
__device__ __forceinline__ bf16x8 ldstr2(lds_ptr p, int d) { s16x4 a = ldstr(p), b = ldstr(p + d); return (bf16x8){a[0], a[1], a[2], a[3], b[0], b[1], b[2], b[3]}; }
__device__ __forceinline__ unsigned uni(unsigned x) { return (unsigned)__builtin_amdgcn_readfirstlane((int)x); }
__device__ __forceinline__ int lane_id() { int l = __builtin_amdgcn_mbcnt_hi(~0u, __builtin_amdgcn_mbcnt_lo(~0u, 0u)); PIN(l); return l; }
__device__ __forceinline__ u32x4 swap16(unsigned a0, unsigned a1, unsigned b0, unsigned b1) {
    const auto r0 = __builtin_amdgcn_permlane16_swap(a0, b0, false, false), r1 = __builtin_amdgcn_permlane16_swap(a1, b1, false, false);
    return (u32x4){r0[0], r1[0], r0[1], r1[1]};
}
__device__ __forceinline__ u32x4 swap32(unsigned a0, unsigned a1, unsigned b0, unsigned b1) {
    const auto r0 = __builtin_amdgcn_permlane32_swap(a0, b0, false, false), r1 = __builtin_amdgcn_permlane32_swap(a1, b1, false, false);
    return (u32x4){r0[0], r1[0], r0[1], r1[1]};
}

#define STAMP_DECL do {} while (0)
#define STAMP(i) do {} while (0)
#define STAMP_FLUSH(kid) do {} while (0)
#define PSTAMP_FLUSH(kid) do {} while (0)

template <int CTRL> __device__ __forceinline__ float dppf(float v) { return __builtin_bit_cast(float, __builtin_amdgcn_mov_dpp(__builtin_bit_cast(int, v), CTRL, 0xf, 0xf, true)); }
__device__ __forceinline__ float half_sum(float v) { const auto r = __builtin_amdgcn_permlane32_swap(__float_as_uint(v), __float_as_uint(v), false, false); return __uint_as_float(r[0]) + __uint_as_float(r[1]); }
__device__ __forceinline__ float rows_sum(float v) {
    const auto r = __builtin_amdgcn_permlane16_swap(__float_as_uint(v), __float_as_uint(v), false, false); return half_sum(__uint_as_float(r[0]) + __uint_as_float(r[1]));
}
__device__ __forceinline__ float rows_max(float v) {
    auto r = __builtin_amdgcn_permlane16_swap(__float_as_uint(v), __float_as_uint(v), false, false); v = fmaxf(__uint_as_float(r[0]), __uint_as_float(r[1]));
    r = __builtin_amdgcn_permlane32_swap(__float_as_uint(v), __float_as_uint(v), false, false); return fmaxf(__uint_as_float(r[0]), __uint_as_float(r[1]));
}
__device__ __forceinline__ float row16_sum(float v) { v += dppf<0xB1>(v); v += dppf<0x4E>(v); v += dppf<0x141>(v); v += dppf<0x140>(v); return v; }
__device__ __forceinline__ float row16_max(float v) { v = fmaxf(v, dppf<0xB1>(v)); v = fmaxf(v, dppf<0x4E>(v)); v = fmaxf(v, dppf<0x141>(v)); v = fmaxf(v, dppf<0x140>(v)); return v; }
__device__ __forceinline__ float wsum(float v) { return rows_sum(row16_sum(v)); }
__device__ __forceinline__ float wmax(float v) { return rows_max(row16_max(v)); }
__device__ __forceinline__ f32x4 sum8(f32x4 (&p)[8]) {
    asm volatile("" : "+v"(p[0]), "+v"(p[1]), "+v"(p[2]), "+v"(p[3]), "+v"(p[4]), "+v"(p[5]), "+v"(p[6]), "+v"(p[7]));
    return ((p[0] + p[1]) + (p[2] + p[3])) + ((p[4] + p[5]) + (p[6] + p[7]));
}

__device__ __forceinline__ void signal(char* ws, int idx, int) {
    unsigned long long sv;
    asm volatile("s_mov_b64 %0, exec\n\ts_mov_b64 exec, 1\n\ts_nop 2\n\tglobal_atomic_add %1, %2, %3\n\ts_mov_b64 exec, %0" : "=&s"(sv) : "v"(idx * 256), "v"(1u), "s"(ws + WS_CNT) : "memory");
}
template <int SLEEP>
__device__ __forceinline__ void await(char* ws, int idx, int n, unsigned need, int lane) {
    if (MEGA) {
        GAS unsigned* p = (GAS unsigned*)(ws + WS_CNT) + (idx + (lane < n ? lane : 0)) * 64;
        for (unsigned spins = 0; spins < (1u << 15); ++spins) {
            if (__all(__hip_atomic_load(p, __ATOMIC_RELAXED, __HIP_MEMORY_SCOPE_AGENT) >= need)) break;
            __builtin_amdgcn_s_sleep(SLEEP);
        }
        asm volatile("s_waitcnt vmcnt(0)" ::: "memory");
    }
}
__device__ __forceinline__ void raise(char* ws, int idx, unsigned long long tag, int) {
    unsigned long long sv;
    asm volatile("s_mov_b64 %0, exec\n\ts_mov_b64 exec, 1\n\ts_nop 2\n\tglobal_store_dwordx2 %1, %2, %3 sc1\n\ts_mov_b64 exec, %0" : "=&s"(sv) : "v"(idx * 128), "v"(tag), "s"(ws + WS_FLG) : "memory");
}
__device__ __forceinline__ void expect(char* ws, int idx0, int off, unsigned long long tag) {
    GAS unsigned long long* p = (GAS unsigned long long*)(ws + WS_FLG) + (idx0 + (off < 0 ? 0 : off)) * 16;
    for (unsigned spins = 0; spins < (1u << 15); ++spins) {
        if (__all(off < 0 || __hip_atomic_load(p, __ATOMIC_RELAXED, __HIP_MEMORY_SCOPE_AGENT) == tag)) break;
        __builtin_amdgcn_s_sleep(X_SLEEP);
    }
    asm volatile("s_waitcnt vmcnt(0)" ::: "memory");
}
__device__ __forceinline__ void drain_raise(char* ws, lds_ptr word, int idx, unsigned long long tag, int lane) {
    asm volatile("s_waitcnt vmcnt(0)" ::: "memory");
    unsigned old = 0u;
    if (lane == 0) old = __hip_atomic_fetch_add((LAS unsigned*)word, 1u, __ATOMIC_RELAXED, __HIP_MEMORY_SCOPE_WORKGROUP);
    if (uni(old) == 7u) raise(ws, idx, tag, lane);
}
#define INPUT_BARRIER() BAR_L()

__device__ __forceinline__ void pack_load(const char* src  , unsigned pitch, unsigned ldsaddr, int lane) {
    const unsigned vo = (unsigned)(lane >> 3) * pitch, sw = (lane & 7) ^ (((lane >> 4) & 1) << 1);
#pragma unroll
    for (int i = 0; i < 4; ++i) dma16(src + (size_t)i * 8 * pitch, vo + ((sw ^ ((i & 1) << 2)) << 4), ldsaddr + i * 1024);
}
__device__ __forceinline__ bf16x8 pack_frag(lds_ptr area, int nf, int lane) {
    const int g = lane >> 4, q = (lane & 15) >> 2;
    return ldstr2(area + (8 * g + q) * 128 + ((nf ^ (((q >> 1) & 1) | ((g & 1) << 1))) << 5) + (lane & 3) * 8, 512);
}
__device__ __forceinline__ void mod_unit(lds_ptr area, bf16x8 sc, lds_ptr red, int lane) {
    f32x4 d[4];
#pragma unroll
    for (int nf = 0; nf < 4; ++nf) d[nf] = MFMA16(pack_frag(area, nf, lane), sc, ZERO4);
    if ((lane & 15) == 0) {
#pragma unroll
        for (int nf = 0; nf < 4; ++nf) *(LAS f32x4*)(red + nf * 64 + (lane >> 4) * 16) = d[nf];
    }
}

__device__ __forceinline__ float silu_bf(float cv) { return rbf(cv / (1.f + __expf(-cv))); }
__device__ __forceinline__ bf16x8 silu_frag(u32x4 cw) {
    PIN(cw);
    u32x4 r;
#pragma unroll
    for (int j = 0; j < 4; ++j) r[j] = cvtpk(silu_bf(bflo(cw[j])), silu_bf(bfhi(cw[j])));
    return __builtin_bit_cast(bf16x8, r);
}

__device__ __forceinline__ void tile128x64(int bid, int& rb, int& ct) { const int xcd = bid & 7, idx = bid >> 3; rb = (xcd >> 1) * 4 + (idx & 3); ct = (xcd & 1) * 8 + (idx >> 2); }
__device__ __forceinline__ unsigned long long epoch_load(char* ws) { return __hip_atomic_load((GAS unsigned long long*)(ws + WS_EPOCH), __ATOMIC_RELAXED, __HIP_MEMORY_SCOPE_AGENT); }
__device__ __forceinline__ unsigned long long epoch_tag(unsigned long long e) { return ((((unsigned long long)uni((unsigned)(e >> 32)) << 32) | uni((unsigned)e)) + 1ull) << 24; }
__device__ __forceinline__ void tile_down(int bid, int& rb, int& ct, int& kh) { const int xcd = bid & 7, idx = bid >> 3; kh = xcd & 1; rb = (xcd >> 1) * 4 + (idx & 3); ct = idx >> 2; }
struct Ctx { char* ws; lds_ptr lds; unsigned lds0; int bid, w; unsigned long long tag; };

__device__ __forceinline__ void ln1kv_consume(Ctx& cx, const u16* __restrict__ x, const u16* __restrict__ c, const u16* __restrict__ wmod) {
    char* ws = cx.ws; const lds_ptr lds = cx.lds; const int wid = cx.w, lane = lane_id(), tid = wid * 64 + lane;
    STAMP_DECL; STAMP(0);
    const int rb = (cx.bid >> 4) * 8 + (cx.bid & 7), isv = (cx.bid >> 3) & 1;
    const int idx = cx.bid >> 3, cs = ((((idx & 3) << 3) | (cx.bid & 7)) << 1) | ((idx >> 2) & 1), kq = idx >> 3, kr = kq * 256 + (tid >> 2);
    const u32x4 w0 = *(const u32x4*)(wmod + (size_t)kr * 6144 + cs * 32 + (tid & 3) * 8), w1 = *(const u32x4*)(wmod + (size_t)(kr + 128) * 6144 + cs * 32 + (tid & 3) * 8);
    const u16 c0 = c[kr], c1 = c[kr + 128];
    const unsigned long long ep = epoch_load(ws);
    u32x4 xv[2][2];
#pragma unroll
    for (int rr = 0; rr < 2; ++rr)
#pragma unroll
        for (int hh = 0; hh < 2; ++hh) xv[rr][hh] = *(const u32x4*)(x + (size_t)(rb * 16 + 2 * wid + rr) * D + hh * 512 + lane * 8);
    {
        const float s0 = silu_bf(bf2f(c0)), s1 = silu_bf(bf2f(c1));
        float a[8];
#pragma unroll
        for (int j = 0; j < 4; ++j) { a[2 * j] = s0 * bflo(w0[j]) + s1 * bflo(w1[j]); a[2 * j + 1] = s0 * bfhi(w0[j]) + s1 * bfhi(w1[j]); }
#pragma unroll
        for (int j = 0; j < 8; ++j) { a[j] += dppf<0x124>(a[j]); a[j] += dppf<0x128>(a[j]); a[j] = rows_sum(a[j]); }
        cx.tag = epoch_tag(ep);
        asm volatile("" : "+s"(cx.tag));
        if (lane < 4) { *(LAS f32x4*)(lds + 114688 + wid * 128 + lane * 32) = (f32x4){a[0], a[1], a[2], a[3]}; *(LAS f32x4*)(lds + 114688 + wid * 128 + lane * 32 + 16) = (f32x4){a[4], a[5], a[6], a[7]}; }
    }
    BAR_L();
    STAMP(6);
    const unsigned long long tag = cx.tag;
    if (wid == 0 && lane < 32) {
        const LAS float* r = (const LAS float*)(lds + 114688) + lane;
        const unsigned pv = __float_as_uint(((r[0] + r[32]) + (r[64] + r[96])) + ((r[128] + r[160]) + (r[192] + r[224])));
        __hip_atomic_store((GAS unsigned long long*)(ws + WS_MP1) + (kq * 2048 + ((cs * 32 + 1024) & 2047) + lane), tag | ((pv + 0x80u) >> 8), __ATOMIC_RELAXED, __HIP_MEMORY_SCOPE_AGENT);
    }
    STAMP(5);
    float xn[2][16];
#pragma unroll
    for (int rr = 0; rr < 2; ++rr) {
        float* v = xn[rr];
#pragma unroll
        for (int hh = 0; hh < 2; ++hh)
#pragma unroll
            for (int j = 0; j < 4; ++j) { v[hh * 8 + 2 * j] = bflo(xv[rr][hh][j]); v[hh * 8 + 2 * j + 1] = bfhi(xv[rr][hh][j]); }
        float sm = 0.f;
#pragma unroll
        for (int j = 0; j < 16; ++j) sm += v[j];
        const float mean = wsum(sm) * (1.f / 1024.f);
        float q = 0.f;
#pragma unroll
        for (int j = 0; j < 16; ++j) { v[j] -= mean; q += v[j] * v[j]; }
        const float rstd = 1.f / sqrtf(wsum(q) * (1.f / 1024.f) + 1e-5f);
#pragma unroll
        for (int j = 0; j < 16; ++j) { v[j] *= rstd; PIN(v[j]); }
    }
    LAS float* tab = (LAS float*)(lds + 98304);
    {
        u32x4 g[4][2];
        const unsigned tl = (unsigned)tag, th = (unsigned)(tag >> 32);
        __builtin_amdgcn_s_sleep(X_S1);
        for (unsigned spins = 0; spins < (1u << 15); ++spins) {
            asm volatile("s_nop 4\n\tglobal_load_dwordx4 %0, %8, %12 sc1\n\tglobal_load_dwordx4 %1, %8, %13 sc1\n\tglobal_load_dwordx4 %2, %9, %12 sc1\n\tglobal_load_dwordx4 %3, %9, %13 sc1\n\t"
                         "global_load_dwordx4 %4, %10, %12 sc1\n\tglobal_load_dwordx4 %5, %10, %13 sc1\n\tglobal_load_dwordx4 %6, %11, %12 sc1\n\tglobal_load_dwordx4 %7, %11, %13 sc1\n\ts_waitcnt vmcnt(0)"
                         : "=&v"(g[0][0]), "=&v"(g[0][1]), "=&v"(g[1][0]), "=&v"(g[1][1]), "=&v"(g[2][0]), "=&v"(g[2][1]), "=&v"(g[3][0]), "=&v"(g[3][1])
                         : "v"(tid * 16), "v"(tid * 16 + 16384), "v"(tid * 16 + 32768), "v"(tid * 16 + 49152), "s"(ws + WS_MP1), "s"(ws + WS_MP1 + 8192) : "memory");
            unsigned bad = 0u;
#pragma unroll
            for (int k = 0; k < 4; ++k)
                bad |= ((g[k][0][0] ^ tl) | (g[k][0][2] ^ tl) | (g[k][1][0] ^ tl) | (g[k][1][2] ^ tl)) >> 24 | (g[k][0][1] ^ th) | (g[k][0][3] ^ th) | (g[k][1][1] ^ th) | (g[k][1][3] ^ th);
            if (__all(bad == 0u)) break;
            __builtin_amdgcn_s_sleep(X_S2);
        }
        f32x4 v;
#pragma unroll
        for (int j = 0; j < 4; ++j)
            v[j] = (__uint_as_float(g[0][j >> 1][(j & 1) * 2] << 8) + __uint_as_float(g[1][j >> 1][(j & 1) * 2] << 8)) + (__uint_as_float(g[2][j >> 1][(j & 1) * 2] << 8) + __uint_as_float(g[3][j >> 1][(j & 1) * 2] << 8));
        v = rbf4(v);
        *(LAS f32x2*)(tab + tid * 2) = (f32x2){rbf(1.f + v[0]), rbf(1.f + v[1])};
        *(LAS f32x2*)(tab + 1024 + tid * 2) = (f32x2){v[2], v[3]};
    }
    BAR_L();
    if (cx.bid == 0 && tid == 0) __hip_atomic_store((GAS unsigned long long*)(ws + WS_EPOCH), tag >> 24, __ATOMIC_RELAXED, __HIP_MEMORY_SCOPE_AGENT);
    STAMP(1);
    const lds_ptr img = lds + 65536;
#pragma unroll
    for (int hh = 0; hh < 2; ++hh) {
        const int c = hh * 64 + lane;
        const f32x4 g0 = *(const LAS f32x4*)(tab + c * 8), g1 = *(const LAS f32x4*)(tab + c * 8 + 4);
        const f32x4 b0 = *(const LAS f32x4*)(tab + 1024 + c * 8), b1 = *(const LAS f32x4*)(tab + 1024 + c * 8 + 4);
#pragma unroll
        for (int rr = 0; rr < 2; ++rr) {
            float h[8];
#pragma unroll
            for (int j = 0; j < 4; ++j) { h[j] = xn[rr][hh * 8 + j] * g0[j] + b0[j]; h[4 + j] = xn[rr][hh * 8 + 4 + j] * g1[j] + b1[j]; }
            *(LAS u32x4*)(img + c * 256 + (((2 * wid + rr) ^ (c & 15)) << 4)) = (u32x4){cvtpk(h[0], h[1]), cvtpk(h[2], h[3]), cvtpk(h[4], h[5]), cvtpk(h[6], h[7])};
        }
    }
    BAR_L();
    STAMP(2);
    f32x4 acc[2] = {ZERO4, ZERO4};
    const int g = lane >> 4, q = (lane & 15) >> 2, p = lane & 3;
    bf16x8 hf[4], wf[4][2];
#pragma unroll
    for (int i = 0; i < 4; ++i) {
        const int s = wid * 4 + i, cc = 4 * s + g;
        hf[i] = ldsr128(img + cc * 256 + (((lane & 15) ^ (cc & 15)) << 4));
#pragma unroll
        for (int nf = 0; nf < 2; ++nf) wf[i][nf] = ldstr2(lds + (32 * s + 8 * g + q) * 64 + ((nf ^ (g & 1)) << 5) + 8 * p, 256);
    }
    LAS float* red = (LAS float*)(lds + 147456);
    const char* hb = ws + WS_H1 + ((size_t)rb * 128 + 16 * wid) * 256;
    if (isv) {
#pragma unroll
        for (int i = 0; i < 4; ++i) { acc[0] = MFMA16(hf[i], wf[i][0], acc[0]); acc[1] = MFMA16(hf[i], wf[i][1], acc[1]); }
        st16(hb, lane * 16 + 1024, hf[1]); st16(hb, lane * 16 + 3072, hf[3]);
#pragma unroll
        for (int nf = 0; nf < 2; ++nf) *(LAS f32x4*)(red + wid * 512 + (nf * 16 + (lane & 15)) * 16 + 4 * g) = acc[nf];
    } else {
#pragma unroll
        for (int i = 0; i < 4; ++i) { acc[0] = MFMA16(wf[i][0], hf[i], acc[0]); acc[1] = MFMA16(wf[i][1], hf[i], acc[1]); }
        st16(hb, lane * 16, hf[0]); st16(hb, lane * 16 + 2048, hf[2]);
#pragma unroll
        for (int nf = 0; nf < 2; ++nf) *(LAS f32x4*)(red + wid * 512 + (lane & 15) * 32 + nf * 16 + 4 * g) = acc[nf];
    }
    WAITVL_BAR(0);
    STAMP(3);
    if (wid == 0) {
        const int o0 = isv ? (lane & 31) * 16 + 4 * (lane >> 5) : (lane & 15) * 32 + (g >> 1) * 16 + (g & 1) * 4, o1 = o0 + 8;
        f32x4 a = ZERO4, b = ZERO4;
#pragma unroll
        for (int w = 0; w < 8; ++w) { a += *(const LAS f32x4*)(red + w * 512 + o0); b += *(const LAS f32x4*)(red + w * 512 + o1); }
        const u32x4 v = {cvtpk(a[0], a[1]), cvtpk(a[2], a[3]), cvtpk(b[0], b[1]), cvtpk(b[2], b[3])};
        st16(ws + WS_KV + (size_t)(rb >> 1) * 4096, isv ? 2048 + (rb & 1) * 1024 + lane * 16 : (g >> 1) * 1024 + ((g & 1) * 32 + (rb & 1) * 16 + (lane & 15)) * 16, v);
        float n2 = 0.f, mx = 0.f;
#pragma unroll
        for (int j = 0; j < 4; ++j) { n2 += bflo(v[j]) * bflo(v[j]) + bfhi(v[j]) * bfhi(v[j]); mx = fmaxf(mx, fmaxf(fabsf(bflo(v[j])), fabsf(bfhi(v[j])))); }
        if (!isv) mx = rows_sum(n2);
        if (!(n2 - n2 == 0.f)) mx = INFINITY;
        mx = wmax(mx);
        if (lane == 0) st4(ws + WS_KN, (unsigned)(isv * 128 + rb) * 4u, mx);
    }
    WAITV_BAR(0);
    STAMP(4); STAMP_FLUSH(1);
}
template <class Next>
__device__ __forceinline__ void ln1kv_produce(Ctx& cx, const u16* __restrict__ wq, const u16* __restrict__ wk, const u16* __restrict__ wv, const u16* __restrict__ c, const u16* __restrict__ wmod, Next next_pre) {
    char* ws = cx.ws; const int pw = cx.w, lane = lane_id();
    cx.tag = epoch_tag(epoch_load(ws));
    asm volatile("" : "+s"(cx.tag));
    int rbq, hp; tile128x64(cx.bid, rbq, hp);
    if (cx.bid == 0 && pw == 2) { if (lane < C_NUM) st4(ws + WS_CNT, (unsigned)lane * 256u, 0.f); WAIT_V(0); }
    if (pw < 2) { pack_load((const char*)wq + (size_t)(rbq * 2 + pw) * 65536 + hp * 128, 2048, cx.lds0 + 106496 + pw * 4096, lane); }
    BARRIER();
    {
        const char* w = (const char*)(((cx.bid >> 3) & 1) ? wv : wk) + pw * 16384;
        const unsigned voff = (lane >> 2) * 64 + (((lane & 3) ^ (((lane >> 5) & 1) << 1)) << 4);
#pragma unroll
        for (int i = 0; i < 16; ++i) dma16(w + i * 1024, voff, cx.lds0 + pw * 16384 + i * 1024);
    }
    const int k0 = (cx.bid >> 5) * 128 + pw * 32, col2 = 1024 + (cx.bid & 31) * 64;
    u32x4 cw;
    pack_load((const char*)wmod + (size_t)k0 * 12288 + (2048 + col2) * 2, 12288, cx.lds0 + 126976 + pw * 4096, lane);
    LD16(cw, c, (unsigned)(k0 + (lane >> 4) * 8) * 2u);
    if (pw < 2) {
        WAIT_V(21);
#pragma unroll
        for (int nf = 0; nf < 4; ++nf) st16(ws + WS_WQ + (size_t)(hp * 32 + rbq * 2 + pw) * 4096, lane * 16 + nf * 1024, pack_frag(cx.lds + 106496 + pw * 4096, nf, lane));
    }
    WAIT_V(0);
    mod_unit(cx.lds + 126976 + pw * 4096, silu_frag(cw), cx.lds + 143360 + pw * 256, lane);
    BAR_L();
    if (pw == 0) {
        raise(ws, F_WQ + hp * 16 + rbq, cx.tag, lane);
        const LAS float* r = (const LAS float*)(cx.lds + 143360) + lane;
        st4(ws + WS_MP2, (unsigned)((cx.bid >> 5) * 4096 + col2 + lane) * 4u, (r[0] + r[64]) + (r[128] + r[192]));
    }
    BARRIER();
    BARRIER();
    if (pw == 0) raise(ws, F_H1 + cx.bid, cx.tag, lane);
    expect(ws, F_WQ + hp * 16, lane < 16 ? lane : -1, cx.tag);
    next_pre();
}

template <int WB>
__device__ __forceinline__ void g64_pre(const Ctx& cx, const char* Wp, int lane) {
#pragma unroll
    for (int t = 0; t < 10; ++t) { const unsigned d = cx.lds0 + (WB + t * 8192) % 163840 + cx.w * 2048; dmac(Wp + cx.w * 2048 + t * 8192, lane * 16, d); dmac(Wp + cx.w * 2048 + t * 8192 + 1024, lane * 16, d + 1024); }
}
template <int WB, int AB, int NTAIL, class Issued, class First, class Tail>
__device__ __forceinline__ void g64_produce(const Ctx& cx, const char* Ap, const char* Wp, int lane, Issued issued, First first, Tail tail) {
    const char* Aw = Ap + (size_t)(2 * cx.w) * 32768; const char* Ww = Wp + cx.w * 2048;
    const unsigned voff = lane * 16, dA = cx.lds0 + AB + cx.w * 4096, dW = cx.lds0 + cx.w * 2048;
#define G64_A(t) do { const char* a_ = Aw + (t) * 2048; const unsigned d_ = dA + ((t) % 5) * 16384; dmac(a_, voff, d_); dmac(a_ + 32768, voff, d_ + 2048); dmac(a_ + 1024, voff, d_ + 1024); dmac(a_ + 33792, voff, d_ + 3072); } while (0)
#define G64_W(t, slot) do { const unsigned d_ = dW + (WB + (slot) * 8192) % 163840; dmac(Ww + (t) * 8192, voff, d_); dmac(Ww + (t) * 8192 + 1024, voff, d_ + 1024); } while (0)
    G64_A(0); G64_A(1);
    issued();
    WAITV_BAR(4);
    G64_A(2); G64_A(3); G64_A(4);
    first();
    WAITV_BAR(12);
    WAITV_BAR(8); G64_A(5); G64_W(10, 0);
    WAITV_BAR(10); G64_A(6); G64_W(11, 1);
    WAITV_BAR(12); G64_A(7); G64_W(12, 2);
    WAITV_BAR(14); G64_A(8); G64_W(13, 3);
    WAITV_BAR(14); G64_A(9);
    WAITV_BAR(12); G64_A(10);
    WAITV_BAR(10); G64_A(11);
    WAITV_BAR(8); G64_A(12);
    WAITV_BAR(8); G64_A(13); G64_W(14, 8);
    WAITV_BAR(10); G64_A(14); G64_W(15, 9);
    WAITV_BAR(12); G64_A(15);
    tail();
    asm volatile("s_waitcnt vmcnt(%0)\n\ts_barrier" ::"i"(12 + NTAIL) : "memory");
    asm volatile("s_waitcnt vmcnt(%0)\n\ts_barrier" ::"i"(6 + NTAIL) : "memory");
    asm volatile("s_waitcnt vmcnt(%0) lgkmcnt(0)\n\ts_barrier" ::"i"(NTAIL) : "memory");
#undef G64_W
#undef G64_A
}
template <int WB, int AB, class Start, class Mid, class Late, class St>
__device__ __forceinline__ void g64_consume(f32x16& acc, const Ctx& cx, int lane, Start start, Mid mid, Late late, St stamp) {
    const int lo = (lane >> 5) * 256 + (lane & 15) * 16;
    const lds_ptr pa = cx.lds + AB + (cx.w & 3) * 4096 + ((lane >> 4) & 1) * 2048 + lo, pw = cx.lds + (cx.w >> 2) * 2048 + ((lane >> 4) & 1) * 1024 + lo;
    acc = (f32x16){};
    bf16x8 aA[4], wA[4], aB[4], wB[4];
    start();
    BARRIER(); SBAR();
    stamp();
#pragma unroll
    for (int k = 0; k < 4; ++k) { wA[k] = ldsr128(pw + WB + (k >> 1) * 4096 + (k & 1) * 512); aA[k] = ldsr128(pa + k * 512); }
    SBAR();
    int sa = 16384, sw = WB + 8192;
#define G64_STEP(AC, WC, AN, WN, DO_NEXT, HOOK) do { \
        if (DO_NEXT) BARRIER(); SBAR(); HOOK; \
        const lds_ptr pwn = pw + sw, pan = pa + sa; \
        MFMA32_ACC(acc, WC[0], AC[0]); SBAR(); \
        if (DO_NEXT) { WN[0] = ldsr128(pwn); AN[0] = ldsr128(pan); WN[1] = ldsr128(pwn + 512); } SBAR(); \
        MFMA32_ACC(acc, WC[1], AC[1]); SBAR(); \
        if (DO_NEXT) { AN[1] = ldsr128(pan + 512); WN[2] = ldsr128(pwn + 4096); AN[2] = ldsr128(pan + 1024); } SBAR(); \
        MFMA32_ACC(acc, WC[2], AC[2]); SBAR(); \
        if (DO_NEXT) { WN[3] = ldsr128(pwn + 4608); AN[3] = ldsr128(pan + 1536); } SBAR(); \
        MFMA32_ACC(acc, WC[3], AC[3]); SBAR(); \
        sa = sa == 65536 ? 0 : sa + 16384; sw = sw == (WB + 9 * 8192) % 163840 ? WB : (sw == 155648 ? 0 : sw + 8192); } while (0)
#define G64_PAIR() do { G64_STEP(aA, wA, aB, wB, true, (void)0); G64_STEP(aB, wB, aA, wA, true, (void)0); } while (0)
#pragma unroll 1
    for (int t = 0; t < 6; t += 2) G64_PAIR();
    mid(6);
#pragma unroll 1
    for (int t = 6; t < 10; t += 2) G64_PAIR();
    mid(10);
    G64_PAIR();
    mid(12);
    G64_STEP(aA, wA, aB, wB, true, (void)0);
    sw = (WB + 8 * 8192) % 163840;
    G64_STEP(aB, wB, aA, wA, true, (void)0);
    G64_STEP(aA, wA, aB, wB, true, late());
    G64_STEP(aB, wB, aA, wA, false, (void)0);
    asm volatile("s_nop 7\n\ts_nop 7" : "+v"(acc));
    SBAR();
#undef G64_PAIR
#undef G64_STEP
}

__device__ __forceinline__ void attn_pre(const Ctx& cx) { int rb, hp; tile128x64(cx.bid, rb, hp); g64_pre<0>(cx, cx.ws + WS_WQ + (size_t)hp * 131072, lane_id()); }
template <class Next>
__device__ __forceinline__ void attn_produce(const Ctx& cx, const u16* __restrict__ wo, const u16* __restrict__ wg, const u16* __restrict__ wu, const u16* __restrict__ wd, const u16* __restrict__ c, const u16* __restrict__ wmod, Next next_pre) {
    char* ws = cx.ws; const int pw = cx.w, lane = lane_id();
    int rb, hp; tile128x64(cx.bid, rb, hp);
    const char* kvg = ws + WS_KV + pw * 8192;
    const unsigned voff = lane * 16, kvd = cx.lds0 + pw * 8192;
#define KV_STAGE(st, so) do { _Pragma("unroll") for (int i_ = 0; i_ < 8; ++i_) dmac(kvg + (st) * 32768 + i_ * 1024, voff, kvd + (so) + i_ * 1024); } while (0)
    STAMP_DECL; STAMP(0);
    expect(ws, F_H1 + rb * 16, lane < 16 ? lane : -1, cx.tag);
    STAMP(1);
    if (pw == 0) signal(ws, C_SIDE + (cx.bid & 15), lane);
    g64_produce<0, 81920, 8>(cx, ws + WS_H1 + (size_t)rb * 8 * 32768, ws + WS_WQ + (size_t)hp * 131072, lane,
                    [&]() { BARRIER(); if (pw == 0) signal(ws, C_KV + (cx.bid & 15), lane); },
                    [&]() { STAMP(2); },
                    [&]() { KV_STAGE(0, 32768); });
    WAITV_BAR(0);
    KV_STAGE(1, 65536);
    const int ao = (pw & 1) * 16384 + (pw >> 1) * 131072;
    const lds_ptr area = cx.lds + ao; const unsigned area0 = cx.lds0 + ao;
    const int k0 = (cx.bid >> 5) * 128 + pw * 32, col2 = (cx.bid & 15) * 64 + ((cx.bid >> 4) & 1) * 3072;
    const int gw = cx.bid * 4 + pw, isgu = gw < 896, mat = gw >= 448, k8 = (gw - mat * 448) / 56, cg = gw - mat * 448 - k8 * 56;
    u32x4 cw; f32x4 vp[8];
    pack_load((const char*)wmod + (size_t)k0 * 12288 + (2048 + col2) * 2, 12288, area0, lane);
    LD16(cw, c, (unsigned)(k0 + (lane >> 4) * 8) * 2u);
    auto unit_load = [&](int j) {
        const unsigned dst = area0 + 8192 + (j & 1) * 4096;
        if (!isgu) { const int u = (gw - 896) * 4 + (j & 3); pack_load((const char*)wo + (size_t)(u >> 4) * 65536 + (u & 15) * 128, 2048, dst, lane); }
        else if (j < 4) pack_load((const char*)(mat ? wu : wg) + (size_t)(k8 * 4 + j) * 32 * 7168 + cg * 128, 7168, dst, lane);
        else { const int u = (j - 4) * 896 + gw; pack_load((const char*)wd + (size_t)(u >> 4) * 65536 + (u & 15) * 128, 2048, dst, lane); }
    };
    f32x4 ut[4] = {ZERO4, ZERO4, ZERO4, ZERO4};
    unsigned guo[4];
#pragma unroll
    for (int nf = 0; nf < 4; ++nf) {
        const int cj = cg * 8 + 2 * nf + ((lane >> 3) & 1), ct = cj / 14, r = cj - ct * 14, wn = r >= 7, c = r - wn * 7;
        const int fr = c < 6 ? mat * 3 + (c >> 1) : 6, hf = c < 6 ? (c & 1) : mat;
        guo[nf] = (unsigned)((ct * 32 * 14 + wn * 7 + fr) * 1024 + (lane >> 4) * 256 + (hf * 8 + (lane & 7)) * 16);
        PIN(guo[nf]);
    }
    auto unit_store = [&](int j) {
        const lds_ptr src = area + 8192 + (j & 1) * 4096;
        bf16x8 f[4];
#pragma unroll
        for (int nf = 0; nf < 4; ++nf) f[nf] = pack_frag(src, nf, lane);
        if (!isgu || j >= 4) {
            const int u = isgu ? (j - 4) * 896 + gw : (gw - 896) * 4 + (j & 3);
            const char* d = isgu ? ws + WS_WD + (size_t)((((u & 15) >> 1) * 112 + (u >> 4)) * 2 + (u & 1)) * 4096 : (j < 4 ? ws + WS_WO + (size_t)((u & 15) * 32 + (u >> 4)) * 4096 : ws + WS_UT + 458752 + (gw & 15) * 4096);
#pragma unroll
            for (int nf = 0; nf < 4; ++nf) st16(d, voff + nf * 1024, f[nf]);
        } else {
            bf16x8 vf = ldsr128(area + 4096 + (lane & 1) * 256 + j * 64 + (lane >> 4) * 16);
#pragma unroll
            for (int nf = 0; nf < 4; ++nf) {
                st16(ws + WS_WGU + (size_t)(k8 * 4 + j) * 14336, guo[nf], f[nf]);
                ut[nf] = MFMA16(f[nf], vf, ut[nf]);
            }
        }
    };
    {
        const unsigned vo = (unsigned)((lane < 32 ? 2048 : 1024) + (k8 & 7) * 128 + (lane & 31) * 4) * 4u;
#pragma unroll
        for (int i = 0; i < 8; ++i) LD16C(vp[i], ws + WS_MP2, vo + i * 16384);
    }
#define DBA(i) do {} while (0)
#define DBB(i) do {} while (0)
    unit_load(0);
    STAMP(3);
    WAITV_BAR(17);
    KV_STAGE(2, 98304);
    WAIT_V(8);
    mod_unit(area, silu_frag(cw), area + 5120, lane);
    {
        f32x4 v = rbf4(sum8(vp));
        if (lane < 32) v = rbf4(v + 1.f);
        *(LAS u32x2*)(area + 4096 + lane * 8) = (u32x2){cvtpk(v[0], v[1]), cvtpk(v[2], v[3])};
    }
    unit_store(0); unit_load(1);
    STAMP(4);
    WAITVL_BAR(8);
    KV_STAGE(3, 32768);
    if (pw == 0) {
        const LAS float* r = (const LAS float*)(cx.lds + 5120) + lane;
        st4(ws + WS_MP2, (unsigned)((cx.bid >> 5) * 4096 + col2 + lane) * 4u, (r[0] + r[4096]) + (r[32768] + r[36864]));
    }
    WAIT_V(8); unit_store(1); unit_load(2);
    STAMP(5);
    DBA(0); WAITVL_BAR(8); DBB(0); KV_STAGE(4, 65536); WAIT_V(8); unit_store(2); unit_load(3);
    DBA(1); WAITVL_BAR(8); DBB(1); KV_STAGE(5, 98304); WAIT_V(8); unit_store(3); unit_load(4);
    if (isgu && (lane & 15) < 2) {
#pragma unroll
        for (int nf = 0; nf < 4; ++nf) st16(ws + WS_UT, (unsigned)((k8 * 4 + mat * 2 + (lane & 15)) * F + cg * 64 + nf * 16 + (lane >> 4) * 4) * 4u, ut[nf]);
    }
    DBA(2); WAITVL_BAR(8); DBB(2); KV_STAGE(6, 32768); WAIT_V(8); unit_store(4); unit_load(5);
    DBA(3); WAITVL_BAR(8); DBB(3);
    if (pw == 0) signal(ws, C_EARLY + (cx.bid & 15), lane);
    KV_STAGE(7, 65536); WAIT_V(8); unit_store(5);
    {
        const u32x4 e = {~0u, ~0u, ~0u, ~0u};
#pragma unroll
        for (int i = 0; i < 4; ++i) st16(ws + WS_XCH + (size_t)gw * 4096, voff + i * 1024, e);
    }
    WAITVL_BAR(0);
    if (pw == 0) signal(ws, C_SIDE + (cx.bid & 15), lane);
#undef KV_STAGE
    __builtin_amdgcn_s_setprio(3);
    await<16>(ws, C_EARLY, 16, 16, lane);
    STAMP(6);
    next_pre();
    STAMP(7);
    BARRIER();
    if (pw == 0) raise(ws, F_O + rb * 16 + hp, cx.tag, lane);
    PSTAMP_FLUSH(10);
}
__device__ __forceinline__ void attn_consume(const Ctx& cx) {
    char* ws = cx.ws; const lds_ptr lds = cx.lds; const int wid = cx.w, lane = lane_id();
    STAMP_DECL; STAMP(0);
    f32x16 qa; f32x2 kn2, vm2; float knm, vmm;
    unsigned cv;
    int rb, hp; tile128x64(cx.bid, rb, hp);
    g64_consume<0, 81920>(qa, cx, lane, []() {}, [&](int t) {
            if (MEGA) {
                if (t == 6) asm volatile("s_nop 4\n\tglobal_load_dword %0, %1, %2 sc1" : "=v"(cv) : "v"(wid == 0 ? (lane & 31) * 256 : 0), "s"(ws + WS_CNT) : "memory");
                if (t == 10) { asm volatile("s_waitcnt vmcnt(0)" : "+v"(cv) :: "memory"); if (wid == 0 && !__all(cv >= 16u)) await<6>(ws, C_KV, 32, 16, lane); }
            }
            if (t == 12) asm volatile("s_nop 4\n\tglobal_load_dwordx2 %0, %2, %3 sc1\n\tglobal_load_dwordx2 %1, %2, %3 offset:512 sc1" : "=&v"(kn2), "=&v"(vm2) : "v"(lane * 8), "s"(ws + WS_KN) : "memory"); },
        [&]() { asm volatile("s_waitcnt vmcnt(0)" : "+v"(kn2), "+v"(vm2) :: "memory"); knm = wmax(fmaxf(kn2[0], kn2[1])); vmm = wmax(fmaxf(vm2[0], vm2[1])); PIN(knm); PIN(vmm); SBAR(); },
        [&]() { STAMP(5); });
    BAR_L();
    STAMP(2);
    const lds_ptr pl = lds + lane * 16;
    bf16x8 k0 = ldsr128(pl + 32768), k1 = ldsr128(pl + 32768 + 1024), v0 = ldsr128(pl + 32768 + 2048);
    constexpr float QS = 0.17677669529663687f * 1.4426950408889634f;
    u32x4 qw0, qw1;
#pragma unroll
    for (int j = 0; j < 4; ++j) { qw0[j] = cvtpk(qa[2 * j] * QS, qa[2 * j + 1] * QS); qw1[j] = cvtpk(qa[8 + 2 * j] * QS, qa[8 + 2 * j + 1] * QS); }
    const bf16x8 q0 = __builtin_bit_cast(bf16x8, qw0), q1 = __builtin_bit_cast(bf16x8, qw1);
    f32x16 s0 = MFMA32(k0, q0, ((f32x16){})); s0 = MFMA32(k1, q1, s0);
    PIN(s0);
    float qn = 0.f;
#pragma unroll
    for (int j = 0; j < 4; ++j) { qn += bflo(qw0[j]) * bflo(qw0[j]) + bfhi(qw0[j]) * bfhi(qw0[j]) + bflo(qw1[j]) * bflo(qw1[j]) + bfhi(qw1[j]) * bfhi(qw1[j]); }
    qn = half_sum(qn);
    const bool fast = __all((qn * knm <= 88.f * 88.f) && (vmm <= 16777216.f) && (vmm >= 9.5367431640625e-7f));
    f32x16 o = {};
    float linv;
#define EX(x) __builtin_amdgcn_exp2f(x)
#define BF8(x) __builtin_bit_cast(bf16x8, x)
    if (fast) {
        const short ov = ((lane & 15) == ((lane >> 4) & 1)) ? (short)0x3f80 : (short)0;
        const bf16x8 sel = {ov, ov, ov, ov, ov, ov, ov, ov};
        f32x4 ls = ZERO4;
        const f32x16 zero = {};
        f32x16 sA, sB;
        bf16x8 v1 = v0;
        u32x4 p0, p1 = {0u, 0u, 0u, 0u};
        sA = s0;
        k0 = ldsr128(pl + 32768 + 4096); k1 = ldsr128(pl + 32768 + 4096 + 1024);
#define ATT_TILE(SC, SN, PK, PV0, PV1) do { \
            SN = MFMA32(k0, q0, zero); PIN(SN); SBAR(); k0 = ldsr128(PK); \
            SC[0] = EX(SC[0]); SC[1] = EX(SC[1]); SC[2] = EX(SC[2]); SC[3] = EX(SC[3]); PIN(SC); SBAR(); \
            o = MFMA32(v1, BF8(p1), o); PIN(o); SBAR(); v1 = ldsr128(PV1); \
            SC[4] = EX(SC[4]); SC[5] = EX(SC[5]); SC[6] = EX(SC[6]); PIN(SC); SBAR(); \
            ls = MFMA16(sel, BF8(p1), ls); PIN(ls); SBAR(); SC[7] = EX(SC[7]); p0[0] = cvtpk(SC[0], SC[1]); p0[1] = cvtpk(SC[2], SC[3]); PIN(SC); PIN(p0); SBAR(); \
            SN = MFMA32(k1, q1, SN); PIN(SN); SBAR(); k1 = ldsr128((PK) + 1024); \
            p0[2] = cvtpk(SC[4], SC[5]); p0[3] = cvtpk(SC[6], SC[7]); PIN(p0); \
            SC[8] = EX(SC[8]); SC[9] = EX(SC[9]); SC[10] = EX(SC[10]); PIN(SC); SBAR(); \
            o = MFMA32(v0, BF8(p0), o); PIN(o); SBAR(); v0 = ldsr128(PV0); \
            SC[11] = EX(SC[11]); SC[12] = EX(SC[12]); SC[13] = EX(SC[13]); PIN(SC); SBAR(); \
            ls = MFMA16(sel, BF8(p0), ls); PIN(ls); SBAR(); \
            SC[14] = EX(SC[14]); SC[15] = EX(SC[15]); \
            p1[0] = cvtpk(SC[8], SC[9]); p1[1] = cvtpk(SC[10], SC[11]); p1[2] = cvtpk(SC[12], SC[13]); p1[3] = cvtpk(SC[14], SC[15]); PIN(p1); SBAR(); } while (0)
        int so = 32768;
        STAMP(1);
#pragma unroll 1
        for (int st = 0; st < 8; ++st) {
            const int sn = so == 98304 ? 32768 : so + 32768;
            const lds_ptr pc = pl + so, pn = pl + sn;
            __builtin_amdgcn_s_setprio(1);
            ATT_TILE(sA, sB, pc + 2 * 4096, pc + 1 * 4096 + 2048, pc + 0 * 4096 + 3072);
            ATT_TILE(sB, sA, pc + 3 * 4096, pc + 2 * 4096 + 2048, pc + 1 * 4096 + 3072);
            ATT_TILE(sA, sB, pc + 4 * 4096, pc + 3 * 4096 + 2048, pc + 2 * 4096 + 3072);
            ATT_TILE(sB, sA, pc + 5 * 4096, pc + 4 * 4096 + 2048, pc + 3 * 4096 + 3072);
            __builtin_amdgcn_s_setprio(0);
            ATT_TILE(sA, sB, pc + 6 * 4096, pc + 5 * 4096 + 2048, pc + 4 * 4096 + 3072);
            ATT_TILE(sB, sA, pc + 7 * 4096, pc + 6 * 4096 + 2048, pc + 5 * 4096 + 3072);
            if (st < 7) BAR_L();
            if (st < 7) __builtin_amdgcn_s_setprio(2);
            SBAR();
            ATT_TILE(sA, sB, pn, pc + 7 * 4096 + 2048, pc + 6 * 4096 + 3072);
            ATT_TILE(sB, sA, pn + 4096, pn + 2048, pc + 7 * 4096 + 3072);
            so = sn;
        }
        o = MFMA32(v1, BF8(p1), o);
        ls = MFMA16(sel, BF8(p1), ls);
#undef ATT_TILE
        const float la = __shfl(ls[0], lane & 15), lb = __shfl(ls[1], lane & 15);
        linv = 1.f / ((lane & 16) ? lb : la);
    } else {
        float m = -INFINITY, l = 0.f;
        const f32x16 zero = {};
        int so = 32768;
#pragma unroll 1
        for (int st = 0; st < 8; ++st) {
            const lds_ptr pb = pl + so;
#pragma unroll 1
            for (int t = 0; t < 8; ++t) {
                if (t == 6 && st < 7) BAR_L();
                k0 = ldsr128(pb + t * 4096); k1 = ldsr128(pb + t * 4096 + 1024); v0 = ldsr128(pb + t * 4096 + 2048); const bf16x8 v1 = ldsr128(pb + t * 4096 + 3072);
                f32x16 s = MFMA32(k0, q0, zero);
                s = MFMA32(k1, q1, s);
                float tm = s[0];
#pragma unroll
                for (int j = 1; j < 16; ++j) tm = fmaxf(tm, s[j]);
                tm = fmaxf(tm, __shfl_xor(tm, 32));
                const float mn = fmaxf(m, tm), al = EX(m - mn);
                m = mn; l *= al;
#pragma unroll
                for (int j = 0; j < 16; ++j) o[j] *= al;
                u32x4 p0, p1;
#pragma unroll
                for (int j = 0; j < 4; ++j) {
                    p0[j] = cvtpk(EX(s[2 * j] - mn), EX(s[2 * j + 1] - mn));
                    p1[j] = cvtpk(EX(s[8 + 2 * j] - mn), EX(s[8 + 2 * j + 1] - mn));
                    l += (bflo(p0[j]) + bfhi(p0[j])) + (bflo(p1[j]) + bfhi(p1[j]));
                }
                o = MFMA32(v0, BF8(p0), o);
                o = MFMA32(v1, BF8(p1), o);
            }
            so = so == 98304 ? 32768 : so + 32768;
        }
        linv = 1.f / half_sum(l);
    }
#undef EX
#undef BF8
    STAMP(3);
    {
        const char* ob = ws + WS_O + ((size_t)(rb * 8 + (wid & 3) * 2) * 128 + (hp * 2 + (wid >> 2)) * 4) * 256;
        const unsigned vo = (unsigned)(((lane >> 4) & 1) * 32768 + (lane >> 5) * 256 + (lane & 15) * 16);
#pragma unroll
        for (int gp = 0; gp < 2; ++gp) {
            const auto r0 = __builtin_amdgcn_permlane32_swap(cvtpk(o[8 * gp] * linv, o[8 * gp + 1] * linv), cvtpk(o[8 * gp + 4] * linv, o[8 * gp + 5] * linv), false, false);
            const auto r1 = __builtin_amdgcn_permlane32_swap(cvtpk(o[8 * gp + 2] * linv, o[8 * gp + 3] * linv), cvtpk(o[8 * gp + 6] * linv, o[8 * gp + 7] * linv), false, false);
            st16(ob, vo + gp * 512, ((u32x4){r0[0], r1[0], r0[1], r1[1]}));
        }
    }
    WAITVL_BAR(0);
    STAMP(4); STAMP_FLUSH(2);
}

constexpr int O_STAT = 159744;
struct OPre { f32x4 p[8]; };
__device__ __forceinline__ void oproj_pre(const Ctx& cx, OPre& o) {
    const int lane = lane_id(); int rb, ct; tile128x64(cx.bid, rb, ct);
    const unsigned vo = (unsigned)(((lane >> 4) & 1) * 2048 + ct * 64 + (lane & 15) * 4) * 4u;
#pragma unroll
    for (int i = 0; i < 8; ++i) LD16C(o.p[i], cx.ws + WS_MP2, vo + i * 16384);
    g64_pre<98304>(cx, cx.ws + WS_WO + (size_t)ct * 131072, lane);
}
template <class Next>
__device__ __forceinline__ void oproj_produce(const Ctx& cx, OPre& o, const u16* __restrict__ x, Next next_pre) {
    char* ws = cx.ws; const int pw = cx.w, lane = lane_id();
    int rb, ct; tile128x64(cx.bid, rb, ct);
    STAMP_DECL; STAMP(0);
    if (pw == 0) expect(ws, F_O + rb * 16, lane < 2 ? lane : -1, cx.tag);
    INPUT_BARRIER();
    STAMP(1);
    f32x4 tv;
    g64_produce<98304, 16384, 4>(cx, ws + WS_O + (size_t)rb * 8 * 32768, ws + WS_WO + (size_t)ct * 131072, lane, []() {},
        [&]() { STAMP(2); tv = rbf4(sum8(o.p)); if (lane & 16) tv = rbf4(tv + 1.f); PIN(tv); },
        [&]() {
            const unsigned voffX = (unsigned)((rb * 128 + pw * 32 + (lane >> 3)) * 2048 + ct * 128 + (((lane & 7) ^ ((lane >> 3) & 7)) << 4));
#pragma unroll
            for (int i = 0; i < 4; ++i) dma16((const char*)x + i * 8 * 2048, voffX, cx.lds0 + 131072 + pw * 4096 + i * 1024);
            if (pw == 0 && lane < 32) *(LAS f32x4*)(cx.lds + 147456 + lane * 16) = tv; });
    WAITV_BAR(0);
    if (pw == 0 && lane == 0) *(LAS unsigned*)cx.lds = 0u;
    BAR_L();
    STAMP(3);
    next_pre();
    STAMP(4);
    PSTAMP_FLUSH(11);
}
__device__ __forceinline__ void oproj_consume(const Ctx& cx) {
    char* ws = cx.ws; const lds_ptr lds = cx.lds; const int wid = cx.w, lane = lane_id();
    STAMP_DECL; STAMP(0);
    f32x16 acc;
    int rb, ct; tile128x64(cx.bid, rb, ct);
    g64_consume<98304, 16384>(acc, cx, lane, [&]() { INPUT_BARRIER(); if (wid == 0) expect(ws, F_O + rb * 16, lane < 16 ? lane : -1, cx.tag); }, [](int) {}, []() {}, [&]() { STAMP(4); });
    BAR_L();
    STAMP(1);
    const int tb = wid & 3, nh = wid >> 2, h = lane >> 5, rl = tb * 32 + (lane & 31);
    const unsigned so = (unsigned)(((lane >> 4) & 1) * 32768 + h * 256 + (lane & 15) * 16);
    u32x4 x1v[2]; float v[16];
#pragma unroll
    for (int gp = 0; gp < 2; ++gp) {
        unsigned u[2][2], y[2][2];
#pragma unroll
        for (int k = 0; k < 2; ++k) {
            const int G = 2 * gp + k;
            const f32x4 ga = *(const LAS f32x4*)(lds + 147456 + (nh * 8 + G * 2 + h) * 16), cb = *(const LAS f32x4*)(lds + 147456 + 256 + (nh * 8 + G * 2 + h) * 16);
            const u32x2 xv = *(const LAS u32x2*)(lds + 131072 + rl * 128 + (((nh * 4 + G) ^ (rl & 7)) << 4) + 8 * h);
            u[k][0] = cvtpk(bflo(xv[0]) + ga[0] * acc[4 * G], bfhi(xv[0]) + ga[1] * acc[4 * G + 1]);
            u[k][1] = cvtpk(bflo(xv[1]) + ga[2] * acc[4 * G + 2], bfhi(xv[1]) + ga[3] * acc[4 * G + 3]);
            v[4 * G] = bflo(u[k][0]); v[4 * G + 1] = bfhi(u[k][0]); v[4 * G + 2] = bflo(u[k][1]); v[4 * G + 3] = bfhi(u[k][1]);
            y[k][0] = cvtpk(v[4 * G] * cb[0], v[4 * G + 1] * cb[1]); y[k][1] = cvtpk(v[4 * G + 2] * cb[2], v[4 * G + 3] * cb[3]);
        }
        st16(ws + WS_Y + ((size_t)(rb * 8 + tb * 2) * 128 + ct * 8 + nh * 4) * 256, so + gp * 512, swap32(y[0][0], y[0][1], y[1][0], y[1][1]));
        x1v[gp] = swap32(u[0][0], u[0][1], u[1][0], u[1][1]);
    }
    STAMP(5);
    BAR_L();
    STAMP(6);
    {
        float sm = 0.f;
#pragma unroll
        for (int j = 0; j < 16; ++j) sm += v[j];
        const float mean = half_sum(sm) * (1.f / 32.f);
        float q = 0.f;
#pragma unroll
        for (int j = 0; j < 16; ++j) q += (v[j] - mean) * (v[j] - mean);
        q = half_sum(q);
        if (h == 0) *(LAS f32x2*)(lds + O_STAT + (rl * 2 + nh) * 8) = (f32x2){mean, q};
    }
    STAMP(7);
    drain_raise(ws, lds, F_Y + rb * 16 + ct, cx.tag, lane);
    STAMP(3);
#pragma unroll
    for (int gp = 0; gp < 2; ++gp) st16(ws + WS_X1 + ((size_t)(rb * 8 + tb * 2) * 128 + ct * 8 + nh * 4) * 256, so + gp * 512, x1v[gp]);
    STAMP(2); STAMP_FLUSH(3);
}

constexpr int GSL = 30720, G_ROW = 5 * GSL + 1024, G_COL = G_ROW + 2048;
__device__ __forceinline__ void tile_glu(int bid, int& rp, int& ct) { const int xcd = bid & 7, idx = bid >> 3; rp = (xcd & 1) * 4 + (idx & 3); ct = (xcd >> 1) * 8 + (idx >> 2); }
#define dmax dmac
#define GLU_W(t, so) do { const char* w_ = Wb + (size_t)(t) * 14336; const unsigned d2_ = pw < 3 ? dW + (so) + 2048 : cx.lds0 + 5 * GSL; \
        dmax(w_, voff, dW + (so)); dmax(w_ + 1024, voff, dW + (so) + 1024); dmax(w_, voff2, d2_); dmax(w_ + 1024, voff2, pw < 3 ? d2_ + 1024 : d2_); } while (0)
__device__ __forceinline__ void glu_pre(const Ctx& cx) {
    const int pw = cx.w, lane = lane_id(); int rp, ct; tile_glu(cx.bid, rp, ct);
    const char* Wb = cx.ws + WS_WGU + (size_t)ct * 32 * 14336 + pw * 4096;
    const unsigned voff = lane * 16, dW = cx.lds0 + 16384 + pw * 4096;
#pragma unroll
    for (int t = 0; t < 4; ++t) {
        dmac(Wb + t * 14336, voff, dW + t * GSL); dmac(Wb + t * 14336 + 1024, voff, dW + t * GSL + 1024);
        if (pw < 3) { dmac(Wb + t * 14336 + 2048, voff, dW + t * GSL + 2048); dmac(Wb + t * 14336 + 3072, voff, dW + t * GSL + 3072); }
    }
}
template <class Next>
__device__ __forceinline__ void glu_produce(const Ctx& cx, Next next_pre) {
    char* ws = cx.ws; const int pw = cx.w, lane = lane_id(); int rp, ct; tile_glu(cx.bid, rp, ct);
    const char* Ab = ws + WS_Y + (size_t)(rp * 16 + 4 * pw) * 32768;
    const char* Wb = ws + WS_WGU + (size_t)ct * 32 * 14336 + pw * 4096;
    const unsigned voff = lane * 16, voff2 = pw < 3 ? lane * 16 + 2048 : 0u, dA = cx.lds0 + pw * 4096, dW = cx.lds0 + 16384 + pw * 4096;
#define GLU_A(t, so) do { const char* a_ = Ab + (size_t)(t) * 1024; dmax(a_, voff, dA + (so)); dmax(a_ + 32768, voff, dA + (so) + 1024); dmax(a_ + 65536, voff, dA + (so) + 2048); dmax(a_ + 98304, voff, dA + (so) + 3072); } while (0)
    STAMP_DECL; STAMP(0);
    if (pw == 0) expect(ws, F_Y + rp * 32, lane < 32 && (lane & 15) == 0 ? lane : -1, cx.tag);
    INPUT_BARRIER();
    STAMP(1);
    GLU_A(0, 0); GLU_A(1, GSL);
    STAMP(2);
    WAITV_BAR(4);
    GLU_A(2, 2 * GSL); GLU_A(3, 3 * GSL);
    STAMP(3);
    if (pw == 0) { int rbo, cto; tile128x64(cx.bid, rbo, cto); signal(ws, C_ST + (rbo >> 1), lane); }
    WAITV_BAR(8); GLU_W(4, 4 * GSL); GLU_A(4, 4 * GSL);
    WAITV_BAR(12); GLU_W(5, 0); GLU_A(5, 0);
    int sf = GSL;
#define GLU_NEXT(t, VM) do { WAITV_BAR(VM); GLU_W((t) + 4, sf); GLU_A((t) + 4, sf); sf = sf == 4 * GSL ? 0 : sf + GSL; } while (0)
#pragma unroll 1
    for (int t = 2; t < 6; ++t) GLU_NEXT(t, 16);
    {
        unsigned cv;
        asm volatile("s_nop 4\n\tglobal_load_dword %0, %1, %2 sc1" : "=v"(cv) : "v"((C_ST + rp) * 256), "s"(ws + WS_CNT) : "memory");
        GLU_NEXT(6, 17); GLU_NEXT(7, 17); GLU_NEXT(8, 17); GLU_NEXT(9, 16);
        PIN(cv);
        if (!__all(cv >= 32u)) await<4>(ws, C_ST + rp, 1, 32, lane);
    }
    {
        f32x2 sp[16]; f32x4 cp[8];
        const char* sb_ = ws + WS_ST2 + (size_t)(rp * 256 + pw * 64) * 8;
#pragma unroll
        for (int i = 0; i < 8; ++i) LD8C(sp[i], sb_, (unsigned)(lane * 8 + i * 16384));
        GLU_NEXT(10, 24);
#pragma unroll
        for (int i = 8; i < 16; ++i) LD8C(sp[i], sb_, (unsigned)(lane * 8 + i * 16384));
        GLU_NEXT(11, 32);
        int i4 = pw * 64 + lane; i4 = i4 < 112 ? i4 : 0;
        const unsigned vo = (unsigned)((i4 / 28) * F + ct * 112 + (i4 % 28) * 4) * 4u;
#pragma unroll
        for (int i = 0; i < 8; ++i) LD16C(cp[i], ws + WS_UT, vo + i * (4 * F * 4));
        GLU_NEXT(12, 40); GLU_NEXT(13, 32); GLU_NEXT(14, 24); GLU_NEXT(15, 16);
        __builtin_amdgcn_s_setprio(0);
        float mean = 0.f, q = 0.f;
#pragma unroll
        for (int i = 0; i < 16; ++i) { PIN(sp[i]); mean += sp[i][0]; q += sp[i][1]; }
        mean *= 1.f / 16.f; PIN(mean); PIN(q);
        GLU_NEXT(16, 16);
        float dq = 0.f;
#pragma unroll
        for (int i = 0; i < 16; ++i) dq += (sp[i][0] - mean) * (sp[i][0] - mean);
        PIN(dq);
        GLU_NEXT(17, 16);
        const float rstd = 1.f / sqrtf((q + 64.f * dq) * (1.f / 1024.f) + 1e-5f);
        *(LAS f32x2*)(cx.lds + G_ROW + (pw * 64 + lane) * 8) = (f32x2){rstd, -mean * rstd};
        GLU_NEXT(18, 16);
        const f32x4 c = sum8(cp);
        if (pw * 64 + lane < 112) *(LAS f32x4*)(cx.lds + G_COL + (pw * 64 + lane) * 16) = c;
        __builtin_amdgcn_s_setprio(3);
        GLU_NEXT(19, 16);
    }
    {
        unsigned cv;
        int rbd, ctd, khd; tile_down(cx.bid, rbd, ctd, khd);
        asm volatile("s_nop 4\n\tglobal_load_dword %0, %1, %2 sc1" : "=v"(cv) : "v"((lane < 16 ? C_SIDE + lane : C_ST + (rbd >> 1)) * 256), "s"(ws + WS_CNT) : "memory");
        GLU_NEXT(20, 17); GLU_NEXT(21, 17); GLU_NEXT(22, 17); GLU_NEXT(23, 16);
        PIN(cv);
        if (!__all(cv >= 32u)) { await<4>(ws, C_SIDE, 16, 32, lane); await<4>(ws, C_ST + (rbd >> 1), 1, 32, lane); }
    }
#pragma unroll 1
    for (int t = 24; t < 28; ++t) GLU_NEXT(t, 16);
    WAITV_BAR(16); WAITV_BAR(8); WAITVL_BAR(0);
#undef GLU_NEXT
#undef GLU_A
    if (pw == 0 && lane == 0) *(LAS unsigned*)(cx.lds + G_COL + 1920) = 0u;
    BAR_L();
    next_pre();
    PSTAMP_FLUSH(13);
}
#undef GLU_W
__device__ __forceinline__ void glu_consume(const Ctx& cx) {
    char* ws = cx.ws; const lds_ptr lds = cx.lds; const int wm = cx.w & 3, wn = cx.w >> 2;
    STAMP_DECL; STAMP(0);
    f32x4 acc[4][7];
    {
        const int lane = lane_id();
        const lds_ptr pa = lds + wm * 4096 + lane * 16, pw = lds + 16384 + wn * 7168 + lane * 16;
        bf16x8 af[4], wf[4];
#pragma unroll
        for (int n = 0; n < 7; ++n)
#pragma unroll
            for (int m = 0; m < 4; ++m) acc[m][n] = ZERO4;
        INPUT_BARRIER();
        if (cx.w == 1 || cx.w == 2) {
            int rbo, cto; tile128x64(cx.bid, rbo, cto);
            const int rl = (cx.w - 1) * 64 + lane;
            const f32x4 s = *(const LAS f32x4*)(lds + O_STAT + rl * 16);
            const float dm = s[0] - s[2];
            st8(ws + WS_ST2 + (size_t)(cto * 2048 + rbo * 128) * 8, (unsigned)rl * 8u, ((f32x2){0.5f * (s[0] + s[2]), s[1] + s[3] + 16.f * dm * dm}));
        }
        if (cx.w == 0) { int rp, ct; tile_glu(cx.bid, rp, ct); expect(ws, F_Y + rp * 32, lane < 32 ? lane : -1, cx.tag); }
        WAITV_BAR(0);
        STAMP(1);
#pragma unroll
        for (int m = 0; m < 4; ++m) { af[m] = ldsr128(pa + m * 1024); wf[m] = ldsr128(pw + m * 1024); }
        int sc = 0, sn = GSL;
#define LAD(p) __builtin_amdgcn_s_setprio(p)
#define DBG_PRE(U) do {} while (0)
#define DBG_POST(U) do {} while (0)
#define GW(U, n) wf[(7 * (U) + (n)) & 3]
#define GLU_STEP(U, DO_NEXT) do { \
            DBG_PRE(U); if (DO_NEXT) BARRIER(); DBG_POST(U); LAD(1); SBAR(); \
            const lds_ptr pwc = pw + sc, pwn = pw + sn, pan = pa + sn; \
            _Pragma("unroll") for (int m = 0; m < 4; ++m) { MFMA16_ACC(acc[m][0], GW(U, 0), af[m]); MFMA16_ACC(acc[m][1], GW(U, 1), af[m]); } \
            SBAR(); GW(U, 4) = ldsr128(pwc + 4096); GW(U, 5) = ldsr128(pwc + 5120); SBAR(); \
            _Pragma("unroll") for (int m = 0; m < 4; ++m) MFMA16_ACC(acc[m][2], GW(U, 2), af[m]); \
            SBAR(); GW(U, 6) = ldsr128(pwc + 6144); SBAR(); \
            _Pragma("unroll") for (int m = 0; m < 4; ++m) MFMA16_ACC(acc[m][3], GW(U, 3), af[m]); \
            SBAR(); if (DO_NEXT) GW(U, 7) = ldsr128(pwn); SBAR(); \
            _Pragma("unroll") for (int m = 0; m < 4; ++m) MFMA16_ACC(acc[m][4], GW(U, 4), af[m]); \
            SBAR(); if (DO_NEXT) GW(U, 8) = ldsr128(pwn + 1024); SBAR(); \
            _Pragma("unroll") for (int m = 0; m < 4; ++m) { MFMA16_ACC(acc[m][5], GW(U, 5), af[m]); MFMA16_ACC(acc[m][6], GW(U, 6), af[m]); \
                SBAR(); if (DO_NEXT) af[m] = ldsr128(pan + m * 1024); if (m == X_GDROP) LAD(0); SBAR(); } \
            if (DO_NEXT) { GW(U, 9) = ldsr128(pwn + 2048); GW(U, 10) = ldsr128(pwn + 3072); } SBAR(); \
            sc = sn; sn = sn == 4 * GSL ? 0 : sn + GSL; } while (0)
#pragma unroll 1
        for (int t = 0; t < 28; t += 4) { GLU_STEP(0, true); GLU_STEP(1, true); GLU_STEP(2, true); GLU_STEP(3, true); }
        GLU_STEP(0, true); GLU_STEP(1, true); GLU_STEP(2, true); GLU_STEP(3, false);
#undef GLU_STEP
#undef GW
    }
    asm volatile("s_nop 7\n\ts_nop 7" : "+v"(acc[0][6]), "+v"(acc[1][6]), "+v"(acc[2][6]), "+v"(acc[3][6]), "+v"(acc[0][5]), "+v"(acc[1][5]), "+v"(acc[2][5]), "+v"(acc[3][5]) :: "memory");
    SBAR();
    STAMP(2);
    const int lane = lane_id(), g = lane >> 4, gc = ((g & 1) << 1) | (g >> 1);
    int rp, ct; tile_glu(cx.bid, rp, ct);
    const char* hb = ws + WS_HH + ((size_t)(rp * 16 + wm * 4) * (F / 8) + ct * 14 + wn * 7) * 256;
    const unsigned vo = (lane & 15) * 16;
    constexpr unsigned RB = (F / 8) * 256;
    f32x2 rw[4];
#pragma unroll
    for (int m = 0; m < 4; ++m) rw[m] = *(const LAS f32x2*)(lds + G_ROW + (wm * 64 + m * 16 + (lane & 15)) * 8);
#define GLU_OUT(m, f, ug, tg, uu, tu, d0, d1) do { const f32x4 gt = acc[m][f] * rw[m][0] + (ug * rw[m][1] + tg), up = acc[m][3 + (f)] * rw[m][0] + (uu * rw[m][1] + tu); \
        d0 = cvtpk(fmaxf(gt[0], 0.f) * up[0], fmaxf(gt[1], 0.f) * up[1]); d1 = cvtpk(fmaxf(gt[2], 0.f) * up[2], fmaxf(gt[3], 0.f) * up[3]); } while (0)
    {
        const lds_ptr pc = lds + G_COL + (wn * 14 + g) * 16;
        f32x4 ct_[3][4];
#pragma unroll
        for (int f = 0; f < 2; ++f)
#pragma unroll
            for (int k = 0; k < 4; ++k) ct_[f][k] = *(const LAS f32x4*)(pc + f * 64 + k * 448);
#pragma unroll
        for (int m = 0; m < 4; ++m) {
            unsigned a0, a1, b0, b1;
            GLU_OUT(m, 0, ct_[0][0], ct_[0][1], ct_[0][2], ct_[0][3], a0, a1); GLU_OUT(m, 1, ct_[1][0], ct_[1][1], ct_[1][2], ct_[1][3], b0, b1);
            st16(hb, vo + m * RB + gc * 256, swap16(a0, a1, b0, b1));
            if (m == 1) { _Pragma("unroll") for (int k = 0; k < 4; ++k) ct_[2][k] = *(const LAS f32x4*)(pc + 128 + k * 448); }
            SBAR();
        }
#pragma unroll
        for (int mp = 0; mp < 2; ++mp) {
            unsigned a0, a1, b0, b1;
            GLU_OUT(2 * mp, 2, ct_[2][0], ct_[2][1], ct_[2][2], ct_[2][3], a0, a1); GLU_OUT(2 * mp + 1, 2, ct_[2][0], ct_[2][1], ct_[2][2], ct_[2][3], b0, b1);
            st16(hb, vo + (2 * mp + (g & 1)) * RB + (4 + (g >> 1)) * 256, swap16(a0, a1, b0, b1));
            SBAR();
        }
    }
#undef GLU_OUT
    {
        const lds_ptr pc = lds + G_COL + (g >> 1) * 896 + (wn * 14 + 12 + (g & 1)) * 16;
        const f32x4 uc = *(const LAS f32x4*)pc, tc = *(const LAS f32x4*)(pc + 448);
        unsigned ux[2][2];
#pragma unroll
        for (int mp = 0; mp < 2; ++mp) {
            const f32x4 a = acc[2 * mp][6] * rw[2 * mp][0] + (uc * rw[2 * mp][1] + tc), b = acc[2 * mp + 1][6] * rw[2 * mp + 1][0] + (uc * rw[2 * mp + 1][1] + tc);
            float hv[4];
#pragma unroll
            for (int r = 0; r < 4; ++r) {
                const auto x = __builtin_amdgcn_permlane32_swap(__float_as_uint(a[r]), __float_as_uint(b[r]), false, false);
                hv[r] = fmaxf(__uint_as_float(x[0]), 0.f) * __uint_as_float(x[1]);
            }
            ux[mp][0] = cvtpk(hv[0], hv[1]); ux[mp][1] = cvtpk(hv[2], hv[3]);
        }
        st16(hb, vo + gc * RB + 6 * 256, swap16(ux[0][0], ux[0][1], ux[1][0], ux[1][1]));
    }
    STAMP(3);
    BARRIER();
    drain_raise(ws, lds + G_COL + 1920, F_HH + rp * 32 + ct, cx.tag, lane);
    STAMP(4); STAMP_FLUSH(5);
}

#define DN_W(t, so) do { const char* w_ = Wb + (size_t)(t) * 16384; dmax(w_, voff, dW + (so)); dmax(w_ + 1024, voff, dW + (so) + 1024); dmax(w_ + 2048, voff, dW + (so) + 2048); dmax(w_ + 3072, voff, dW + (so) + 3072); } while (0)
__device__ __forceinline__ void down_pre(const Ctx& cx, OPre& o) {
    const int pw = cx.w, lane = lane_id(); int rb, ct, kh; tile_down(cx.bid, rb, ct, kh);
    const unsigned vo = (unsigned)(3072 + ct * 128 + (lane & 31) * 4) * 4u;
#pragma unroll
    for (int i = 0; i < 8; ++i) LD16C(o.p[i], cx.ws + WS_MP2, vo + i * 16384);
    const char* Wb = cx.ws + WS_WD + (size_t)(ct * 112 + kh * 56) * 8192 + pw * 4096;
    const unsigned voff = lane * 16, dW = cx.lds0 + 16384 + pw * 4096;
#pragma unroll
    for (int t = 0; t < 4; ++t) DN_W(t, t * 32768);
}
__device__ __forceinline__ void down_produce(const Ctx& cx, OPre& o) {
    char* ws = cx.ws; const int pw = cx.w, lane = lane_id(); int rb, ct, kh; tile_down(cx.bid, rb, ct, kh);
    const char* Ab = ws + WS_HH + ((size_t)(rb * 8 + 2 * pw) * (F / 8) + kh * 224) * 256;
    const char* Wb = ws + WS_WD + (size_t)(ct * 112 + kh * 56) * 8192 + pw * 4096;
    const unsigned voff = lane * 16, dA = cx.lds0 + pw * 4096, dW = cx.lds0 + 16384 + pw * 4096;
#define DN_A(t, so) do { const char* a_ = Ab + (size_t)(t) * 2048; dmax(a_, voff, dA + (so)); dmax(a_ + 1024, voff, dA + (so) + 1024); \
        dmax(a_ + (F / 8) * 256, voff, dA + (so) + 2048); dmax(a_ + (F / 8) * 256 + 1024, voff, dA + (so) + 3072); } while (0)
    if (pw == 0) expect(ws, F_HH + (rb >> 1) * 32 + kh * 16, lane < 2 ? lane : -1, cx.tag);
    INPUT_BARRIER();
    DN_A(0, 0); DN_A(1, 32768);
    WAITV_BAR(4);
    DN_A(2, 65536); DN_A(3, 98304);
    WAITV_BAR(8); DN_W(4, 131072); DN_A(4, 131072);
    f32x4 tv = rbf4(sum8(o.p)); PIN(tv);
    WAITV_BAR(12); DN_W(5, 0); DN_A(5, 0);
    int sf = 32768;
#pragma unroll 1
    for (int t = 2; t < 24; ++t) {
        WAITV_BAR(16);
        DN_W(t + 4, sf); DN_A(t + 4, sf);
        sf = sf == 131072 ? 0 : sf + 32768;
    }
    WAITV_BAR(16);
    {
        const char* xb = ws + WS_X1 + ((size_t)(rb * 8 + (pw >> 1) * 4 + kh * 2 + (pw & 1)) * 128 + ct * 16) * 256;
#pragma unroll
        for (int i = 0; i < 4; ++i) dmac(xb + i * 1024, voff, cx.lds0 + 98304 + pw * 4096 + i * 1024);
        if (pw == 0 && lane < 32) *(LAS f32x4*)(cx.lds + 114688 + lane * 16) = tv;
    }
    WAITV_BAR(12); WAITV_BAR(4); WAITVL_BAR(0);
#undef DN_A
}
#undef DN_W
__device__ __forceinline__ void down_consume(const Ctx& cx, u16* __restrict__ out) {
    char* ws = cx.ws; const lds_ptr lds = cx.lds; const int wid = cx.w, lane = lane_id(), wm = wid >> 2, wn = wid & 3, g = lane >> 4;
    STAMP_DECL; STAMP(0);
    const lds_ptr pa = lds + wm * 8192 + lane * 16, pw = lds + 16384 + wn * 2048 + lane * 16;
    f32x4 acc[4][2];
#pragma unroll
    for (int m = 0; m < 4; ++m) { acc[m][0] = ZERO4; acc[m][1] = ZERO4; }
    bf16x8 aA[8], wA[4], aB[8], wB[4];
    INPUT_BARRIER();
    if (wid == 0) { int rb, ct, kh; tile_down(cx.bid, rb, ct, kh); expect(ws, F_HH + (rb >> 1) * 32 + kh * 16, lane < 16 ? lane : -1, cx.tag); }
    BARRIER(); SBAR();
    STAMP(1);
#pragma unroll
    for (int ks = 0; ks < 2; ++ks) {
        wA[2 * ks] = ldsr128(pw + ks * 8192); wA[2 * ks + 1] = ldsr128(pw + ks * 8192 + 1024);
#pragma unroll
        for (int m = 0; m < 4; ++m) aA[4 * ks + m] = ldsr128(pa + m * 2048 + ks * 1024);
    }
    SBAR();
    int sn = 32768;
#define DN_STEP(AC, WC, AN, WN, DO_NEXT) do { \
        if (DO_NEXT) BARRIER(); LAD(1); SBAR(); \
        const lds_ptr pan = pa + sn, pwn = pw + sn; \
        _Pragma("unroll") for (int i = 0; i < 8; ++i) { const int ks = i >> 2, m = i & 3; \
            MFMA16_ACC(acc[m][0], WC[2 * ks], AC[i]); MFMA16_ACC(acc[m][1], WC[2 * ks + 1], AC[i]); SBAR(); \
            if (DO_NEXT) { if (m < 2) WN[2 * ks + m] = ldsr128(pwn + ks * 8192 + m * 1024); AN[i] = ldsr128(pan + m * 2048 + ks * 1024); } \
            if (i == X_HDROP) LAD(0); \
            SBAR(); } \
        sn = sn == 131072 ? 0 : sn + 32768; } while (0)
#pragma unroll 1
    for (int t = 0; t < 26; t += 2) {
        DN_STEP(aA, wA, aB, wB, true); DN_STEP(aB, wB, aA, wA, true);
    }
    DN_STEP(aA, wA, aB, wB, true); DN_STEP(aB, wB, aA, wA, false);
#undef DN_STEP
    asm volatile("s_nop 7\n\ts_nop 7" : "+v"(acc[0][0]), "+v"(acc[0][1]), "+v"(acc[1][0]), "+v"(acc[1][1]), "+v"(acc[2][0]), "+v"(acc[2][1]), "+v"(acc[3][0]), "+v"(acc[3][1]));
    SBAR();
    STAMP(2);
    int rb, ct, kh; tile_down(cx.bid, rb, ct, kh);
    f32x4 keep[2][2];
#pragma unroll
    for (int mm = 0; mm < 2; ++mm) {
        const f32x4 g0 = kh ? acc[mm][0] : acc[2 + mm][0], g1 = kh ? acc[mm][1] : acc[2 + mm][1];
        st16(ws + WS_XCH + ((size_t)cx.bid * 8 + wid) * 2048, lane * 16 + mm * 1024, ((u32x4){cvtpk(g0[0], g0[1]), cvtpk(g0[2], g0[3]), cvtpk(g1[0], g1[1]), cvtpk(g1[2], g1[3])}));
        keep[mm][0] = kh ? acc[2 + mm][0] : acc[mm][0]; keep[mm][1] = kh ? acc[2 + mm][1] : acc[mm][1];
    }
    BARRIER();
    u32x2 xr[2][2]; f32x4 gb[2];
#pragma unroll
    for (int nf = 0; nf < 2; ++nf) {
        gb[nf] = *(const LAS f32x4*)(lds + 114688 + (wn * 8 + nf * 4 + g) * 16);
#pragma unroll
        for (int mm = 0; mm < 2; ++mm) xr[mm][nf] = *(const LAS u32x2*)(lds + 98304 + ((wm * 2 + mm) * 16 + wn * 4 + nf * 2 + (g >> 1)) * 256 + (lane & 15) * 16 + 8 * (g & 1));
    }
    STAMP(5);
    u32x4 got[2];
    {
        const char* xi = ws + WS_XCH + ((size_t)(cx.bid ^ 1) * 8 + wid) * 2048;
        for (unsigned spins = 0; spins < (1u << 15); ++spins) {
            asm volatile("s_nop 4\n\tglobal_load_dwordx4 %0, %2, %3 sc1\n\tglobal_load_dwordx4 %1, %2, %3 offset:1024 sc1\n\ts_waitcnt vmcnt(0)" : "=&v"(got[0]), "=&v"(got[1]) : "v"(lane * 16), "s"(xi) : "memory");
            const bool ok = got[0][0] != ~0u && got[0][1] != ~0u && got[0][2] != ~0u && got[0][3] != ~0u && got[1][0] != ~0u && got[1][1] != ~0u && got[1][2] != ~0u && got[1][3] != ~0u;
            if (__all(ok)) break;
            __builtin_amdgcn_s_sleep(X_XS);
        }
    }
    STAMP(3);
#pragma unroll
    for (int mm = 0; mm < 2; ++mm) {
        unsigned u[2][2];
#pragma unroll
        for (int nf = 0; nf < 2; ++nf) {
            const f32x4 gg = gb[nf], k = keep[mm][nf];
            u[nf][0] = cvtpk(bflo(xr[mm][nf][0]) + gg[0] * (k[0] + bflo(got[mm][2 * nf])), bfhi(xr[mm][nf][0]) + gg[1] * (k[1] + bfhi(got[mm][2 * nf])));
            u[nf][1] = cvtpk(bflo(xr[mm][nf][1]) + gg[2] * (k[2] + bflo(got[mm][2 * nf + 1])), bfhi(xr[mm][nf][1]) + gg[3] * (k[3] + bfhi(got[mm][2 * nf + 1])));
        }
        st16(out + (size_t)(rb * 128 + wm * 64 + (2 * kh + mm) * 16) * D + ct * 128 + wn * 32, (unsigned)((lane & 15) * 2048 + (((g & 1) << 1) | (g >> 1)) * 16), swap16(u[0][0], u[0][1], u[1][0], u[1][1]));
    }
    STAMP(4); STAMP_FLUSH(6);
}

#define CTX_INIT extern __shared__ __attribute__((aligned(16))) char smem[]; \
    const int wid_ = uni(threadIdx.x >> 6); const bool prod = wid_ >= 8; \
    Ctx cx = {ws, (lds_ptr)smem, uni((unsigned)(uintptr_t)(lds_ptr)smem), (int)blockIdx.x, prod ? wid_ - 8 : wid_, 0ull}; \
    if (prod) __builtin_amdgcn_s_setprio(3)
__global__ __launch_bounds__(768, 3) void k_main(const u16* __restrict__ x, const u16* __restrict__ wq, const u16* __restrict__ wk, const u16* __restrict__ wv, const u16* __restrict__ wo,
                                                 const u16* __restrict__ wg, const u16* __restrict__ wu, const u16* __restrict__ wd, const u16* __restrict__ c, const u16* __restrict__ wmod,
                                                 char* __restrict__ ws, u16* __restrict__ out) {
    CTX_INIT;
    if (prod) {
        OPre o;
        ln1kv_produce(cx, wq, wk, wv, c, wmod, [&]() { attn_pre(cx); });
        attn_produce(cx, wo, wg, wu, wd, c, wmod, [&]() { oproj_pre(cx, o); });
        oproj_produce(cx, o, x, [&]() { glu_pre(cx); });
        glu_produce(cx, [&]() { down_pre(cx, o); });
        down_produce(cx, o);
    } else {
        ln1kv_consume(cx, x, c, wmod);
        attn_consume(cx);
        oproj_consume(cx);
        glu_consume(cx);
        down_consume(cx, out);
    }
}

extern "C" void kernel_launch(void* const* d_in, const int* in_sizes, int n_in, void* d_out, int out_size, void* d_ws, size_t ws_size, hipStream_t stream) {
    constexpr int LDS = 163840;
    (void)hipFuncSetAttribute((const void*)k_main, hipFuncAttributeMaxDynamicSharedMemorySize, LDS);
    k_main<<<256, 768, LDS, stream>>>((const u16*)d_in[0], (const u16*)d_in[1], (const u16*)d_in[2], (const u16*)d_in[3], (const u16*)d_in[4], (const u16*)d_in[6], (const u16*)d_in[7],
                                      (const u16*)d_in[8], (const u16*)d_in[9], (const u16*)d_in[10], (char*)d_ws, (u16*)d_out);
}
```

```cpp
#include <hip/hip_runtime.h>
#include <hip/hip_bf16.h>

#define LAS __attribute__((address_space(3)))
#define GAS __attribute__((address_space(1)))
typedef unsigned short u16;
typedef short bf16x8 __attribute__((ext_vector_type(8)));
typedef short s16x4 __attribute__((ext_vector_type(4)));
typedef float f32x2 __attribute__((ext_vector_type(2)));
typedef float f32x4 __attribute__((ext_vector_type(4)));
typedef float f32x16 __attribute__((ext_vector_type(16)));
typedef unsigned u32x4 __attribute__((ext_vector_type(4)));
typedef unsigned u32x2 __attribute__((ext_vector_type(2)));
typedef __bf16 bf16x2v __attribute__((ext_vector_type(2)));
typedef LAS char* lds_ptr;

constexpr int D = 1024, F = 3584;
constexpr size_t WS_CNT = 0;
constexpr int C_KV = 0, C_SIDE = 16, C_ST = 32, C_EARLY = 40, C_NUM = 56;
constexpr size_t WS_FLG = 1u << 20;
constexpr int F_H1 = 0, F_O = 256, F_Y = 512, F_HH = 768, F_WQ = 1024;
constexpr size_t WS_EPOCH = 98304 + 4096;
constexpr size_t WS_MP1 = 32768;
constexpr size_t WS_MP2 = 786432;
constexpr size_t WS_KN = 98304;
constexpr size_t WS_KV = 262144;
constexpr size_t WS_ST2 = 524288;
constexpr size_t WS_UT = (1u << 20) + 262144;
constexpr size_t WS_H1 = 2u << 20;
constexpr size_t WS_O = WS_H1 + (4u << 20);
constexpr size_t WS_X1 = WS_O + (4u << 20);
constexpr size_t WS_Y = WS_X1 + (4u << 20);
constexpr size_t WS_HH = WS_Y + (4u << 20);
constexpr size_t WS_XCH = WS_HH + (16u << 20);
constexpr size_t WS_WQ = WS_XCH + (4u << 20);
constexpr size_t WS_WO = WS_WQ + (2u << 20);
constexpr size_t WS_WGU = WS_WO + (2u << 20);
constexpr size_t WS_WD = WS_WGU + (16u << 20);

__device__ __forceinline__ float bf2f(u16 b) { return __uint_as_float(((unsigned)b) << 16); }
__device__ __forceinline__ float bflo(unsigned w) { return __uint_as_float(w << 16); }
__device__ __forceinline__ float bfhi(unsigned w) { return __uint_as_float(w & 0xffff0000u); }
__device__ __forceinline__ unsigned cvtpk(float lo, float hi) { return __builtin_bit_cast(unsigned, __builtin_convertvector((f32x2){lo, hi}, bf16x2v)); }
__device__ __forceinline__ float rbf(float x) { return bflo(cvtpk(x, x)); }
__device__ __forceinline__ f32x4 rbf4(f32x4 s) { return (f32x4){rbf(s[0]), rbf(s[1]), rbf(s[2]), rbf(s[3])}; }

__device__ __forceinline__ void dma16(const void* gbase, unsigned voff, unsigned ldsaddr) {
    asm volatile("s_mov_b32 m0, %0\n\ts_nop 3\n\tglobal_load_lds_dwordx4 %1, %2" ::"s"(ldsaddr), "v"(voff), "s"(gbase) : "memory");
}
__device__ __forceinline__ void dmac(const void* gbase, unsigned voff, unsigned ldsaddr) {
    asm volatile("s_mov_b32 m0, %0\n\ts_nop 3\n\tglobal_load_lds_dwordx4 %1, %2 sc1" ::"s"(ldsaddr), "v"(voff), "s"(gbase) : "memory");
}
#define LD16(dst, base, voff) asm volatile("s_nop 4\n\tglobal_load_dwordx4 %0, %1, %2" : "=v"(dst) : "v"(voff), "s"(base) : "memory")
#define LD16C(dst, base, voff) asm volatile("s_nop 4\n\tglobal_load_dwordx4 %0, %1, %2 sc1" : "=v"(dst) : "v"(voff), "s"(base) : "memory")
#define LD8C(dst, base, voff) asm volatile("s_nop 4\n\tglobal_load_dwordx2 %0, %1, %2 sc1" : "=v"(dst) : "v"(voff), "s"(base) : "memory")
#define TOUCH(reg, base, voff) asm volatile("s_nop 4\n\tglobal_load_dword %0, %1, %2 sc1" : "+v"(reg) : "v"(voff), "s"(base) : "memory")
template <class T> __device__ __forceinline__ void st16(const void* base, unsigned voff, T v) {
    asm volatile("s_nop 4\n\tglobal_store_dwordx4 %0, %1, %2 sc1\n\ts_nop 1" ::"v"(voff), "v"(v), "s"(base) : "memory");
}
template <class T> __device__ __forceinline__ void st8(const void* base, unsigned voff, T v) { asm volatile("s_nop 4\n\tglobal_store_dwordx2 %0, %1, %2 sc1" ::"v"(voff), "v"(v), "s"(base) : "memory"); }
__device__ __forceinline__ void st4(const void* base, unsigned voff, float v) { asm volatile("s_nop 4\n\tglobal_store_dword %0, %1, %2 sc1" ::"v"(voff), "v"(v), "s"(base) : "memory"); }
#define WAIT_V(n) asm volatile("s_waitcnt vmcnt(" #n ")" ::: "memory")
#define BARRIER() asm volatile("s_barrier" ::: "memory")
#define BAR_L() asm volatile("s_waitcnt lgkmcnt(0)\n\ts_barrier" ::: "memory")
#define WAITV_BAR(n) asm volatile("s_waitcnt vmcnt(" #n ")\n\ts_barrier" ::: "memory")
#define WAITVL_BAR(n) asm volatile("s_waitcnt vmcnt(" #n ") lgkmcnt(0)\n\ts_barrier" ::: "memory")
#define PIN(x) asm volatile("" : "+v"(x))
#define SBAR() __builtin_amdgcn_sched_barrier(0)
#define MFMA32(a, b, c) __builtin_amdgcn_mfma_f32_32x32x16_bf16(a, b, c, 0, 0, 0)
#define MFMA16(a, b, c) __builtin_amdgcn_mfma_f32_16x16x32_bf16(a, b, c, 0, 0, 0)
#define MFMA16_ACC(c, a, b) asm volatile("v_mfma_f32_16x16x32_bf16 %0, %1, %2, %0" : "+v"(c) : "v"(a), "v"(b))
#define MFMA32_ACC(c, a, b) asm volatile("v_mfma_f32_32x32x16_bf16 %0, %1, %2, %0" : "+v"(c) : "v"(a), "v"(b))
#define ZERO4 ((f32x4){0.f, 0.f, 0.f, 0.f})
__device__ __forceinline__ bf16x8 ldsr128(lds_ptr p) { return *(const LAS bf16x8*)p; }
__device__ __forceinline__ s16x4 ldstr(lds_ptr p) { return __builtin_amdgcn_ds_read_tr16_b64_v4i16((LAS s16x4*)p); }
__device__ __forceinline__ bf16x8 ldstr2(lds_ptr p, int d) { s16x4 a = ldstr(p), b = ldstr(p + d); return (bf16x8){a[0], a[1], a[2], a[3], b[0], b[1], b[2], b[3]}; }
__device__ __forceinline__ unsigned uni(unsigned x) { return (unsigned)__builtin_amdgcn_readfirstlane((int)x); }
__device__ __forceinline__ int lane_id() { int l = __builtin_amdgcn_mbcnt_hi(~0u, __builtin_amdgcn_mbcnt_lo(~0u, 0u)); PIN(l); return l; }
__device__ __forceinline__ u32x4 swap16(unsigned a0, unsigned a1, unsigned b0, unsigned b1) {
    const auto r0 = __builtin_amdgcn_permlane16_swap(a0, b0, false, false), r1 = __builtin_amdgcn_permlane16_swap(a1, b1, false, false);
    return (u32x4){r0[0], r1[0], r0[1], r1[1]};
}
__device__ __forceinline__ u32x4 swap32(unsigned a0, unsigned a1, unsigned b0, unsigned b1) {
    const auto r0 = __builtin_amdgcn_permlane32_swap(a0, b0, false, false), r1 = __builtin_amdgcn_permlane32_swap(a1, b1, false, false);
    return (u32x4){r0[0], r1[0], r0[1], r1[1]};
}


template <int CTRL> __device__ __forceinline__ float dppf(float v) { return __builtin_bit_cast(float, __builtin_amdgcn_mov_dpp(__builtin_bit_cast(int, v), CTRL, 0xf, 0xf, true)); }
__device__ __forceinline__ float half_sum(float v) { const auto r = __builtin_amdgcn_permlane32_swap(__float_as_uint(v), __float_as_uint(v), false, false); return __uint_as_float(r[0]) + __uint_as_float(r[1]); }
__device__ __forceinline__ float rows_sum(float v) {
    const auto r = __builtin_amdgcn_permlane16_swap(__float_as_uint(v), __float_as_uint(v), false, false); return half_sum(__uint_as_float(r[0]) + __uint_as_float(r[1]));
}
__device__ __forceinline__ float rows_max(float v) {
    auto r = __builtin_amdgcn_permlane16_swap(__float_as_uint(v), __float_as_uint(v), false, false); v = fmaxf(__uint_as_float(r[0]), __uint_as_float(r[1]));
    r = __builtin_amdgcn_permlane32_swap(__float_as_uint(v), __float_as_uint(v), false, false); return fmaxf(__uint_as_float(r[0]), __uint_as_float(r[1]));
}
__device__ __forceinline__ float row16_sum(float v) { v += dppf<0xB1>(v); v += dppf<0x4E>(v); v += dppf<0x141>(v); v += dppf<0x140>(v); return v; }
__device__ __forceinline__ float row16_max(float v) { v = fmaxf(v, dppf<0xB1>(v)); v = fmaxf(v, dppf<0x4E>(v)); v = fmaxf(v, dppf<0x141>(v)); v = fmaxf(v, dppf<0x140>(v)); return v; }
__device__ __forceinline__ float wsum(float v) { return rows_sum(row16_sum(v)); }
__device__ __forceinline__ float wmax(float v) { return rows_max(row16_max(v)); }
__device__ __forceinline__ f32x4 sum8(f32x4 (&p)[8]) {
    asm volatile("" : "+v"(p[0]), "+v"(p[1]), "+v"(p[2]), "+v"(p[3]), "+v"(p[4]), "+v"(p[5]), "+v"(p[6]), "+v"(p[7]));
    return ((p[0] + p[1]) + (p[2] + p[3])) + ((p[4] + p[5]) + (p[6] + p[7]));
}

__device__ __forceinline__ void signal(char* ws, int idx, int) {
    unsigned long long sv;
    asm volatile("s_mov_b64 %0, exec\n\ts_mov_b64 exec, 1\n\ts_nop 2\n\tglobal_atomic_add %1, %2, %3\n\ts_mov_b64 exec, %0" : "=&s"(sv) : "v"(idx * 256), "v"(1u), "s"(ws + WS_CNT) : "memory");
}
template <int SLEEP>
__device__ __forceinline__ void await(char* ws, int idx, int n, unsigned need, int lane) {
    {
        GAS unsigned* p = (GAS unsigned*)(ws + WS_CNT) + (idx + (lane < n ? lane : 0)) * 64;
        for (unsigned spins = 0; spins < (1u << 15); ++spins) {
            if (__all(__hip_atomic_load(p, __ATOMIC_RELAXED, __HIP_MEMORY_SCOPE_AGENT) >= need)) break;
            __builtin_amdgcn_s_sleep(SLEEP);
        }
        asm volatile("s_waitcnt vmcnt(0)" ::: "memory");
    }
}
__device__ __forceinline__ void raise(char* ws, int idx, unsigned long long tag, int) {
    unsigned long long sv;
    asm volatile("s_mov_b64 %0, exec\n\ts_mov_b64 exec, 1\n\ts_nop 2\n\tglobal_store_dwordx2 %1, %2, %3 sc1\n\ts_mov_b64 exec, %0" : "=&s"(sv) : "v"(idx * 128), "v"(tag), "s"(ws + WS_FLG) : "memory");
}
__device__ __forceinline__ void expect(char* ws, int idx0, int off, unsigned long long tag) {
    GAS unsigned long long* p = (GAS unsigned long long*)(ws + WS_FLG) + (idx0 + (off < 0 ? 0 : off)) * 16;
    for (unsigned spins = 0; spins < (1u << 15); ++spins) {
        if (__all(off < 0 || __hip_atomic_load(p, __ATOMIC_RELAXED, __HIP_MEMORY_SCOPE_AGENT) == tag)) break;
        __builtin_amdgcn_s_sleep(2);
    }
    asm volatile("s_waitcnt vmcnt(0)" ::: "memory");
}
__device__ __forceinline__ void drain_raise(char* ws, lds_ptr word, int idx, unsigned long long tag, int lane) {
    asm volatile("s_waitcnt vmcnt(0)" ::: "memory");
    unsigned old = 0u;
    if (lane == 0) old = __hip_atomic_fetch_add((LAS unsigned*)word, 1u, __ATOMIC_RELAXED, __HIP_MEMORY_SCOPE_WORKGROUP);
    if (uni(old) == 7u) raise(ws, idx, tag, lane);
}
#define INPUT_BARRIER() BAR_L()

__device__ __forceinline__ void pack_load(const char* src  , unsigned pitch, unsigned ldsaddr, int lane) {
    const unsigned vo = (unsigned)(lane >> 3) * pitch, sw = (lane & 7) ^ (((lane >> 4) & 1) << 1);
#pragma unroll
    for (int i = 0; i < 4; ++i) dma16(src + (size_t)i * 8 * pitch, vo + ((sw ^ ((i & 1) << 2)) << 4), ldsaddr + i * 1024);
}
__device__ __forceinline__ bf16x8 pack_frag(lds_ptr area, int nf, int lane) {
    const int g = lane >> 4, q = (lane & 15) >> 2;
    return ldstr2(area + (8 * g + q) * 128 + ((nf ^ (((q >> 1) & 1) | ((g & 1) << 1))) << 5) + (lane & 3) * 8, 512);
}
__device__ __forceinline__ void mod_unit(lds_ptr area, bf16x8 sc, lds_ptr red, int lane) {
    f32x4 d[4];
#pragma unroll
    for (int nf = 0; nf < 4; ++nf) d[nf] = MFMA16(pack_frag(area, nf, lane), sc, ZERO4);
    if ((lane & 15) == 0) {
#pragma unroll
        for (int nf = 0; nf < 4; ++nf) *(LAS f32x4*)(red + nf * 64 + (lane >> 4) * 16) = d[nf];
    }
}

__device__ __forceinline__ float silu_bf(float cv) { return rbf(cv / (1.f + __expf(-cv))); }
__device__ __forceinline__ bf16x8 silu_frag(u32x4 cw) {
    PIN(cw);
    u32x4 r;
#pragma unroll
    for (int j = 0; j < 4; ++j) r[j] = cvtpk(silu_bf(bflo(cw[j])), silu_bf(bfhi(cw[j])));
    return __builtin_bit_cast(bf16x8, r);
}

__device__ __forceinline__ void tile128x64(int bid, int& rb, int& ct) { const int xcd = bid & 7, idx = bid >> 3; rb = (xcd >> 1) * 4 + (idx & 3); ct = (xcd & 1) * 8 + (idx >> 2); }
__device__ __forceinline__ unsigned long long epoch_load(char* ws) { return __hip_atomic_load((GAS unsigned long long*)(ws + WS_EPOCH), __ATOMIC_RELAXED, __HIP_MEMORY_SCOPE_AGENT); }
__device__ __forceinline__ unsigned long long epoch_tag(unsigned long long e) { return ((((unsigned long long)uni((unsigned)(e >> 32)) << 32) | uni((unsigned)e)) + 1ull) << 24; }
__device__ __forceinline__ void tile_down(int bid, int& rb, int& ct, int& kh) { const int xcd = bid & 7, idx = bid >> 3; kh = xcd & 1; rb = (xcd >> 1) * 4 + (idx & 3); ct = idx >> 2; }
struct Ctx { char* ws; lds_ptr lds; unsigned lds0; int bid, w; unsigned long long tag;
};

__device__ __forceinline__ void ln1kv_consume(Ctx& cx, const u16* __restrict__ x, const u16* __restrict__ c, const u16* __restrict__ wmod) {
    char* ws = cx.ws; const lds_ptr lds = cx.lds; const int wid = cx.w, lane = lane_id(), tid = wid * 64 + lane;
    const int rb = (cx.bid >> 4) * 8 + (cx.bid & 7), isv = (cx.bid >> 3) & 1;
    const int idx = cx.bid >> 3, cs = ((((idx & 3) << 3) | (cx.bid & 7)) << 1) | ((idx >> 2) & 1), kq = idx >> 3, kr = kq * 256 + (tid >> 2);
    const u32x4 w0 = *(const u32x4*)(wmod + (size_t)kr * 6144 + cs * 32 + (tid & 3) * 8), w1 = *(const u32x4*)(wmod + (size_t)(kr + 128) * 6144 + cs * 32 + (tid & 3) * 8);
    const u16 c0 = c[kr], c1 = c[kr + 128];
    const unsigned long long ep = epoch_load(ws);
    u32x4 xv[2][2];
#pragma unroll
    for (int rr = 0; rr < 2; ++rr)
#pragma unroll
        for (int hh = 0; hh < 2; ++hh) xv[rr][hh] = *(const u32x4*)(x + (size_t)(rb * 16 + 2 * wid + rr) * D + hh * 512 + lane * 8);
    {
        const float s0 = silu_bf(bf2f(c0)), s1 = silu_bf(bf2f(c1));
        float a[8];
#pragma unroll
        for (int j = 0; j < 4; ++j) { a[2 * j] = s0 * bflo(w0[j]) + s1 * bflo(w1[j]); a[2 * j + 1] = s0 * bfhi(w0[j]) + s1 * bfhi(w1[j]); }
#pragma unroll
        for (int j = 0; j < 8; ++j) { a[j] += dppf<0x124>(a[j]); a[j] += dppf<0x128>(a[j]); a[j] = rows_sum(a[j]); }
        cx.tag = epoch_tag(ep);
        asm volatile("" : "+s"(cx.tag));
        if (lane < 4) { *(LAS f32x4*)(lds + 114688 + wid * 128 + lane * 32) = (f32x4){a[0], a[1], a[2], a[3]}; *(LAS f32x4*)(lds + 114688 + wid * 128 + lane * 32 + 16) = (f32x4){a[4], a[5], a[6], a[7]}; }
    }
    BAR_L();
    const unsigned long long tag = cx.tag;
    if (wid == 0 && lane < 32) {
        const LAS float* r = (const LAS float*)(lds + 114688) + lane;
        const unsigned pv = __float_as_uint(((r[0] + r[32]) + (r[64] + r[96])) + ((r[128] + r[160]) + (r[192] + r[224])));
        __hip_atomic_store((GAS unsigned long long*)(ws + WS_MP1) + (kq * 2048 + ((cs * 32 + 1024) & 2047) + lane), tag | ((pv + 0x80u) >> 8), __ATOMIC_RELAXED, __HIP_MEMORY_SCOPE_AGENT);
    }
    float xn[2][16];
#pragma unroll
    for (int rr = 0; rr < 2; ++rr) {
        float* v = xn[rr];
#pragma unroll
        for (int hh = 0; hh < 2; ++hh)
#pragma unroll
            for (int j = 0; j < 4; ++j) { v[hh * 8 + 2 * j] = bflo(xv[rr][hh][j]); v[hh * 8 + 2 * j + 1] = bfhi(xv[rr][hh][j]); }
        float sm = 0.f;
#pragma unroll
        for (int j = 0; j < 16; ++j) sm += v[j];
        const float mean = wsum(sm) * (1.f / 1024.f);
        float q = 0.f;
#pragma unroll
        for (int j = 0; j < 16; ++j) { v[j] -= mean; q += v[j] * v[j]; }
        const float rstd = 1.f / sqrtf(wsum(q) * (1.f / 1024.f) + 1e-5f);
#pragma unroll
        for (int j = 0; j < 16; ++j) { v[j] *= rstd; PIN(v[j]); }
    }
    LAS float* tab = (LAS float*)(lds + 98304);
    {
        u32x4 g[4][2];
        const unsigned tl = (unsigned)tag, th = (unsigned)(tag >> 32);
        __builtin_amdgcn_s_sleep(18);
        for (unsigned spins = 0; spins < (1u << 15); ++spins) {
            asm volatile("s_nop 4\n\tglobal_load_dwordx4 %0, %8, %12 sc1\n\tglobal_load_dwordx4 %1, %8, %13 sc1\n\tglobal_load_dwordx4 %2, %9, %12 sc1\n\tglobal_load_dwordx4 %3, %9, %13 sc1\n\t"
                         "global_load_dwordx4 %4, %10, %12 sc1\n\tglobal_load_dwordx4 %5, %10, %13 sc1\n\tglobal_load_dwordx4 %6, %11, %12 sc1\n\tglobal_load_dwordx4 %7, %11, %13 sc1\n\ts_waitcnt vmcnt(0)"
                         : "=&v"(g[0][0]), "=&v"(g[0][1]), "=&v"(g[1][0]), "=&v"(g[1][1]), "=&v"(g[2][0]), "=&v"(g[2][1]), "=&v"(g[3][0]), "=&v"(g[3][1])
                         : "v"(tid * 16), "v"(tid * 16 + 16384), "v"(tid * 16 + 32768), "v"(tid * 16 + 49152), "s"(ws + WS_MP1), "s"(ws + WS_MP1 + 8192) : "memory");
            unsigned bad = 0u;
#pragma unroll
            for (int k = 0; k < 4; ++k)
                bad |= ((g[k][0][0] ^ tl) | (g[k][0][2] ^ tl) | (g[k][1][0] ^ tl) | (g[k][1][2] ^ tl)) >> 24 | (g[k][0][1] ^ th) | (g[k][0][3] ^ th) | (g[k][1][1] ^ th) | (g[k][1][3] ^ th);
            if (__all(bad == 0u)) break;
            __builtin_amdgcn_s_sleep(8);
        }
        f32x4 v;
#pragma unroll
        for (int j = 0; j < 4; ++j)
            v[j] = (__uint_as_float(g[0][j >> 1][(j & 1) * 2] << 8) + __uint_as_float(g[1][j >> 1][(j & 1) * 2] << 8)) + (__uint_as_float(g[2][j >> 1][(j & 1) * 2] << 8) + __uint_as_float(g[3][j >> 1][(j & 1) * 2] << 8));
        v = rbf4(v);
        *(LAS f32x2*)(tab + tid * 2) = (f32x2){rbf(1.f + v[0]), rbf(1.f + v[1])};
        *(LAS f32x2*)(tab + 1024 + tid * 2) = (f32x2){v[2], v[3]};
    }
    BAR_L();
    if (cx.bid == 0 && tid == 0) __hip_atomic_store((GAS unsigned long long*)(ws + WS_EPOCH), tag >> 24, __ATOMIC_RELAXED, __HIP_MEMORY_SCOPE_AGENT);
    const lds_ptr img = lds + 65536;
#pragma unroll
    for (int hh = 0; hh < 2; ++hh) {
        const int c = hh * 64 + lane;
        const f32x4 g0 = *(const LAS f32x4*)(tab + c * 8), g1 = *(const LAS f32x4*)(tab + c * 8 + 4);
        const f32x4 b0 = *(const LAS f32x4*)(tab + 1024 + c * 8), b1 = *(const LAS f32x4*)(tab + 1024 + c * 8 + 4);
#pragma unroll
        for (int rr = 0; rr < 2; ++rr) {
            float h[8];
#pragma unroll
            for (int j = 0; j < 4; ++j) { h[j] = xn[rr][hh * 8 + j] * g0[j] + b0[j]; h[4 + j] = xn[rr][hh * 8 + 4 + j] * g1[j] + b1[j]; }
            *(LAS u32x4*)(img + c * 256 + (((2 * wid + rr) ^ (c & 15)) << 4)) = (u32x4){cvtpk(h[0], h[1]), cvtpk(h[2], h[3]), cvtpk(h[4], h[5]), cvtpk(h[6], h[7])};
        }
    }
    BAR_L();
    f32x4 acc[2] = {ZERO4, ZERO4};
    const int g = lane >> 4, q = (lane & 15) >> 2, p = lane & 3;
    bf16x8 hf[4], wf[4][2];
#pragma unroll
    for (int i = 0; i < 4; ++i) { const int cc = 4 * (wid * 4 + i) + g; hf[i] = ldsr128(img + cc * 256 + (((lane & 15) ^ (cc & 15)) << 4)); }
    const char* hb = ws + WS_H1 + ((size_t)rb * 128 + 16 * wid) * 256;
    if (isv) { st16(hb, lane * 16 + 1024, hf[1]); st16(hb, lane * 16 + 3072, hf[3]); } else { st16(hb, lane * 16, hf[0]); st16(hb, lane * 16 + 2048, hf[2]); }
#pragma unroll
    for (int i = 0; i < 4; ++i)
#pragma unroll
        for (int nf = 0; nf < 2; ++nf) wf[i][nf] = ldstr2(lds + (32 * (wid * 4 + i) + 8 * g + q) * 64 + ((nf ^ (g & 1)) << 5) + 8 * p, 256);
    LAS float* red = (LAS float*)(lds + 147456);
    if (isv) {
#pragma unroll
        for (int i = 0; i < 4; ++i) { acc[0] = MFMA16(hf[i], wf[i][0], acc[0]); acc[1] = MFMA16(hf[i], wf[i][1], acc[1]); }
#pragma unroll
        for (int nf = 0; nf < 2; ++nf) *(LAS f32x4*)(red + wid * 512 + (nf * 16 + (lane & 15)) * 16 + 4 * g) = acc[nf];
    } else {
#pragma unroll
        for (int i = 0; i < 4; ++i) { acc[0] = MFMA16(wf[i][0], hf[i], acc[0]); acc[1] = MFMA16(wf[i][1], hf[i], acc[1]); }
#pragma unroll
        for (int nf = 0; nf < 2; ++nf) *(LAS f32x4*)(red + wid * 512 + (lane & 15) * 32 + nf * 16 + 4 * g) = acc[nf];
    }
    WAITVL_BAR(0);
    if (wid == 0) {
        const int o0 = isv ? (lane & 31) * 16 + 4 * (lane >> 5) : (lane & 15) * 32 + (g >> 1) * 16 + (g & 1) * 4, o1 = o0 + 8;
        f32x4 a = ZERO4, b = ZERO4;
#pragma unroll
        for (int w = 0; w < 8; ++w) { a += *(const LAS f32x4*)(red + w * 512 + o0); b += *(const LAS f32x4*)(red + w * 512 + o1); }
        const u32x4 v = {cvtpk(a[0], a[1]), cvtpk(a[2], a[3]), cvtpk(b[0], b[1]), cvtpk(b[2], b[3])};
        st16(ws + WS_KV + (size_t)(rb >> 1) * 4096, isv ? 2048 + (rb & 1) * 1024 + lane * 16 : (g >> 1) * 1024 + ((g & 1) * 32 + (rb & 1) * 16 + (lane & 15)) * 16, v);
        float n2 = 0.f, mx = 0.f;
#pragma unroll
        for (int j = 0; j < 4; ++j) { n2 += bflo(v[j]) * bflo(v[j]) + bfhi(v[j]) * bfhi(v[j]); mx = fmaxf(mx, fmaxf(fabsf(bflo(v[j])), fabsf(bfhi(v[j])))); }
        if (!isv) mx = rows_sum(n2);
        if (!(n2 - n2 == 0.f)) mx = INFINITY;
        mx = wmax(mx);
        if (lane == 0) st4(ws + WS_KN, (unsigned)(isv * 128 + rb) * 4u, mx);
    }
    WAITV_BAR(0);
}
template <class Next>
__device__ __forceinline__ void ln1kv_produce(Ctx& cx, const u16* __restrict__ wq, const u16* __restrict__ wk, const u16* __restrict__ wv, const u16* __restrict__ c, const u16* __restrict__ wmod, Next next_pre) {
    char* ws = cx.ws; const int pw = cx.w, lane = lane_id();
    cx.tag = epoch_tag(epoch_load(ws));
    asm volatile("" : "+s"(cx.tag));
    int rbq, hp; tile128x64(cx.bid, rbq, hp);
    if (cx.bid == 0 && pw == 2) { if (lane < C_NUM) st4(ws + WS_CNT, (unsigned)lane * 256u, 0.f); WAIT_V(0); }
    if (pw < 2) { pack_load((const char*)wq + (size_t)(rbq * 2 + pw) * 65536 + hp * 128, 2048, cx.lds0 + 106496 + pw * 4096, lane); }
    BARRIER();
    {
        const char* w = (const char*)(((cx.bid >> 3) & 1) ? wv : wk) + pw * 16384;
        const unsigned voff = (lane >> 2) * 64 + (((lane & 3) ^ (((lane >> 5) & 1) << 1)) << 4);
#pragma unroll
        for (int i = 0; i < 16; ++i) dma16(w + i * 1024, voff, cx.lds0 + pw * 16384 + i * 1024);
    }
    const int k0 = (cx.bid >> 5) * 128 + pw * 32, col2 = 1024 + (cx.bid & 31) * 64;
    u32x4 cw;
    pack_load((const char*)wmod + (size_t)k0 * 12288 + (2048 + col2) * 2, 12288, cx.lds0 + 126976 + pw * 4096, lane);
    LD16(cw, c, (unsigned)(k0 + (lane >> 4) * 8) * 2u);
    if (pw < 2) {
        WAIT_V(21);
#pragma unroll
        for (int nf = 0; nf < 4; ++nf) st16(ws + WS_WQ + (size_t)(hp * 32 + rbq * 2 + pw) * 4096, lane * 16 + nf * 1024, pack_frag(cx.lds + 106496 + pw * 4096, nf, lane));
    }
    WAIT_V(0);
    mod_unit(cx.lds + 126976 + pw * 4096, silu_frag(cw), cx.lds + 143360 + pw * 256, lane);
    BAR_L();
    if (pw == 0) {
        raise(ws, F_WQ + hp * 16 + rbq, cx.tag, lane);
        const LAS float* r = (const LAS float*)(cx.lds + 143360) + lane;
        st4(ws + WS_MP2, (unsigned)((cx.bid >> 5) * 4096 + col2 + lane) * 4u, (r[0] + r[64]) + (r[128] + r[192]));
    }
    BARRIER();
    expect(ws, F_WQ + hp * 16, lane < 16 ? lane : -1, cx.tag);
    BARRIER();
    if (pw == 0) raise(ws, F_H1 + cx.bid, cx.tag, lane);
    next_pre();
}

template <int WB, int NPRE>
__device__ __forceinline__ void g64_pre(const Ctx& cx, const char* Wp, int lane) {
#pragma unroll
    for (int t = 0; t < NPRE; ++t) { const unsigned d = cx.lds0 + (WB + t * 8192) % 163840 + cx.w * 2048; dmac(Wp + cx.w * 2048 + t * 8192, lane * 16, d); dmac(Wp + cx.w * 2048 + t * 8192 + 1024, lane * 16, d + 1024); }
}
template <int WB, int AB, int NPRE, int NTAIL, class Issued, class First, class Rest, class Tail>
__device__ __forceinline__ void g64_produce(const Ctx& cx, const char* Ap, const char* Wp, int lane, Issued issued, First first, Rest rest, Tail tail) {
    const char* Aw = Ap + (size_t)(2 * cx.w) * 32768; const char* Ww = Wp + cx.w * 2048;
    const unsigned voff = lane * 16, dA = cx.lds0 + AB + cx.w * 4096, dW = cx.lds0 + cx.w * 2048;
#define G64_A(t) do { const char* a_ = Aw + (t) * 2048; const unsigned d_ = dA + ((t) % 5) * 16384; dmac(a_, voff, d_); dmac(a_ + 32768, voff, d_ + 2048); dmac(a_ + 1024, voff, d_ + 1024); dmac(a_ + 33792, voff, d_ + 3072); } while (0)
#define G64_W(t, slot) do { const unsigned d_ = dW + (WB + (slot) * 8192) % 163840; dmac(Ww + (t) * 8192, voff, d_); dmac(Ww + (t) * 8192 + 1024, voff, d_ + 1024); } while (0)
    G64_A(0); G64_A(1);
    issued();
    WAITV_BAR(4);
    G64_A(2); G64_A(3); G64_A(4);
    constexpr int E = (10 - NPRE) * 2;
    if (NPRE == 6) { G64_W(6, 6); G64_W(7, 7); G64_W(8, 8); G64_W(9, 9); }
    first();
#define G64_BAR(n) asm volatile("s_waitcnt vmcnt(%0)\n\ts_barrier" ::"i"(n) : "memory")
    G64_BAR(12 + E);
    rest();
    G64_BAR(8 + E); G64_A(5); G64_W(10, 0);
    G64_BAR(10 + E); G64_A(6); G64_W(11, 1);
    G64_BAR(12 + E); G64_A(7); G64_W(12, 2);
    G64_BAR(14); G64_A(8); G64_W(13, 3);
    G64_BAR(14); G64_A(9);
    G64_BAR(12); G64_A(10);
    G64_BAR(10); G64_A(11);
    G64_BAR(8); G64_A(12);
    tail();
    G64_BAR(8 + NTAIL); G64_A(13); G64_W(14, 8);
    G64_BAR(10 + NTAIL); G64_A(14); G64_W(15, 9);
    G64_BAR(12 + NTAIL); G64_A(15);
    G64_BAR(12);
    G64_BAR(6);
    WAITVL_BAR(0);
#undef G64_BAR
#undef G64_W
#undef G64_A
}
template <int WB, int AB, class Start, class Mid, class Late, class St>
__device__ __forceinline__ void g64_consume(f32x16& acc, const Ctx& cx, int lane, Start start, Mid mid, Late late, St stamp) {
    const int lo = (lane >> 5) * 256 + (lane & 15) * 16;
    const lds_ptr pa = cx.lds + AB + (cx.w & 3) * 4096 + ((lane >> 4) & 1) * 2048 + lo, pw = cx.lds + (cx.w >> 2) * 2048 + ((lane >> 4) & 1) * 1024 + lo;
    acc = (f32x16){};
    bf16x8 aA[4], wA[4], aB[4], wB[4];
    start();
    BARRIER(); SBAR();
    stamp();
#pragma unroll
    for (int k = 0; k < 4; ++k) { wA[k] = ldsr128(pw + WB + (k >> 1) * 4096 + (k & 1) * 512); aA[k] = ldsr128(pa + k * 512); }
    SBAR();
    int sa = 16384, sw = WB + 8192;
#define G64_STEP(AC, WC, AN, WN, DO_NEXT, HOOK) do { \
        if (DO_NEXT) BARRIER(); SBAR(); HOOK; \
        const lds_ptr pwn = pw + sw, pan = pa + sa; \
        MFMA32_ACC(acc, WC[0], AC[0]); SBAR(); \
        if (DO_NEXT) { WN[0] = ldsr128(pwn); AN[0] = ldsr128(pan); WN[1] = ldsr128(pwn + 512); } SBAR(); \
        MFMA32_ACC(acc, WC[1], AC[1]); SBAR(); \
        if (DO_NEXT) { AN[1] = ldsr128(pan + 512); WN[2] = ldsr128(pwn + 4096); AN[2] = ldsr128(pan + 1024); } SBAR(); \
        MFMA32_ACC(acc, WC[2], AC[2]); SBAR(); \
        if (DO_NEXT) { WN[3] = ldsr128(pwn + 4608); AN[3] = ldsr128(pan + 1536); } SBAR(); \
        MFMA32_ACC(acc, WC[3], AC[3]); SBAR(); \
        sa = sa == 65536 ? 0 : sa + 16384; sw = sw == (WB + 9 * 8192) % 163840 ? WB : (sw == 155648 ? 0 : sw + 8192); } while (0)
#define G64_PAIR() do { G64_STEP(aA, wA, aB, wB, true, (void)0); G64_STEP(aB, wB, aA, wA, true, (void)0); } while (0)
#pragma unroll 1
    for (int t = 0; t < 4; t += 2) G64_PAIR();
    mid(4);
#pragma unroll 1
    for (int t = 4; t < 8; t += 2) G64_PAIR();
    mid(8);
    G64_PAIR();
    mid(10);
    G64_PAIR();
    G64_STEP(aA, wA, aB, wB, true, (void)0);
    sw = (WB + 8 * 8192) % 163840;
    G64_STEP(aB, wB, aA, wA, true, (void)0);
    G64_STEP(aA, wA, aB, wB, true, late());
    G64_STEP(aB, wB, aA, wA, false, (void)0);
    asm volatile("s_nop 7\n\ts_nop 7" : "+v"(acc));
    SBAR();
#undef G64_PAIR
#undef G64_STEP
}

__device__ __forceinline__ void attn_pre(const Ctx& cx) { int rb, hp; tile128x64(cx.bid, rb, hp); g64_pre<0, 6>(cx, cx.ws + WS_WQ + (size_t)hp * 131072, lane_id()); }
template <class Next>
__device__ __forceinline__ void attn_produce(const Ctx& cx, const u16* __restrict__ wo, const u16* __restrict__ wg, const u16* __restrict__ wu, const u16* __restrict__ wd, const u16* __restrict__ c, const u16* __restrict__ wmod, Next next_pre) {
    char* ws = cx.ws; const int pw = cx.w, lane = lane_id();
    int rb, hp; tile128x64(cx.bid, rb, hp);
    const char* kvg = ws + WS_KV + pw * 8192;
    const unsigned voff = lane * 16, kvd = cx.lds0 + pw * 8192;
#define KV_STAGE(st, so) do { _Pragma("unroll") for (int i_ = 0; i_ < 8; ++i_) dmac(kvg + (st) * 32768 + i_ * 1024, voff, kvd + (so) + i_ * 1024); } while (0)
    expect(ws, F_H1 + rb * 16, lane < 16 ? lane : -1, cx.tag);
    if (pw == 0) signal(ws, C_SIDE + (cx.bid & 15), lane);
    g64_produce<0, 81920, 6, 8>(cx, ws + WS_H1 + (size_t)rb * 8 * 32768, ws + WS_WQ + (size_t)hp * 131072, lane,
                    [&]() { BARRIER(); if (pw == 0) signal(ws, C_KV + (cx.bid & 15), lane); },
                    [&]() { }, []() {},
                    [&]() { KV_STAGE(0, 32768); });
    BARRIER();
    KV_STAGE(1, 65536);
    const int ao = (pw & 1) * 16384 + (pw >> 1) * 131072;
    const lds_ptr area = cx.lds + ao; const unsigned area0 = cx.lds0 + ao;
    const int k0 = (cx.bid >> 5) * 128 + pw * 32, col2 = (cx.bid & 15) * 64 + ((cx.bid >> 4) & 1) * 3072;
    const int gw = cx.bid * 4 + pw, isgu = gw < 896, mat = gw >= 448, k8 = (gw - mat * 448) / 56, cg = gw - mat * 448 - k8 * 56;
    u32x4 cw; f32x4 vp[8];
    pack_load((const char*)wmod + (size_t)k0 * 12288 + (2048 + col2) * 2, 12288, area0, lane);
    LD16(cw, c, (unsigned)(k0 + (lane >> 4) * 8) * 2u);
    auto unit_load = [&](int j) {
        const unsigned dst = area0 + 8192 + (j & 1) * 4096;
        if (!isgu) { const int u = (gw - 896) * 4 + (j & 3); pack_load((const char*)wo + (size_t)(u >> 4) * 65536 + (u & 15) * 128, 2048, dst, lane); }
        else if (j < 4) pack_load((const char*)(mat ? wu : wg) + (size_t)(k8 * 4 + j) * 32 * 7168 + cg * 128, 7168, dst, lane);
        else { const int u = (j - 4) * 896 + gw; pack_load((const char*)wd + (size_t)(u >> 4) * 65536 + (u & 15) * 128, 2048, dst, lane); }
    };
    f32x4 ut[4] = {ZERO4, ZERO4, ZERO4, ZERO4};
    unsigned guo[4];
#pragma unroll
    for (int nf = 0; nf < 4; ++nf) {
        const int cj = cg * 8 + 2 * nf + ((lane >> 3) & 1), ct = cj / 14, r = cj - ct * 14, wn = r >= 7, c = r - wn * 7;
        const int fr = c < 6 ? mat * 3 + (c >> 1) : 6, hf = c < 6 ? (c & 1) : mat;
        guo[nf] = (unsigned)((ct * 32 * 14 + wn * 7 + fr) * 1024 + (lane >> 4) * 256 + (hf * 8 + (lane & 7)) * 16);
        PIN(guo[nf]);
    }
    auto unit_store = [&](int j) {
        const lds_ptr src = area + 8192 + (j & 1) * 4096;
        bf16x8 f[4];
#pragma unroll
        for (int nf = 0; nf < 4; ++nf) f[nf] = pack_frag(src, nf, lane);
        if (!isgu || j >= 4) {
            const int u = isgu ? (j - 4) * 896 + gw : (gw - 896) * 4 + (j & 3);
            const char* d = isgu ? ws + WS_WD + (size_t)((((u & 15) >> 1) * 112 + (u >> 4)) * 2 + (u & 1)) * 4096 : (j < 4 ? ws + WS_WO + (size_t)((u & 15) * 32 + (u >> 4)) * 4096 : ws + WS_UT + 458752 + (gw & 15) * 4096);
#pragma unroll
            for (int nf = 0; nf < 4; ++nf) st16(d, voff + nf * 1024, f[nf]);
        } else {
            bf16x8 vf = ldsr128(area + 4096 + (lane & 1) * 256 + j * 64 + (lane >> 4) * 16);
#pragma unroll
            for (int nf = 0; nf < 4; ++nf) {
                st16(ws + WS_WGU + (size_t)(k8 * 4 + j) * 14336, guo[nf], f[nf]);
                ut[nf] = MFMA16(f[nf], vf, ut[nf]);
            }
        }
    };
    {
        const unsigned vo = (unsigned)((lane < 32 ? 2048 : 1024) + (k8 & 7) * 128 + (lane & 31) * 4) * 4u;
#pragma unroll
        for (int i = 0; i < 8; ++i) LD16C(vp[i], ws + WS_MP2, vo + i * 16384);
    }
    unit_load(0);
    WAITV_BAR(17);
    KV_STAGE(2, 98304);
    WAIT_V(8);
    mod_unit(area, silu_frag(cw), area + 5120, lane);
    {
        f32x4 v = rbf4(sum8(vp));
        if (lane < 32) v = rbf4(v + 1.f);
        *(LAS u32x2*)(area + 4096 + lane * 8) = (u32x2){cvtpk(v[0], v[1]), cvtpk(v[2], v[3])};
    }
    unit_store(0); unit_load(1);
    WAITVL_BAR(8);
    KV_STAGE(3, 32768);
    if (pw == 0) {
        const LAS float* r = (const LAS float*)(cx.lds + 5120) + lane;
        st4(ws + WS_MP2, (unsigned)((cx.bid >> 5) * 4096 + col2 + lane) * 4u, (r[0] + r[4096]) + (r[32768] + r[36864]));
    }
    WAIT_V(8); unit_store(1); unit_load(2);
    WAITVL_BAR(8); KV_STAGE(4, 65536); WAIT_V(8); unit_store(2); unit_load(3);
    WAITVL_BAR(8); KV_STAGE(5, 98304); WAIT_V(8); unit_store(3); unit_load(4);
    if (isgu && (lane & 15) < 2) {
#pragma unroll
        for (int nf = 0; nf < 4; ++nf) st16(ws + WS_UT, (unsigned)((k8 * 4 + mat * 2 + (lane & 15)) * F + cg * 64 + nf * 16 + (lane >> 4) * 4) * 4u, ut[nf]);
    }
    WAITVL_BAR(8); KV_STAGE(6, 32768); WAIT_V(8); unit_store(4); unit_load(5);
    WAITVL_BAR(8);
    if (pw == 0) signal(ws, C_EARLY + (cx.bid & 15), lane);
    KV_STAGE(7, 65536); WAIT_V(8); unit_store(5);
    {
        const u32x4 e = {~0u, ~0u, ~0u, ~0u};
#pragma unroll
        for (int i = 0; i < 4; ++i) st16(ws + WS_XCH + (size_t)gw * 4096, voff + i * 1024, e);
    }
    WAITVL_BAR(0);
    if (pw == 0) signal(ws, C_SIDE + (cx.bid & 15), lane);
#undef KV_STAGE
    __builtin_amdgcn_s_setprio(3);
    await<16>(ws, C_EARLY, 16, 16, lane);
    next_pre();
    BARRIER();
    if (pw == 0) raise(ws, F_O + rb * 16 + hp, cx.tag, lane);
}
__device__ __forceinline__ void attn_consume(const Ctx& cx) {
    char* ws = cx.ws; const lds_ptr lds = cx.lds; const int wid = cx.w, lane = lane_id();
    f32x16 qa; f32x2 kn2, vm2; float knm, vmm;
    unsigned cv;
    int rb, hp; tile128x64(cx.bid, rb, hp);
    g64_consume<0, 81920>(qa, cx, lane, []() {}, [&](int t) {
            {
                if (t == 4) asm volatile("s_nop 4\n\tglobal_load_dword %0, %1, %2 sc1" : "=v"(cv) : "v"(wid == 0 ? (lane & 31) * 256 : 0), "s"(ws + WS_CNT) : "memory");
                if (t == 8) { asm volatile("s_waitcnt vmcnt(0)" : "+v"(cv) :: "memory"); if (wid == 0 && !__all(cv >= 16u)) await<6>(ws, C_KV, 32, 16, lane); }
            }
            if (t == 10) asm volatile("s_nop 4\n\tglobal_load_dwordx2 %0, %2, %3 sc1\n\tglobal_load_dwordx2 %1, %2, %3 offset:512 sc1" : "=&v"(kn2), "=&v"(vm2) : "v"(lane * 8), "s"(ws + WS_KN) : "memory"); },
        [&]() { asm volatile("s_waitcnt vmcnt(0)" : "+v"(kn2), "+v"(vm2) :: "memory"); knm = wmax(fmaxf(kn2[0], kn2[1])); vmm = wmax(fmaxf(vm2[0], vm2[1])); PIN(knm); PIN(vmm); SBAR(); },
        [&]() { });
    BAR_L();
    const lds_ptr pl = lds + lane * 16;
    bf16x8 k0 = ldsr128(pl + 32768), k1 = ldsr128(pl + 32768 + 1024), v0 = ldsr128(pl + 32768 + 2048);
    constexpr float QS = 0.17677669529663687f * 1.4426950408889634f;
    u32x4 qw0, qw1;
#pragma unroll
    for (int j = 0; j < 4; ++j) { qw0[j] = cvtpk(qa[2 * j] * QS, qa[2 * j + 1] * QS); qw1[j] = cvtpk(qa[8 + 2 * j] * QS, qa[8 + 2 * j + 1] * QS); }
    const bf16x8 q0 = __builtin_bit_cast(bf16x8, qw0), q1 = __builtin_bit_cast(bf16x8, qw1);
    f32x16 s0 = MFMA32(k0, q0, ((f32x16){})); s0 = MFMA32(k1, q1, s0);
    PIN(s0);
    float qn = 0.f;
#pragma unroll
    for (int j = 0; j < 4; ++j) { qn += bflo(qw0[j]) * bflo(qw0[j]) + bfhi(qw0[j]) * bfhi(qw0[j]) + bflo(qw1[j]) * bflo(qw1[j]) + bfhi(qw1[j]) * bfhi(qw1[j]); }
    qn = half_sum(qn);
    const bool fast = __all((qn * knm <= 88.f * 88.f) && (vmm <= 16777216.f) && (vmm >= 9.5367431640625e-7f));
    f32x16 o = {};
    float linv;
#define EX(x) __builtin_amdgcn_exp2f(x)
#define BF8(x) __builtin_bit_cast(bf16x8, x)
    if (fast) {
        const short ov = ((lane & 15) == ((lane >> 4) & 1)) ? (short)0x3f80 : (short)0;
        const bf16x8 sel = {ov, ov, ov, ov, ov, ov, ov, ov};
        f32x4 ls = ZERO4;
        const f32x16 zero = {};
        f32x16 sA, sB;
        bf16x8 v1 = v0;
        u32x4 p0, p1 = {0u, 0u, 0u, 0u};
        sA = s0;
        k0 = ldsr128(pl + 32768 + 4096); k1 = ldsr128(pl + 32768 + 4096 + 1024);
#define ATT_TILE(SC, SN, PK, PV0, PV1) do { \
            SN = MFMA32(k0, q0, zero); PIN(SN); SBAR(); k0 = ldsr128(PK); \
            SC[0] = EX(SC[0]); SC[1] = EX(SC[1]); SC[2] = EX(SC[2]); SC[3] = EX(SC[3]); PIN(SC); SBAR(); \
            o = MFMA32(v1, BF8(p1), o); PIN(o); SBAR(); v1 = ldsr128(PV1); \
            SC[4] = EX(SC[4]); SC[5] = EX(SC[5]); SC[6] = EX(SC[6]); PIN(SC); SBAR(); \
            ls = MFMA16(sel, BF8(p1), ls); PIN(ls); SBAR(); SC[7] = EX(SC[7]); p0[0] = cvtpk(SC[0], SC[1]); p0[1] = cvtpk(SC[2], SC[3]); PIN(SC); PIN(p0); SBAR(); \
            SN = MFMA32(k1, q1, SN); PIN(SN); SBAR(); k1 = ldsr128((PK) + 1024); \
            p0[2] = cvtpk(SC[4], SC[5]); p0[3] = cvtpk(SC[6], SC[7]); PIN(p0); \
            SC[8] = EX(SC[8]); SC[9] = EX(SC[9]); SC[10] = EX(SC[10]); PIN(SC); SBAR(); \
            o = MFMA32(v0, BF8(p0), o); PIN(o); SBAR(); v0 = ldsr128(PV0); \
            SC[11] = EX(SC[11]); SC[12] = EX(SC[12]); SC[13] = EX(SC[13]); PIN(SC); SBAR(); \
            ls = MFMA16(sel, BF8(p0), ls); PIN(ls); SBAR(); \
            SC[14] = EX(SC[14]); SC[15] = EX(SC[15]); \
            p1[0] = cvtpk(SC[8], SC[9]); p1[1] = cvtpk(SC[10], SC[11]); p1[2] = cvtpk(SC[12], SC[13]); p1[3] = cvtpk(SC[14], SC[15]); PIN(p1); SBAR(); } while (0)
        int so = 32768;
#pragma unroll 1
        for (int st = 0; st < 8; ++st) {
            const int sn = so == 98304 ? 32768 : so + 32768;
            const lds_ptr pc = pl + so, pn = pl + sn;
            __builtin_amdgcn_s_setprio(1);
            ATT_TILE(sA, sB, pc + 2 * 4096, pc + 1 * 4096 + 2048, pc + 0 * 4096 + 3072);
            ATT_TILE(sB, sA, pc + 3 * 4096, pc + 2 * 4096 + 2048, pc + 1 * 4096 + 3072);
            ATT_TILE(sA, sB, pc + 4 * 4096, pc + 3 * 4096 + 2048, pc + 2 * 4096 + 3072);
            ATT_TILE(sB, sA, pc + 5 * 4096, pc + 4 * 4096 + 2048, pc + 3 * 4096 + 3072);
            __builtin_amdgcn_s_setprio(0);
            ATT_TILE(sA, sB, pc + 6 * 4096, pc + 5 * 4096 + 2048, pc + 4 * 4096 + 3072);
            ATT_TILE(sB, sA, pc + 7 * 4096, pc + 6 * 4096 + 2048, pc + 5 * 4096 + 3072);
            if (st < 7) BAR_L();
            if (st < 7) __builtin_amdgcn_s_setprio(2);
            SBAR();
            ATT_TILE(sA, sB, pn, pc + 7 * 4096 + 2048, pc + 6 * 4096 + 3072);
            ATT_TILE(sB, sA, pn + 4096, pn + 2048, pc + 7 * 4096 + 3072);
            so = sn;
        }
        o = MFMA32(v1, BF8(p1), o);
        ls = MFMA16(sel, BF8(p1), ls);
#undef ATT_TILE
        const float la = __shfl(ls[0], lane & 15), lb = __shfl(ls[1], lane & 15);
        linv = 1.f / ((lane & 16) ? lb : la);
    } else {
        float m = -INFINITY, l = 0.f;
        const f32x16 zero = {};
        int so = 32768;
#pragma unroll 1
        for (int st = 0; st < 8; ++st) {
            const lds_ptr pb = pl + so;
#pragma unroll 1
            for (int t = 0; t < 8; ++t) {
                if (t == 6 && st < 7) BAR_L();
                k0 = ldsr128(pb + t * 4096); k1 = ldsr128(pb + t * 4096 + 1024); v0 = ldsr128(pb + t * 4096 + 2048); const bf16x8 v1 = ldsr128(pb + t * 4096 + 3072);
                f32x16 s = MFMA32(k0, q0, zero);
                s = MFMA32(k1, q1, s);
                float tm = s[0];
#pragma unroll
                for (int j = 1; j < 16; ++j) tm = fmaxf(tm, s[j]);
                tm = fmaxf(tm, __shfl_xor(tm, 32));
                const float mn = fmaxf(m, tm), al = EX(m - mn);
                m = mn; l *= al;
#pragma unroll
                for (int j = 0; j < 16; ++j) o[j] *= al;
                u32x4 p0, p1;
#pragma unroll
                for (int j = 0; j < 4; ++j) {
                    p0[j] = cvtpk(EX(s[2 * j] - mn), EX(s[2 * j + 1] - mn));
                    p1[j] = cvtpk(EX(s[8 + 2 * j] - mn), EX(s[8 + 2 * j + 1] - mn));
                    l += (bflo(p0[j]) + bfhi(p0[j])) + (bflo(p1[j]) + bfhi(p1[j]));
                }
                o = MFMA32(v0, BF8(p0), o);
                o = MFMA32(v1, BF8(p1), o);
            }
            so = so == 98304 ? 32768 : so + 32768;
        }
        linv = 1.f / half_sum(l);
    }
#undef EX
#undef BF8
    {
        const char* ob = ws + WS_O + ((size_t)(rb * 8 + (wid & 3) * 2) * 128 + (hp * 2 + (wid >> 2)) * 4) * 256;
        const unsigned vo = (unsigned)(((lane >> 4) & 1) * 32768 + (lane >> 5) * 256 + (lane & 15) * 16);
#pragma unroll
        for (int gp = 0; gp < 2; ++gp) {
            const auto r0 = __builtin_amdgcn_permlane32_swap(cvtpk(o[8 * gp] * linv, o[8 * gp + 1] * linv), cvtpk(o[8 * gp + 4] * linv, o[8 * gp + 5] * linv), false, false);
            const auto r1 = __builtin_amdgcn_permlane32_swap(cvtpk(o[8 * gp + 2] * linv, o[8 * gp + 3] * linv), cvtpk(o[8 * gp + 6] * linv, o[8 * gp + 7] * linv), false, false);
            st16(ob, vo + gp * 512, ((u32x4){r0[0], r1[0], r0[1], r1[1]}));
        }
    }
    WAITVL_BAR(0);
}

constexpr int O_STAT = 159744;
struct OPre { f32x4 p[8]; };
__device__ __forceinline__ void oproj_pre(const Ctx& cx, OPre& o) {
    const int lane = lane_id(); int rb, ct; tile128x64(cx.bid, rb, ct);
    const unsigned vo = (unsigned)(((lane >> 4) & 1) * 2048 + ct * 64 + (lane & 15) * 4) * 4u;
#pragma unroll
    for (int i = 0; i < 8; ++i) LD16C(o.p[i], cx.ws + WS_MP2, vo + i * 16384);
    g64_pre<98304, 10>(cx, cx.ws + WS_WO + (size_t)ct * 131072, lane);
}
template <class Next>
__device__ __forceinline__ void oproj_produce(const Ctx& cx, OPre& o, const u16* __restrict__ x, Next next_pre) {
    char* ws = cx.ws; const int pw = cx.w, lane = lane_id();
    int rb, ct; tile128x64(cx.bid, rb, ct);
    if (pw == 0) expect(ws, F_O + rb * 16, lane < 5 ? lane : -1, cx.tag);
    INPUT_BARRIER();
    f32x4 tv;
    g64_produce<98304, 16384, 10, 4>(cx, ws + WS_O + (size_t)rb * 8 * 32768, ws + WS_WO + (size_t)ct * 131072, lane, []() {},
        [&]() { tv = rbf4(sum8(o.p)); if (lane & 16) tv = rbf4(tv + 1.f); PIN(tv); },
        [&]() { if (pw == 0) expect(ws, F_O + rb * 16, lane < 16 ? lane : -1, cx.tag); },
        [&]() {
            const unsigned voffX = (unsigned)((rb * 128 + pw * 32 + (lane >> 3)) * 2048 + ct * 128 + (((lane & 7) ^ ((lane >> 3) & 7)) << 4));
#pragma unroll
            for (int i = 0; i < 4; ++i) dma16((const char*)x + i * 8 * 2048, voffX, cx.lds0 + 131072 + pw * 4096 + i * 1024);
            if (pw == 0 && lane < 32) *(LAS f32x4*)(cx.lds + 147456 + lane * 16) = tv; });
    BARRIER();
    if (pw == 0 && lane == 0) *(LAS unsigned*)cx.lds = 0u;
    BAR_L();
    next_pre();
}
__device__ __forceinline__ void oproj_consume(const Ctx& cx) {
    char* ws = cx.ws; const lds_ptr lds = cx.lds; const int wid = cx.w, lane = lane_id();
    f32x16 acc;
    int rb, ct; tile128x64(cx.bid, rb, ct);
    g64_consume<98304, 16384>(acc, cx, lane, [&]() { INPUT_BARRIER(); }, [](int) {}, []() {}, [&]() { });
    BAR_L();
    const int tb = wid & 3, nh = wid >> 2, h = lane >> 5, rl = tb * 32 + (lane & 31);
    const unsigned so = (unsigned)(((lane >> 4) & 1) * 32768 + h * 256 + (lane & 15) * 16);
    u32x4 x1v[2]; float v[16];
#pragma unroll
    for (int gp = 0; gp < 2; ++gp) {
        unsigned u[2][2], y[2][2];
#pragma unroll
        for (int k = 0; k < 2; ++k) {
            const int G = 2 * gp + k;
            const f32x4 ga = *(const LAS f32x4*)(lds + 147456 + (nh * 8 + G * 2 + h) * 16), cb = *(const LAS f32x4*)(lds + 147456 + 256 + (nh * 8 + G * 2 + h) * 16);
            const u32x2 xv = *(const LAS u32x2*)(lds + 131072 + rl * 128 + (((nh * 4 + G) ^ (rl & 7)) << 4) + 8 * h);
            u[k][0] = cvtpk(bflo(xv[0]) + ga[0] * acc[4 * G], bfhi(xv[0]) + ga[1] * acc[4 * G + 1]);
            u[k][1] = cvtpk(bflo(xv[1]) + ga[2] * acc[4 * G + 2], bfhi(xv[1]) + ga[3] * acc[4 * G + 3]);
            v[4 * G] = bflo(u[k][0]); v[4 * G + 1] = bfhi(u[k][0]); v[4 * G + 2] = bflo(u[k][1]); v[4 * G + 3] = bfhi(u[k][1]);
            y[k][0] = cvtpk(v[4 * G] * cb[0], v[4 * G + 1] * cb[1]); y[k][1] = cvtpk(v[4 * G + 2] * cb[2], v[4 * G + 3] * cb[3]);
        }
        st16(ws + WS_Y + ((size_t)(rb * 8 + tb * 2) * 128 + ct * 8 + nh * 4) * 256, so + gp * 512, swap32(y[0][0], y[0][1], y[1][0], y[1][1]));
        x1v[gp] = swap32(u[0][0], u[0][1], u[1][0], u[1][1]);
    }
    BAR_L();
    {
        float sm = 0.f;
#pragma unroll
        for (int j = 0; j < 16; ++j) sm += v[j];
        const float mean = half_sum(sm) * (1.f / 32.f);
        float q = 0.f;
#pragma unroll
        for (int j = 0; j < 16; ++j) q += (v[j] - mean) * (v[j] - mean);
        q = half_sum(q);
        if (h == 0) *(LAS f32x2*)(lds + O_STAT + (rl * 2 + nh) * 8) = (f32x2){mean, q};
    }
    drain_raise(ws, lds, F_Y + rb * 16 + ct, cx.tag, lane);
#pragma unroll
    for (int gp = 0; gp < 2; ++gp) st16(ws + WS_X1 + ((size_t)(rb * 8 + tb * 2) * 128 + ct * 8 + nh * 4) * 256, so + gp * 512, x1v[gp]);
}

constexpr int GSL = 30720, G_ROW = 5 * GSL + 1024, G_COL = G_ROW + 2048;
__device__ __forceinline__ void tile_glu(int bid, int& rp, int& ct) { const int xcd = bid & 7, idx = bid >> 3; rp = (xcd & 1) * 4 + (idx & 3); ct = (xcd >> 1) * 8 + (idx >> 2); }
#define GLU_W(t, so) do { const char* w_ = Wb + (size_t)(t) * 14336; const unsigned d2_ = pw < 3 ? dW + (so) + 2048 : cx.lds0 + 5 * GSL; \
        dmac(w_, voff, dW + (so)); dmac(w_ + 1024, voff, dW + (so) + 1024); dmac(w_, voff2, d2_); dmac(w_ + 1024, voff2, pw < 3 ? d2_ + 1024 : d2_); } while (0)
__device__ __forceinline__ void glu_pre(const Ctx& cx) {
    const int pw = cx.w, lane = lane_id(); int rp, ct; tile_glu(cx.bid, rp, ct);
    const char* Wb = cx.ws + WS_WGU + (size_t)ct * 32 * 14336 + pw * 4096;
    const unsigned voff = lane * 16, dW = cx.lds0 + 16384 + pw * 4096;
#pragma unroll
    for (int t = 0; t < 4; ++t) {
        dmac(Wb + t * 14336, voff, dW + t * GSL); dmac(Wb + t * 14336 + 1024, voff, dW + t * GSL + 1024);
        if (pw < 3) { dmac(Wb + t * 14336 + 2048, voff, dW + t * GSL + 2048); dmac(Wb + t * 14336 + 3072, voff, dW + t * GSL + 3072); }
    }
}
template <class Next>
__device__ __forceinline__ void glu_produce(const Ctx& cx, Next next_pre) {
    char* ws = cx.ws; const int pw = cx.w, lane = lane_id(); int rp, ct; tile_glu(cx.bid, rp, ct);
    const char* Ab = ws + WS_Y + (size_t)(rp * 16 + 4 * pw) * 32768;
    const char* Wb = ws + WS_WGU + (size_t)ct * 32 * 14336 + pw * 4096;
    const unsigned voff = lane * 16, voff2 = pw < 3 ? lane * 16 + 2048 : 0u, dA = cx.lds0 + pw * 4096, dW = cx.lds0 + 16384 + pw * 4096;
#define GLU_A(t, so) do { const char* a_ = Ab + (size_t)(t) * 1024; dmac(a_, voff, dA + (so)); dmac(a_ + 32768, voff, dA + (so) + 1024); dmac(a_ + 65536, voff, dA + (so) + 2048); dmac(a_ + 98304, voff, dA + (so) + 3072); } while (0)
    if (pw == 0) expect(ws, F_Y + rp * 32, lane < 32 && (lane & 15) < 2 ? lane : -1, cx.tag);
    INPUT_BARRIER();
    GLU_A(0, 0); GLU_A(1, GSL);
    WAITV_BAR(4);
    GLU_A(2, 2 * GSL); GLU_A(3, 3 * GSL);
    WAITV_BAR(8);
    if (pw == 0) expect(ws, F_Y + rp * 32, lane < 32 ? lane : -1, cx.tag);
    WAITV_BAR(4); GLU_W(4, 4 * GSL); GLU_A(4, 4 * GSL); GLU_W(5, 0); GLU_A(5, 0);
    int sf = GSL;
#define GLU_NEXT(t, VM) do { WAITV_BAR(VM); GLU_W((t) + 4, sf); GLU_A((t) + 4, sf); sf = sf == 4 * GSL ? 0 : sf + GSL; } while (0)
    GLU_NEXT(2, 16); GLU_NEXT(3, 16);
    if (pw == 0) { int rbo, cto; tile128x64(cx.bid, rbo, cto); signal(ws, C_ST + (rbo >> 1), lane); }
    GLU_NEXT(4, 16); GLU_NEXT(5, 16);
    {
        unsigned cv;
        asm volatile("s_nop 4\n\tglobal_load_dword %0, %1, %2 sc1" : "=v"(cv) : "v"((C_ST + rp) * 256), "s"(ws + WS_CNT) : "memory");
        GLU_NEXT(6, 17); GLU_NEXT(7, 17); GLU_NEXT(8, 17); GLU_NEXT(9, 16);
        PIN(cv);
        if (!__all(cv >= 32u)) await<4>(ws, C_ST + rp, 1, 32, lane);
    }
    {
        f32x2 sp[16]; f32x4 cp[8];
        const char* sb_ = ws + WS_ST2 + (size_t)(rp * 256 + pw * 64) * 8;
#pragma unroll
        for (int i = 0; i < 8; ++i) LD8C(sp[i], sb_, (unsigned)(lane * 8 + i * 16384));
        GLU_NEXT(10, 24);
#pragma unroll
        for (int i = 8; i < 16; ++i) LD8C(sp[i], sb_, (unsigned)(lane * 8 + i * 16384));
        GLU_NEXT(11, 32);
        int i4 = pw * 64 + lane; i4 = i4 < 112 ? i4 : 0;
        const unsigned vo = (unsigned)((i4 / 28) * F + ct * 112 + (i4 % 28) * 4) * 4u;
#pragma unroll
        for (int i = 0; i < 8; ++i) LD16C(cp[i], ws + WS_UT, vo + i * (4 * F * 4));
        GLU_NEXT(12, 40); GLU_NEXT(13, 32); GLU_NEXT(14, 24); GLU_NEXT(15, 16);
        __builtin_amdgcn_s_setprio(0);
        float mean = 0.f, q = 0.f;
#pragma unroll
        for (int i = 0; i < 16; ++i) { PIN(sp[i]); mean += sp[i][0]; q += sp[i][1]; }
        mean *= 1.f / 16.f; PIN(mean); PIN(q);
        GLU_NEXT(16, 16);
        float dq = 0.f;
#pragma unroll
        for (int i = 0; i < 16; ++i) dq += (sp[i][0] - mean) * (sp[i][0] - mean);
        PIN(dq);
        GLU_NEXT(17, 16);
        const float rstd = 1.f / sqrtf((q + 64.f * dq) * (1.f / 1024.f) + 1e-5f);
        *(LAS f32x2*)(cx.lds + G_ROW + (pw * 64 + lane) * 8) = (f32x2){rstd, -mean * rstd};
        GLU_NEXT(18, 16);
        const f32x4 c = sum8(cp);
        if (pw * 64 + lane < 112) *(LAS f32x4*)(cx.lds + G_COL + (pw * 64 + lane) * 16) = c;
        __builtin_amdgcn_s_setprio(3);
        GLU_NEXT(19, 16);
    }
    {
        unsigned cv;
        int rbd, ctd, khd; tile_down(cx.bid, rbd, ctd, khd);
        asm volatile("s_nop 4\n\tglobal_load_dword %0, %1, %2 sc1" : "=v"(cv) : "v"((lane < 16 ? C_SIDE + lane : C_ST + (rbd >> 1)) * 256), "s"(ws + WS_CNT) : "memory");
        GLU_NEXT(20, 17); GLU_NEXT(21, 17); GLU_NEXT(22, 17); GLU_NEXT(23, 16);
        PIN(cv);
        if (!__all(cv >= 32u)) { await<4>(ws, C_SIDE, 16, 32, lane); await<4>(ws, C_ST + (rbd >> 1), 1, 32, lane); }
    }
#pragma unroll 1
    for (int t = 24; t < 28; ++t) GLU_NEXT(t, 16);
    WAITV_BAR(16); WAITV_BAR(8); WAITVL_BAR(0);
#undef GLU_NEXT
#undef GLU_A
    if (pw == 0 && lane == 0) *(LAS unsigned*)(cx.lds + G_COL + 1920) = 0u;
    BAR_L();
    next_pre();
}
#undef GLU_W
__device__ __forceinline__ void glu_consume(const Ctx& cx) {
    char* ws = cx.ws; const lds_ptr lds = cx.lds; const int wm = cx.w & 3, wn = cx.w >> 2;
    f32x4 acc[4][7];
    {
        const int lane = lane_id();
        const lds_ptr pa = lds + wm * 4096 + lane * 16, pw = lds + 16384 + wn * 7168 + lane * 16;
        bf16x8 af[4], wf[4];
#pragma unroll
        for (int n = 0; n < 7; ++n)
#pragma unroll
            for (int m = 0; m < 4; ++m) acc[m][n] = ZERO4;
        INPUT_BARRIER();
        if (cx.w == 1 || cx.w == 2) {
            int rbo, cto; tile128x64(cx.bid, rbo, cto);
            const int rl = (cx.w - 1) * 64 + lane;
            const f32x4 s = *(const LAS f32x4*)(lds + O_STAT + rl * 16);
            const float dm = s[0] - s[2];
            st8(ws + WS_ST2 + (size_t)(cto * 2048 + rbo * 128) * 8, (unsigned)rl * 8u, ((f32x2){0.5f * (s[0] + s[2]), s[1] + s[3] + 16.f * dm * dm}));
        }
        BARRIER();
#pragma unroll
        for (int m = 0; m < 4; ++m) { af[m] = ldsr128(pa + m * 1024); wf[m] = ldsr128(pw + m * 1024); }
        int sc = 0, sn = GSL;
#define LAD(p) __builtin_amdgcn_s_setprio(p)
#define GW(U, n) wf[(7 * (U) + (n)) & 3]
#define GLU_STEP(U, DO_NEXT) do { \
            if ((U) == 3) WAIT_V(0);   if (DO_NEXT) BARRIER(); LAD(1); SBAR(); \
            const lds_ptr pwc = pw + sc, pwn = pw + sn, pan = pa + sn; \
            _Pragma("unroll") for (int m = 0; m < 4; ++m) { MFMA16_ACC(acc[m][0], GW(U, 0), af[m]); MFMA16_ACC(acc[m][1], GW(U, 1), af[m]); } \
            SBAR(); GW(U, 4) = ldsr128(pwc + 4096); GW(U, 5) = ldsr128(pwc + 5120); SBAR(); \
            _Pragma("unroll") for (int m = 0; m < 4; ++m) MFMA16_ACC(acc[m][2], GW(U, 2), af[m]); \
            SBAR(); GW(U, 6) = ldsr128(pwc + 6144); SBAR(); \
            _Pragma("unroll") for (int m = 0; m < 4; ++m) MFMA16_ACC(acc[m][3], GW(U, 3), af[m]); \
            SBAR(); if (DO_NEXT) GW(U, 7) = ldsr128(pwn); SBAR(); \
            _Pragma("unroll") for (int m = 0; m < 4; ++m) MFMA16_ACC(acc[m][4], GW(U, 4), af[m]); \
            SBAR(); if (DO_NEXT) GW(U, 8) = ldsr128(pwn + 1024); SBAR(); \
            _Pragma("unroll") for (int m = 0; m < 4; ++m) { MFMA16_ACC(acc[m][5], GW(U, 5), af[m]); MFMA16_ACC(acc[m][6], GW(U, 6), af[m]); \
                SBAR(); if (DO_NEXT) af[m] = ldsr128(pan + m * 1024); if (m == 1) LAD(0); SBAR(); } \
            if (DO_NEXT) { GW(U, 9) = ldsr128(pwn + 2048); GW(U, 10) = ldsr128(pwn + 3072); } SBAR(); \
            sc = sn; sn = sn == 4 * GSL ? 0 : sn + GSL; } while (0)
#pragma unroll 1
        for (int t = 0; t < 28; t += 4) { GLU_STEP(0, true); GLU_STEP(1, true); GLU_STEP(2, true); GLU_STEP(3, true); }
        GLU_STEP(0, true); GLU_STEP(1, true); GLU_STEP(2, true); GLU_STEP(3, false);
#undef GLU_STEP
#undef GW
    }
    asm volatile("s_nop 7\n\ts_nop 7" : "+v"(acc[0][6]), "+v"(acc[1][6]), "+v"(acc[2][6]), "+v"(acc[3][6]), "+v"(acc[0][5]), "+v"(acc[1][5]), "+v"(acc[2][5]), "+v"(acc[3][5]) :: "memory");
    SBAR();
    const int lane = lane_id(), g = lane >> 4, gc = ((g & 1) << 1) | (g >> 1);
    int rp, ct; tile_glu(cx.bid, rp, ct);
    const char* hb = ws + WS_HH + ((size_t)(rp * 16 + wm * 4) * (F / 8) + ct * 14 + wn * 7) * 256;
    const unsigned vo = (lane & 15) * 16;
    constexpr unsigned RB = (F / 8) * 256;
    f32x2 rw[4];
#pragma unroll
    for (int m = 0; m < 4; ++m) rw[m] = *(const LAS f32x2*)(lds + G_ROW + (wm * 64 + m * 16 + (lane & 15)) * 8);
#define GLU_OUT(m, f, ug, tg, uu, tu, d0, d1) do { const f32x4 gt = acc[m][f] * rw[m][0] + (ug * rw[m][1] + tg), up = acc[m][3 + (f)] * rw[m][0] + (uu * rw[m][1] + tu); \
        d0 = cvtpk(fmaxf(gt[0], 0.f) * up[0], fmaxf(gt[1], 0.f) * up[1]); d1 = cvtpk(fmaxf(gt[2], 0.f) * up[2], fmaxf(gt[3], 0.f) * up[3]); } while (0)
    {
        const lds_ptr pc = lds + G_COL + (wn * 14 + g) * 16;
        f32x4 ct_[3][4];
#pragma unroll
        for (int f = 0; f < 2; ++f)
#pragma unroll
            for (int k = 0; k < 4; ++k) ct_[f][k] = *(const LAS f32x4*)(pc + f * 64 + k * 448);
#pragma unroll
        for (int m = 0; m < 4; ++m) {
            unsigned a0, a1, b0, b1;
            GLU_OUT(m, 0, ct_[0][0], ct_[0][1], ct_[0][2], ct_[0][3], a0, a1); GLU_OUT(m, 1, ct_[1][0], ct_[1][1], ct_[1][2], ct_[1][3], b0, b1);
            st16(hb, vo + m * RB + gc * 256, swap16(a0, a1, b0, b1));
            if (m == 1) { _Pragma("unroll") for (int k = 0; k < 4; ++k) ct_[2][k] = *(const LAS f32x4*)(pc + 128 + k * 448); }
            SBAR();
        }
#pragma unroll
        for (int mp = 0; mp < 2; ++mp) {
            unsigned a0, a1, b0, b1;
            GLU_OUT(2 * mp, 2, ct_[2][0], ct_[2][1], ct_[2][2], ct_[2][3], a0, a1); GLU_OUT(2 * mp + 1, 2, ct_[2][0], ct_[2][1], ct_[2][2], ct_[2][3], b0, b1);
            st16(hb, vo + (2 * mp + (g & 1)) * RB + (4 + (g >> 1)) * 256, swap16(a0, a1, b0, b1));
            SBAR();
        }
    }
#undef GLU_OUT
    {
        const lds_ptr pc = lds + G_COL + (g >> 1) * 896 + (wn * 14 + 12 + (g & 1)) * 16;
        const f32x4 uc = *(const LAS f32x4*)pc, tc = *(const LAS f32x4*)(pc + 448);
        unsigned ux[2][2];
#pragma unroll
        for (int mp = 0; mp < 2; ++mp) {
            const f32x4 a = acc[2 * mp][6] * rw[2 * mp][0] + (uc * rw[2 * mp][1] + tc), b = acc[2 * mp + 1][6] * rw[2 * mp + 1][0] + (uc * rw[2 * mp + 1][1] + tc);
            float hv[4];
#pragma unroll
            for (int r = 0; r < 4; ++r) {
                const auto x = __builtin_amdgcn_permlane32_swap(__float_as_uint(a[r]), __float_as_uint(b[r]), false, false);
                hv[r] = fmaxf(__uint_as_float(x[0]), 0.f) * __uint_as_float(x[1]);
            }
            ux[mp][0] = cvtpk(hv[0], hv[1]); ux[mp][1] = cvtpk(hv[2], hv[3]);
        }
        st16(hb, vo + gc * RB + 6 * 256, swap16(ux[0][0], ux[0][1], ux[1][0], ux[1][1]));
    }
    BARRIER();
    drain_raise(ws, lds + G_COL + 1920, F_HH + rp * 32 + ct, cx.tag, lane);
}

#define DN_W(t, so) do { const char* w_ = Wb + (size_t)(t) * 16384; dmac(w_, voff, dW + (so)); dmac(w_ + 1024, voff, dW + (so) + 1024); dmac(w_ + 2048, voff, dW + (so) + 2048); dmac(w_ + 3072, voff, dW + (so) + 3072); } while (0)
__device__ __forceinline__ void down_pre(const Ctx& cx, OPre& o) {
    const int pw = cx.w, lane = lane_id(); int rb, ct, kh; tile_down(cx.bid, rb, ct, kh);
    const unsigned vo = (unsigned)(3072 + ct * 128 + (lane & 31) * 4) * 4u;
#pragma unroll
    for (int i = 0; i < 8; ++i) LD16C(o.p[i], cx.ws + WS_MP2, vo + i * 16384);
    const char* Wb = cx.ws + WS_WD + (size_t)(ct * 112 + kh * 56) * 8192 + pw * 4096;
    const unsigned voff = lane * 16, dW = cx.lds0 + 16384 + pw * 4096;
#pragma unroll
    for (int t = 0; t < 4; ++t) DN_W(t, t * 32768);
}
__device__ __forceinline__ void down_produce(const Ctx& cx, OPre& o) {
    char* ws = cx.ws; const int pw = cx.w, lane = lane_id(); int rb, ct, kh; tile_down(cx.bid, rb, ct, kh);
    const char* Ab = ws + WS_HH + ((size_t)(rb * 8 + 2 * pw) * (F / 8) + kh * 224) * 256;
    const char* Wb = ws + WS_WD + (size_t)(ct * 112 + kh * 56) * 8192 + pw * 4096;
    const unsigned voff = lane * 16, dA = cx.lds0 + pw * 4096, dW = cx.lds0 + 16384 + pw * 4096;
#define DN_A(t, so) do { const char* a_ = Ab + (size_t)(t) * 2048; dmac(a_, voff, dA + (so)); dmac(a_ + 1024, voff, dA + (so) + 1024); \
        dmac(a_ + (F / 8) * 256, voff, dA + (so) + 2048); dmac(a_ + (F / 8) * 256 + 1024, voff, dA + (so) + 3072); } while (0)
    if (pw == 0) expect(ws, F_HH + (rb >> 1) * 32 + kh * 16, lane < 3 ? lane : -1, cx.tag);
    INPUT_BARRIER();
    DN_A(0, 0); DN_A(1, 32768);
    WAITV_BAR(4);
    DN_A(2, 65536); DN_A(3, 98304);
    WAITV_BAR(8);
    DN_W(4, 131072);
    if (pw == 0) expect(ws, F_HH + (rb >> 1) * 32 + kh * 16, lane < 16 ? lane : -1, cx.tag);
    f32x4 tv = rbf4(sum8(o.p)); PIN(tv);
    WAITV_BAR(8); DN_A(4, 131072); DN_W(5, 0); DN_A(5, 0);
    int sf = 32768;
#pragma unroll 1
    for (int t = 2; t < 24; ++t) {
        WAITV_BAR(16);
        DN_W(t + 4, sf); DN_A(t + 4, sf);
        sf = sf == 131072 ? 0 : sf + 32768;
    }
    WAITV_BAR(16);
    {
        const char* xb = ws + WS_X1 + ((size_t)(rb * 8 + (pw >> 1) * 4 + kh * 2 + (pw & 1)) * 128 + ct * 16) * 256;
#pragma unroll
        for (int i = 0; i < 4; ++i) dmac(xb + i * 1024, voff, cx.lds0 + 98304 + pw * 4096 + i * 1024);
        if (pw == 0 && lane < 32) *(LAS f32x4*)(cx.lds + 114688 + lane * 16) = tv;
    }
    WAITV_BAR(12); WAITV_BAR(4); WAITVL_BAR(0);
#undef DN_A
}
#undef DN_W
__device__ __forceinline__ void down_consume(const Ctx& cx, u16* __restrict__ out) {
    char* ws = cx.ws; const lds_ptr lds = cx.lds; const int wid = cx.w, lane = lane_id(), wm = wid >> 2, wn = wid & 3, g = lane >> 4;
    const lds_ptr pa = lds + wm * 8192 + lane * 16, pw = lds + 16384 + wn * 2048 + lane * 16;
    f32x4 acc[4][2];
#pragma unroll
    for (int m = 0; m < 4; ++m) { acc[m][0] = ZERO4; acc[m][1] = ZERO4; }
    bf16x8 aA[8], wA[4], aB[8], wB[4];
    INPUT_BARRIER();
    BARRIER(); SBAR();
#pragma unroll
    for (int ks = 0; ks < 2; ++ks) {
        wA[2 * ks] = ldsr128(pw + ks * 8192); wA[2 * ks + 1] = ldsr128(pw + ks * 8192 + 1024);
#pragma unroll
        for (int m = 0; m < 4; ++m) aA[4 * ks + m] = ldsr128(pa + m * 2048 + ks * 1024);
    }
    SBAR();
    int sn = 32768;
#define DN_STEP(AC, WC, AN, WN, DO_NEXT) do { \
        if (DO_NEXT) BARRIER(); LAD(1); SBAR(); \
        const lds_ptr pan = pa + sn, pwn = pw + sn; \
        _Pragma("unroll") for (int i = 0; i < 8; ++i) { const int ks = i >> 2, m = i & 3; \
            MFMA16_ACC(acc[m][0], WC[2 * ks], AC[i]); MFMA16_ACC(acc[m][1], WC[2 * ks + 1], AC[i]); SBAR(); \
            if (DO_NEXT) { if (m < 2) WN[2 * ks + m] = ldsr128(pwn + ks * 8192 + m * 1024); AN[i] = ldsr128(pan + m * 2048 + ks * 1024); } \
            if (i == 5) LAD(0); \
            SBAR(); } \
        sn = sn == 131072 ? 0 : sn + 32768; } while (0)
#pragma unroll 1
    for (int t = 0; t < 26; t += 2) {
        DN_STEP(aA, wA, aB, wB, true); DN_STEP(aB, wB, aA, wA, true);
    }
    DN_STEP(aA, wA, aB, wB, true); DN_STEP(aB, wB, aA, wA, false);
#undef DN_STEP
    asm volatile("s_nop 7\n\ts_nop 7" : "+v"(acc[0][0]), "+v"(acc[0][1]), "+v"(acc[1][0]), "+v"(acc[1][1]), "+v"(acc[2][0]), "+v"(acc[2][1]), "+v"(acc[3][0]), "+v"(acc[3][1]));
    SBAR();
    int rb, ct, kh; tile_down(cx.bid, rb, ct, kh);
    f32x4 keep[2][2];
#pragma unroll
    for (int mm = 0; mm < 2; ++mm) {
        const f32x4 g0 = kh ? acc[mm][0] : acc[2 + mm][0], g1 = kh ? acc[mm][1] : acc[2 + mm][1];
        st16(ws + WS_XCH + ((size_t)cx.bid * 8 + wid) * 2048, lane * 16 + mm * 1024, ((u32x4){cvtpk(g0[0], g0[1]), cvtpk(g0[2], g0[3]), cvtpk(g1[0], g1[1]), cvtpk(g1[2], g1[3])}));
        keep[mm][0] = kh ? acc[2 + mm][0] : acc[mm][0]; keep[mm][1] = kh ? acc[2 + mm][1] : acc[mm][1];
    }
    BARRIER();
    u32x2 xr[2][2]; f32x4 gb[2];
#pragma unroll
    for (int nf = 0; nf < 2; ++nf) {
        gb[nf] = *(const LAS f32x4*)(lds + 114688 + (wn * 8 + nf * 4 + g) * 16);
#pragma unroll
        for (int mm = 0; mm < 2; ++mm) xr[mm][nf] = *(const LAS u32x2*)(lds + 98304 + ((wm * 2 + mm) * 16 + wn * 4 + nf * 2 + (g >> 1)) * 256 + (lane & 15) * 16 + 8 * (g & 1));
    }
    u32x4 got[2];
    {
        const char* xi = ws + WS_XCH + ((size_t)(cx.bid ^ 1) * 8 + wid) * 2048;
        for (unsigned spins = 0; spins < (1u << 15); ++spins) {
            asm volatile("s_nop 4\n\tglobal_load_dwordx4 %0, %2, %3 sc1\n\tglobal_load_dwordx4 %1, %2, %3 offset:1024 sc1\n\ts_waitcnt vmcnt(0)" : "=&v"(got[0]), "=&v"(got[1]) : "v"(lane * 16), "s"(xi) : "memory");
            const bool ok = got[0][0] != ~0u && got[0][1] != ~0u && got[0][2] != ~0u && got[0][3] != ~0u && got[1][0] != ~0u && got[1][1] != ~0u && got[1][2] != ~0u && got[1][3] != ~0u;
            if (__all(ok)) break;
            __builtin_amdgcn_s_sleep(2);
        }
    }
#pragma unroll
    for (int mm = 0; mm < 2; ++mm) {
        unsigned u[2][2];
#pragma unroll
        for (int nf = 0; nf < 2; ++nf) {
            const f32x4 gg = gb[nf], k = keep[mm][nf];
            u[nf][0] = cvtpk(bflo(xr[mm][nf][0]) + gg[0] * (k[0] + bflo(got[mm][2 * nf])), bfhi(xr[mm][nf][0]) + gg[1] * (k[1] + bfhi(got[mm][2 * nf])));
            u[nf][1] = cvtpk(bflo(xr[mm][nf][1]) + gg[2] * (k[2] + bflo(got[mm][2 * nf + 1])), bfhi(xr[mm][nf][1]) + gg[3] * (k[3] + bfhi(got[mm][2 * nf + 1])));
        }
        st16(out + (size_t)(rb * 128 + wm * 64 + (2 * kh + mm) * 16) * D + ct * 128 + wn * 32, (unsigned)((lane & 15) * 2048 + (((g & 1) << 1) | (g >> 1)) * 16), swap16(u[0][0], u[0][1], u[1][0], u[1][1]));
    }
}

#define CTX_INIT extern __shared__ __attribute__((aligned(16))) char smem[]; \
    const int wid_ = uni(threadIdx.x >> 6); const bool prod = wid_ >= 8; \
    Ctx cx = {ws, (lds_ptr)smem, uni((unsigned)(uintptr_t)(lds_ptr)smem), (int)blockIdx.x, prod ? wid_ - 8 : wid_, 0ull}; \
    if (prod) __builtin_amdgcn_s_setprio(3)
__global__ __launch_bounds__(768, 3) void k_main(const u16* __restrict__ x, const u16* __restrict__ wq, const u16* __restrict__ wk, const u16* __restrict__ wv, const u16* __restrict__ wo,
                                                 const u16* __restrict__ wg, const u16* __restrict__ wu, const u16* __restrict__ wd, const u16* __restrict__ c, const u16* __restrict__ wmod,
                                                 char* __restrict__ ws, u16* __restrict__ out) {
    CTX_INIT;
    if (prod) {
        OPre o;
        ln1kv_produce(cx, wq, wk, wv, c, wmod, [&]() { attn_pre(cx); });
        attn_produce(cx, wo, wg, wu, wd, c, wmod, [&]() { oproj_pre(cx, o); });
        oproj_produce(cx, o, x, [&]() { glu_pre(cx); });
        glu_produce(cx, [&]() { down_pre(cx, o); });
        down_produce(cx, o);
    } else {
        ln1kv_consume(cx, x, c, wmod);
        attn_consume(cx);
        oproj_consume(cx);
        glu_consume(cx);
        down_consume(cx, out);
    }
}

extern "C" void kernel_launch(void* const* d_in, const int* in_sizes, int n_in, void* d_out, int out_size, void* d_ws, size_t ws_size, hipStream_t stream) {
    constexpr int LDS = 163840;
    (void)hipFuncSetAttribute((const void*)k_main, hipFuncAttributeMaxDynamicSharedMemorySize, LDS);
    k_main<<<256, 768, LDS, stream>>>((const u16*)d_in[0], (const u16*)d_in[1], (const u16*)d_in[2], (const u16*)d_in[3], (const u16*)d_in[4], (const u16*)d_in[6], (const u16*)d_in[7],
                                      (const u16*)d_in[8], (const u16*)d_in[9], (const u16*)d_in[10], (char*)d_ws, (u16*)d_out);
}
```

```cpp
#include <hip/hip_runtime.h>
#include <hip/hip_bf16.h>

#define LAS __attribute__((address_space(3)))
#define GAS __attribute__((address_space(1)))
typedef unsigned short u16;
typedef short bf16x8 __attribute__((ext_vector_type(8)));
typedef short s16x4 __attribute__((ext_vector_type(4)));
typedef float f32x2 __attribute__((ext_vector_type(2)));
typedef float f32x4 __attribute__((ext_vector_type(4)));
typedef float f32x16 __attribute__((ext_vector_type(16)));
typedef unsigned u32x4 __attribute__((ext_vector_type(4)));
typedef unsigned u32x2 __attribute__((ext_vector_type(2)));
typedef __bf16 bf16x2v __attribute__((ext_vector_type(2)));
typedef LAS char* lds_ptr;

constexpr int D = 1024, F = 3584;
constexpr size_t WS_CNT = 0;
constexpr int C_KV = 0, C_SIDE = 16, C_ST = 32, C_EARLY = 40, C_NUM = 56;
constexpr size_t WS_FLG = 1u << 20;
constexpr int F_H1 = 0, F_O = 256, F_Y = 512, F_HH = 768, F_WQ = 1024;
constexpr size_t WS_EPOCH = 98304 + 4096;
constexpr size_t WS_MP1 = 32768;
constexpr size_t WS_MP2 = 786432;
constexpr size_t WS_KN = 98304;
constexpr size_t WS_KV = 262144;
constexpr size_t WS_ST2 = 524288;
constexpr size_t WS_UT = (1u << 20) + 262144;
constexpr size_t WS_H1 = 2u << 20;
constexpr size_t WS_O = WS_H1 + (4u << 20);
constexpr size_t WS_X1 = WS_O + (4u << 20);
constexpr size_t WS_Y = WS_X1 + (4u << 20);
constexpr size_t WS_HH = WS_Y + (4u << 20);
constexpr size_t WS_XCH = WS_HH + (16u << 20);
constexpr size_t WS_WQ = WS_XCH + (4u << 20);
constexpr size_t WS_WO = WS_WQ + (2u << 20);
constexpr size_t WS_WGU = WS_WO + (2u << 20);
constexpr size_t WS_WD = WS_WGU + (16u << 20);

__device__ __forceinline__ float bf2f(u16 b) { return __uint_as_float(((unsigned)b) << 16); }
__device__ __forceinline__ float bflo(unsigned w) { return __uint_as_float(w << 16); }
__device__ __forceinline__ float bfhi(unsigned w) { return __uint_as_float(w & 0xffff0000u); }
__device__ __forceinline__ unsigned cvtpk(float lo, float hi) { return __builtin_bit_cast(unsigned, __builtin_convertvector((f32x2){lo, hi}, bf16x2v)); }
__device__ __forceinline__ float rbf(float x) { return bflo(cvtpk(x, x)); }
__device__ __forceinline__ f32x4 rbf4(f32x4 s) { return (f32x4){rbf(s[0]), rbf(s[1]), rbf(s[2]), rbf(s[3])}; }

__device__ __forceinline__ void dma16(const void* gbase, unsigned voff, unsigned ldsaddr) {
    asm volatile("s_mov_b32 m0, %0\n\ts_nop 3\n\tglobal_load_lds_dwordx4 %1, %2" ::"s"(ldsaddr), "v"(voff), "s"(gbase) : "memory");
}
__device__ __forceinline__ void dmant(const void* gbase, unsigned voff, unsigned ldsaddr) {
    asm volatile("s_mov_b32 m0, %0\n\ts_nop 3\n\tglobal_load_lds_dwordx4 %1, %2 nt" ::"s"(ldsaddr), "v"(voff), "s"(gbase) : "memory");
}
__device__ __forceinline__ void dmac(const void* gbase, unsigned voff, unsigned ldsaddr) {
    asm volatile("s_mov_b32 m0, %0\n\ts_nop 3\n\tglobal_load_lds_dwordx4 %1, %2 sc1" ::"s"(ldsaddr), "v"(voff), "s"(gbase) : "memory");
}
#define LD16(dst, base, voff) asm volatile("s_nop 4\n\tglobal_load_dwordx4 %0, %1, %2" : "=v"(dst) : "v"(voff), "s"(base) : "memory")
#define LD16C(dst, base, voff) asm volatile("s_nop 4\n\tglobal_load_dwordx4 %0, %1, %2 sc1" : "=v"(dst) : "v"(voff), "s"(base) : "memory")
#define LD8C(dst, base, voff) asm volatile("s_nop 4\n\tglobal_load_dwordx2 %0, %1, %2 sc1" : "=v"(dst) : "v"(voff), "s"(base) : "memory")
#define TOUCH(reg, base, voff) asm volatile("s_nop 4\n\tglobal_load_dword %0, %1, %2 sc1" : "+v"(reg) : "v"(voff), "s"(base) : "memory")
template <class T> __device__ __forceinline__ void st16(const void* base, unsigned voff, T v) {
    asm volatile("s_nop 4\n\tglobal_store_dwordx4 %0, %1, %2 sc1\n\ts_nop 1" ::"v"(voff), "v"(v), "s"(base) : "memory");
}
template <class T> __device__ __forceinline__ void st16o(const void* base, unsigned voff, T v) {
    asm volatile("s_nop 4\n\tglobal_store_dwordx4 %0, %1, %2 sc1 nt\n\ts_nop 1" ::"v"(voff), "v"(v), "s"(base) : "memory");
}
template <class T> __device__ __forceinline__ void st8(const void* base, unsigned voff, T v) { asm volatile("s_nop 4\n\tglobal_store_dwordx2 %0, %1, %2 sc1" ::"v"(voff), "v"(v), "s"(base) : "memory"); }
__device__ __forceinline__ void st4(const void* base, unsigned voff, float v) { asm volatile("s_nop 4\n\tglobal_store_dword %0, %1, %2 sc1" ::"v"(voff), "v"(v), "s"(base) : "memory"); }
#define WAIT_V(n) asm volatile("s_waitcnt vmcnt(" #n ")" ::: "memory")
#define BARRIER() asm volatile("s_barrier" ::: "memory")
#define BAR_L() asm volatile("s_waitcnt lgkmcnt(0)\n\ts_barrier" ::: "memory")
#define WAITV_BAR(n) asm volatile("s_waitcnt vmcnt(" #n ")\n\ts_barrier" ::: "memory")
#define WAITVL_BAR(n) asm volatile("s_waitcnt vmcnt(" #n ") lgkmcnt(0)\n\ts_barrier" ::: "memory")
#define PIN(x) asm volatile("" : "+v"(x))
#define SBAR() __builtin_amdgcn_sched_barrier(0)
#define MFMA32(a, b, c) __builtin_amdgcn_mfma_f32_32x32x16_bf16(a, b, c, 0, 0, 0)
#define MFMA16(a, b, c) __builtin_amdgcn_mfma_f32_16x16x32_bf16(a, b, c, 0, 0, 0)
#define MFMA16_ACC(c, a, b) asm volatile("v_mfma_f32_16x16x32_bf16 %0, %1, %2, %0" : "+v"(c) : "v"(a), "v"(b))
#define MFMA32_ACC(c, a, b) asm volatile("v_mfma_f32_32x32x16_bf16 %0, %1, %2, %0" : "+v"(c) : "v"(a), "v"(b))
#define ZERO4 ((f32x4){0.f, 0.f, 0.f, 0.f})
__device__ __forceinline__ bf16x8 ldsr128(lds_ptr p) { return *(const LAS bf16x8*)p; }
__device__ __forceinline__ s16x4 ldstr(lds_ptr p) { return __builtin_amdgcn_ds_read_tr16_b64_v4i16((LAS s16x4*)p); }
__device__ __forceinline__ bf16x8 ldstr2(lds_ptr p, int d) { s16x4 a = ldstr(p), b = ldstr(p + d); return (bf16x8){a[0], a[1], a[2], a[3], b[0], b[1], b[2], b[3]}; }
__device__ __forceinline__ unsigned uni(unsigned x) { return (unsigned)__builtin_amdgcn_readfirstlane((int)x); }
__device__ __forceinline__ int lane_id() { int l = __builtin_amdgcn_mbcnt_hi(~0u, __builtin_amdgcn_mbcnt_lo(~0u, 0u)); PIN(l); return l; }
__device__ __forceinline__ u32x4 swap16(unsigned a0, unsigned a1, unsigned b0, unsigned b1) {
    const auto r0 = __builtin_amdgcn_permlane16_swap(a0, b0, false, false), r1 = __builtin_amdgcn_permlane16_swap(a1, b1, false, false);
    return (u32x4){r0[0], r1[0], r0[1], r1[1]};
}
__device__ __forceinline__ u32x4 swap32(unsigned a0, unsigned a1, unsigned b0, unsigned b1) {
    const auto r0 = __builtin_amdgcn_permlane32_swap(a0, b0, false, false), r1 = __builtin_amdgcn_permlane32_swap(a1, b1, false, false);
    return (u32x4){r0[0], r1[0], r0[1], r1[1]};
}


template <int CTRL> __device__ __forceinline__ float dppf(float v) { return __builtin_bit_cast(float, __builtin_amdgcn_mov_dpp(__builtin_bit_cast(int, v), CTRL, 0xf, 0xf, true)); }
__device__ __forceinline__ float half_sum(float v) { const auto r = __builtin_amdgcn_permlane32_swap(__float_as_uint(v), __float_as_uint(v), false, false); return __uint_as_float(r[0]) + __uint_as_float(r[1]); }
__device__ __forceinline__ float rows_sum(float v) {
    const auto r = __builtin_amdgcn_permlane16_swap(__float_as_uint(v), __float_as_uint(v), false, false); return half_sum(__uint_as_float(r[0]) + __uint_as_float(r[1]));
}
__device__ __forceinline__ float rows_max(float v) {
    auto r = __builtin_amdgcn_permlane16_swap(__float_as_uint(v), __float_as_uint(v), false, false); v = fmaxf(__uint_as_float(r[0]), __uint_as_float(r[1]));
    r = __builtin_amdgcn_permlane32_swap(__float_as_uint(v), __float_as_uint(v), false, false); return fmaxf(__uint_as_float(r[0]), __uint_as_float(r[1]));
}
__device__ __forceinline__ float row16_sum(float v) { v += dppf<0xB1>(v); v += dppf<0x4E>(v); v += dppf<0x141>(v); v += dppf<0x140>(v); return v; }
__device__ __forceinline__ float row16_max(float v) { v = fmaxf(v, dppf<0xB1>(v)); v = fmaxf(v, dppf<0x4E>(v)); v = fmaxf(v, dppf<0x141>(v)); v = fmaxf(v, dppf<0x140>(v)); return v; }
__device__ __forceinline__ float wsum(float v) { return rows_sum(row16_sum(v)); }
__device__ __forceinline__ float wmax(float v) { return rows_max(row16_max(v)); }
__device__ __forceinline__ f32x4 sum8(f32x4 (&p)[8]) {
    asm volatile("" : "+v"(p[0]), "+v"(p[1]), "+v"(p[2]), "+v"(p[3]), "+v"(p[4]), "+v"(p[5]), "+v"(p[6]), "+v"(p[7]));
    return ((p[0] + p[1]) + (p[2] + p[3])) + ((p[4] + p[5]) + (p[6] + p[7]));
}

__device__ __forceinline__ void signal(char* ws, int idx, int) {
    unsigned long long sv;
    asm volatile("s_mov_b64 %0, exec\n\ts_mov_b64 exec, 1\n\ts_nop 2\n\tglobal_atomic_add %1, %2, %3\n\ts_mov_b64 exec, %0" : "=&s"(sv) : "v"(idx * 256), "v"(1u), "s"(ws + WS_CNT) : "memory");
}
template <int SLEEP>
__device__ __forceinline__ void await(char* ws, int idx, int n, unsigned need, int lane) {
    {
        GAS unsigned* p = (GAS unsigned*)(ws + WS_CNT) + (idx + (lane < n ? lane : 0)) * 64;
        for (unsigned spins = 0; spins < (1u << 15); ++spins) {
            if (__all(__hip_atomic_load(p, __ATOMIC_RELAXED, __HIP_MEMORY_SCOPE_AGENT) >= need)) break;
            __builtin_amdgcn_s_sleep(SLEEP);
        }
        asm volatile("s_waitcnt vmcnt(0)" ::: "memory");
    }
}
__device__ __forceinline__ void raise(char* ws, int idx, unsigned long long tag, int) {
    unsigned long long sv;
    asm volatile("s_mov_b64 %0, exec\n\ts_mov_b64 exec, 1\n\ts_nop 2\n\tglobal_store_dwordx2 %1, %2, %3 sc1\n\ts_mov_b64 exec, %0" : "=&s"(sv) : "v"(idx * 128), "v"(tag), "s"(ws + WS_FLG) : "memory");
}
__device__ __forceinline__ void expect(char* ws, int idx0, int off, unsigned long long tag) {
    GAS unsigned long long* p = (GAS unsigned long long*)(ws + WS_FLG) + (idx0 + (off < 0 ? 0 : off)) * 16;
    for (unsigned spins = 0; spins < (1u << 15); ++spins) {
        if (__all(off < 0 || __hip_atomic_load(p, __ATOMIC_RELAXED, __HIP_MEMORY_SCOPE_AGENT) == tag)) break;
        __builtin_amdgcn_s_sleep(2);
    }
    asm volatile("s_waitcnt vmcnt(0)" ::: "memory");
}
__device__ __forceinline__ void drain_raise(char* ws, lds_ptr word, int idx, unsigned long long tag, int lane) {
    asm volatile("s_waitcnt vmcnt(0)" ::: "memory");
    unsigned old = 0u;
    if (lane == 0) old = __hip_atomic_fetch_add((LAS unsigned*)word, 1u, __ATOMIC_RELAXED, __HIP_MEMORY_SCOPE_WORKGROUP);
    if (uni(old) == 7u) raise(ws, idx, tag, lane);
}
#define INPUT_BARRIER() BAR_L()

__device__ __forceinline__ void pack_load(const char* src  , unsigned pitch, unsigned ldsaddr, int lane) {
    const unsigned vo = (unsigned)(lane >> 3) * pitch, sw = (lane & 7) ^ (((lane >> 4) & 1) << 1);
#pragma unroll
    for (int i = 0; i < 4; ++i) dmant(src + (size_t)i * 8 * pitch, vo + ((sw ^ ((i & 1) << 2)) << 4), ldsaddr + i * 1024);
}
__device__ __forceinline__ bf16x8 pack_frag(lds_ptr area, int nf, int lane) {
    const int g = lane >> 4, q = (lane & 15) >> 2;
    return ldstr2(area + (8 * g + q) * 128 + ((nf ^ (((q >> 1) & 1) | ((g & 1) << 1))) << 5) + (lane & 3) * 8, 512);
}
__device__ __forceinline__ void mod_unit(lds_ptr area, bf16x8 sc, lds_ptr red, int lane) {
    f32x4 d[4];
#pragma unroll
    for (int nf = 0; nf < 4; ++nf) d[nf] = MFMA16(pack_frag(area, nf, lane), sc, ZERO4);
    if ((lane & 15) == 0) {
#pragma unroll
        for (int nf = 0; nf < 4; ++nf) *(LAS f32x4*)(red + nf * 64 + (lane >> 4) * 16) = d[nf];
    }
}

__device__ __forceinline__ float silu_bf(float cv) { return rbf(cv / (1.f + __expf(-cv))); }
__device__ __forceinline__ bf16x8 silu_frag(u32x4 cw) {
    PIN(cw);
    u32x4 r;
#pragma unroll
    for (int j = 0; j < 4; ++j) r[j] = cvtpk(silu_bf(bflo(cw[j])), silu_bf(bfhi(cw[j])));
    return __builtin_bit_cast(bf16x8, r);
}

__device__ __forceinline__ void tile128x64(int bid, int& rb, int& ct) { const int xcd = bid & 7, idx = bid >> 3; rb = (xcd >> 1) * 4 + (idx & 3); ct = (xcd & 1) * 8 + (idx >> 2); }
__device__ __forceinline__ unsigned long long epoch_load(char* ws) { return __hip_atomic_load((GAS unsigned long long*)(ws + WS_EPOCH), __ATOMIC_RELAXED, __HIP_MEMORY_SCOPE_AGENT); }
__device__ __forceinline__ unsigned long long epoch_tag(unsigned long long e) { return ((((unsigned long long)uni((unsigned)(e >> 32)) << 32) | uni((unsigned)e)) + 1ull) << 24; }
__device__ __forceinline__ void tile_down(int bid, int& rb, int& ct, int& kh) { const int xcd = bid & 7, idx = bid >> 3; kh = xcd & 1; rb = (xcd >> 1) * 4 + (idx & 3); ct = idx >> 2; }
struct Ctx { char* ws; lds_ptr lds; unsigned lds0; int bid, w; unsigned long long tag;
};

__device__ __forceinline__ void ln1kv_consume(Ctx& cx, const u16* __restrict__ x, const u16* __restrict__ c, const u16* __restrict__ wmod) {
    char* ws = cx.ws; const lds_ptr lds = cx.lds; const int wid = cx.w, lane = lane_id(), tid = wid * 64 + lane;
    const int rb = (cx.bid >> 4) * 8 + (cx.bid & 7), isv = (cx.bid >> 3) & 1;
    const int idx = cx.bid >> 3, cs = ((((idx & 3) << 3) | (cx.bid & 7)) << 1) | ((idx >> 2) & 1), kq = idx >> 3, kr = kq * 256 + (tid >> 2);
    const u32x4 w0 = __builtin_nontemporal_load((const u32x4*)(wmod + (size_t)kr * 6144 + cs * 32 + (tid & 3) * 8)), w1 = __builtin_nontemporal_load((const u32x4*)(wmod + (size_t)(kr + 128) * 6144 + cs * 32 + (tid & 3) * 8));
    const u16 c0 = c[kr], c1 = c[kr + 128];
    const unsigned long long ep = epoch_load(ws);
    u32x4 xv[2][2];
    {
        const float s0 = silu_bf(bf2f(c0)), s1 = silu_bf(bf2f(c1));
        float a[8];
#pragma unroll
        for (int j = 0; j < 4; ++j) { a[2 * j] = s0 * bflo(w0[j]) + s1 * bflo(w1[j]); a[2 * j + 1] = s0 * bfhi(w0[j]) + s1 * bfhi(w1[j]); }
#pragma unroll
        for (int j = 0; j < 8; ++j) PIN(a[j]);
        asm volatile("" ::: "memory");
#pragma unroll
        for (int rr = 0; rr < 2; ++rr)
#pragma unroll
            for (int hh = 0; hh < 2; ++hh) xv[rr][hh] = __builtin_nontemporal_load((const u32x4*)(x + (size_t)(rb * 16 + 2 * wid + rr) * D + hh * 512 + lane * 8));
        SBAR();
#pragma unroll
        for (int j = 0; j < 8; ++j) { a[j] += dppf<0x124>(a[j]); a[j] += dppf<0x128>(a[j]); a[j] = rows_sum(a[j]); }
        cx.tag = epoch_tag(ep);
        asm volatile("" : "+s"(cx.tag));
        if (lane < 4) { *(LAS f32x4*)(lds + 114688 + wid * 128 + lane * 32) = (f32x4){a[0], a[1], a[2], a[3]}; *(LAS f32x4*)(lds + 114688 + wid * 128 + lane * 32 + 16) = (f32x4){a[4], a[5], a[6], a[7]}; }
    }
    BAR_L();
    const unsigned long long tag = cx.tag;
    if (wid == 0 && lane < 32) {
        const LAS float* r = (const LAS float*)(lds + 114688) + lane;
        const unsigned pv = __float_as_uint(((r[0] + r[32]) + (r[64] + r[96])) + ((r[128] + r[160]) + (r[192] + r[224])));
        __hip_atomic_store((GAS unsigned long long*)(ws + WS_MP1) + (kq * 2048 + ((cs * 32 + 1024) & 2047) + lane), tag | ((pv + 0x80u) >> 8), __ATOMIC_RELAXED, __HIP_MEMORY_SCOPE_AGENT);
    }
    float xn[2][16];
#pragma unroll
    for (int rr = 0; rr < 2; ++rr) {
        float* v = xn[rr];
#pragma unroll
        for (int hh = 0; hh < 2; ++hh)
#pragma unroll
            for (int j = 0; j < 4; ++j) { v[hh * 8 + 2 * j] = bflo(xv[rr][hh][j]); v[hh * 8 + 2 * j + 1] = bfhi(xv[rr][hh][j]); }
        float sm = 0.f;
#pragma unroll
        for (int j = 0; j < 16; ++j) sm += v[j];
        const float mean = wsum(sm) * (1.f / 1024.f);
        float q = 0.f;
#pragma unroll
        for (int j = 0; j < 16; ++j) { v[j] -= mean; q += v[j] * v[j]; }
        const float rstd = 1.f / sqrtf(wsum(q) * (1.f / 1024.f) + 1e-5f);
#pragma unroll
        for (int j = 0; j < 16; ++j) { v[j] *= rstd; PIN(v[j]); }
    }
    LAS float* tab = (LAS float*)(lds + 98304);
    {
        u32x4 g[4][2];
        const unsigned tl = (unsigned)tag, th = (unsigned)(tag >> 32);
        __builtin_amdgcn_s_sleep(18);
        for (unsigned spins = 0; spins < (1u << 15); ++spins) {
            asm volatile("s_nop 4\n\tglobal_load_dwordx4 %0, %8, %12 sc1\n\tglobal_load_dwordx4 %1, %8, %13 sc1\n\tglobal_load_dwordx4 %2, %9, %12 sc1\n\tglobal_load_dwordx4 %3, %9, %13 sc1\n\t"
                         "global_load_dwordx4 %4, %10, %12 sc1\n\tglobal_load_dwordx4 %5, %10, %13 sc1\n\tglobal_load_dwordx4 %6, %11, %12 sc1\n\tglobal_load_dwordx4 %7, %11, %13 sc1\n\ts_waitcnt vmcnt(0)"
                         : "=&v"(g[0][0]), "=&v"(g[0][1]), "=&v"(g[1][0]), "=&v"(g[1][1]), "=&v"(g[2][0]), "=&v"(g[2][1]), "=&v"(g[3][0]), "=&v"(g[3][1])
                         : "v"(tid * 16), "v"(tid * 16 + 16384), "v"(tid * 16 + 32768), "v"(tid * 16 + 49152), "s"(ws + WS_MP1), "s"(ws + WS_MP1 + 8192) : "memory");
            unsigned bad = 0u;
#pragma unroll
            for (int k = 0; k < 4; ++k)
                bad |= ((g[k][0][0] ^ tl) | (g[k][0][2] ^ tl) | (g[k][1][0] ^ tl) | (g[k][1][2] ^ tl)) >> 24 | (g[k][0][1] ^ th) | (g[k][0][3] ^ th) | (g[k][1][1] ^ th) | (g[k][1][3] ^ th);
            if (__all(bad == 0u)) break;
            __builtin_amdgcn_s_sleep(8);
        }
        f32x4 v;
#pragma unroll
        for (int j = 0; j < 4; ++j)
            v[j] = (__uint_as_float(g[0][j >> 1][(j & 1) * 2] << 8) + __uint_as_float(g[1][j >> 1][(j & 1) * 2] << 8)) + (__uint_as_float(g[2][j >> 1][(j & 1) * 2] << 8) + __uint_as_float(g[3][j >> 1][(j & 1) * 2] << 8));
        v = rbf4(v);
        *(LAS f32x2*)(tab + tid * 2) = (f32x2){rbf(1.f + v[0]), rbf(1.f + v[1])};
        *(LAS f32x2*)(tab + 1024 + tid * 2) = (f32x2){v[2], v[3]};
    }
    BAR_L();
    if (cx.bid == 0 && tid == 0) __hip_atomic_store((GAS unsigned long long*)(ws + WS_EPOCH), tag >> 24, __ATOMIC_RELAXED, __HIP_MEMORY_SCOPE_AGENT);
    const lds_ptr img = lds + 65536;
#pragma unroll
    for (int hh = 0; hh < 2; ++hh) {
        const int c = hh * 64 + lane;
        const f32x4 g0 = *(const LAS f32x4*)(tab + c * 8), g1 = *(const LAS f32x4*)(tab + c * 8 + 4);
        const f32x4 b0 = *(const LAS f32x4*)(tab + 1024 + c * 8), b1 = *(const LAS f32x4*)(tab + 1024 + c * 8 + 4);
#pragma unroll
        for (int rr = 0; rr < 2; ++rr) {
            float h[8];
#pragma unroll
            for (int j = 0; j < 4; ++j) { h[j] = xn[rr][hh * 8 + j] * g0[j] + b0[j]; h[4 + j] = xn[rr][hh * 8 + 4 + j] * g1[j] + b1[j]; }
            *(LAS u32x4*)(img + c * 256 + (((2 * wid + rr) ^ (c & 15)) << 4)) = (u32x4){cvtpk(h[0], h[1]), cvtpk(h[2], h[3]), cvtpk(h[4], h[5]), cvtpk(h[6], h[7])};
        }
    }
    BAR_L();
    f32x4 acc[2] = {ZERO4, ZERO4};
    const int g = lane >> 4, q = (lane & 15) >> 2, p = lane & 3;
    bf16x8 hf[4], wf[4][2];
#pragma unroll
    for (int i = 0; i < 4; ++i) { const int cc = 4 * (wid * 4 + i) + g; hf[i] = ldsr128(img + cc * 256 + (((lane & 15) ^ (cc & 15)) << 4)); }
    const char* hb = ws + WS_H1 + ((size_t)rb * 128 + 16 * wid) * 256;
    if (isv) { st16(hb, lane * 16 + 1024, hf[1]); st16(hb, lane * 16 + 3072, hf[3]); } else { st16(hb, lane * 16, hf[0]); st16(hb, lane * 16 + 2048, hf[2]); }
#pragma unroll
    for (int i = 0; i < 4; ++i)
#pragma unroll
        for (int nf = 0; nf < 2; ++nf) wf[i][nf] = ldstr2(lds + (32 * (wid * 4 + i) + 8 * g + q) * 64 + ((nf ^ (g & 1)) << 5) + 8 * p, 256);
    LAS float* red = (LAS float*)(lds + 147456);
    if (isv) {
#pragma unroll
        for (int i = 0; i < 4; ++i) { acc[0] = MFMA16(hf[i], wf[i][0], acc[0]); acc[1] = MFMA16(hf[i], wf[i][1], acc[1]); }
#pragma unroll
        for (int nf = 0; nf < 2; ++nf) *(LAS f32x4*)(red + wid * 512 + (nf * 16 + (lane & 15)) * 16 + 4 * g) = acc[nf];
    } else {
#pragma unroll
        for (int i = 0; i < 4; ++i) { acc[0] = MFMA16(wf[i][0], hf[i], acc[0]); acc[1] = MFMA16(wf[i][1], hf[i], acc[1]); }
#pragma unroll
        for (int nf = 0; nf < 2; ++nf) *(LAS f32x4*)(red + wid * 512 + (lane & 15) * 32 + nf * 16 + 4 * g) = acc[nf];
    }
    WAITVL_BAR(0);
    if (wid == 0) {
        const int o0 = isv ? (lane & 31) * 16 + 4 * (lane >> 5) : (lane & 15) * 32 + (g >> 1) * 16 + (g & 1) * 4, o1 = o0 + 8;
        f32x4 a = ZERO4, b = ZERO4;
#pragma unroll
        for (int w = 0; w < 8; ++w) { a += *(const LAS f32x4*)(red + w * 512 + o0); b += *(const LAS f32x4*)(red + w * 512 + o1); }
        const u32x4 v = {cvtpk(a[0], a[1]), cvtpk(a[2], a[3]), cvtpk(b[0], b[1]), cvtpk(b[2], b[3])};
        st16(ws + WS_KV + (size_t)(rb >> 1) * 4096, isv ? 2048 + (rb & 1) * 1024 + lane * 16 : (g >> 1) * 1024 + ((g & 1) * 32 + (rb & 1) * 16 + (lane & 15)) * 16, v);
        float n2 = 0.f, mx = 0.f;
#pragma unroll
        for (int j = 0; j < 4; ++j) { n2 += bflo(v[j]) * bflo(v[j]) + bfhi(v[j]) * bfhi(v[j]); mx = fmaxf(mx, fmaxf(fabsf(bflo(v[j])), fabsf(bfhi(v[j])))); }
        if (!isv) mx = rows_sum(n2);
        if (!(n2 - n2 == 0.f)) mx = INFINITY;
        mx = wmax(mx);
        if (lane == 0) st4(ws + WS_KN, (unsigned)(isv * 128 + rb) * 4u, mx);
    }
    WAITV_BAR(0);
}
template <class Next>
__device__ __forceinline__ void ln1kv_produce(Ctx& cx, const u16* __restrict__ wq, const u16* __restrict__ wk, const u16* __restrict__ wv, Next next_pre) {
    char* ws = cx.ws; const int pw = cx.w, lane = lane_id();
    cx.tag = epoch_tag(epoch_load(ws));
    asm volatile("" : "+s"(cx.tag));
    int rbq, hp; tile128x64(cx.bid, rbq, hp);
    if (cx.bid == 0 && pw == 2) { if (lane < C_NUM) st4(ws + WS_CNT, (unsigned)lane * 256u, 0.f); WAIT_V(0); }
    BARRIER();
    if (pw < 2) pack_load((const char*)wq + (size_t)(rbq * 2 + pw) * 65536 + hp * 128, 2048, cx.lds0 + 106496 + pw * 4096, lane);
    {
        const char* w = (const char*)(((cx.bid >> 3) & 1) ? wv : wk) + pw * 16384;
        const unsigned voff = (lane >> 2) * 64 + (((lane & 3) ^ (((lane >> 5) & 1) << 1)) << 4);
#pragma unroll
        for (int i = 0; i < 16; ++i) dma16(w + i * 1024, voff, cx.lds0 + pw * 16384 + i * 1024);
    }
    if (pw < 2) {
        WAIT_V(16);
#pragma unroll
        for (int nf = 0; nf < 4; ++nf) st16(ws + WS_WQ + (size_t)(hp * 32 + rbq * 2 + pw) * 4096, lane * 16 + nf * 1024, pack_frag(cx.lds + 106496 + pw * 4096, nf, lane));
    }
    WAITV_BAR(0);
    if (pw == 0) raise(ws, F_WQ + hp * 16 + rbq, cx.tag, lane);
    BARRIER();
    expect(ws, F_WQ + hp * 16, lane < 16 ? lane : -1, cx.tag);
    BARRIER();
    if (pw == 0) raise(ws, F_H1 + cx.bid, cx.tag, lane);
    next_pre();
}

template <int WB, int NPRE>
__device__ __forceinline__ void g64_pre(const Ctx& cx, const char* Wp, int lane) {
#pragma unroll
    for (int t = 0; t < NPRE; ++t) { const unsigned d = cx.lds0 + (WB + t * 8192) % 163840 + cx.w * 2048; dmac(Wp + cx.w * 2048 + t * 8192, lane * 16, d); dmac(Wp + cx.w * 2048 + t * 8192 + 1024, lane * 16, d + 1024); }
}
template <int WB, int AB, int NPRE, int NTAIL, class Issued, class First, class Rest, class Tail>
__device__ __forceinline__ void g64_produce(const Ctx& cx, const char* Ap, const char* Wp, int lane, Issued issued, First first, Rest rest, Tail tail) {
    const char* Aw = Ap + (size_t)(2 * cx.w) * 32768; const char* Ww = Wp + cx.w * 2048;
    const unsigned voff = lane * 16, dA = cx.lds0 + AB + cx.w * 4096, dW = cx.lds0 + cx.w * 2048;
#define G64_A(t) do { const char* a_ = Aw + (t) * 2048; const unsigned d_ = dA + ((t) % 5) * 16384; dmac(a_, voff, d_); dmac(a_ + 32768, voff, d_ + 2048); dmac(a_ + 1024, voff, d_ + 1024); dmac(a_ + 33792, voff, d_ + 3072); } while (0)
#define G64_W(t, slot) do { const unsigned d_ = dW + (WB + (slot) * 8192) % 163840; dmac(Ww + (t) * 8192, voff, d_); dmac(Ww + (t) * 8192 + 1024, voff, d_ + 1024); } while (0)
    G64_A(0); G64_A(1);
    issued();
    WAITV_BAR(4);
    G64_A(2); G64_A(3); G64_A(4);
    constexpr int E = (10 - NPRE) * 2;
    if (NPRE == 6) { G64_W(6, 6); G64_W(7, 7); G64_W(8, 8); G64_W(9, 9); }
    first();
#define G64_BAR(n) asm volatile("s_waitcnt vmcnt(%0)\n\ts_barrier" ::"i"(n) : "memory")
    G64_BAR(12 + E);
    rest();
    G64_BAR(8 + E); G64_A(5); G64_W(10, 0);
    G64_BAR(10 + E); G64_A(6); G64_W(11, 1);
    G64_BAR(12 + E); G64_A(7); G64_W(12, 2);
    G64_BAR(14); G64_A(8); G64_W(13, 3);
    G64_BAR(14); G64_A(9);
    G64_BAR(12); G64_A(10);
    G64_BAR(10); G64_A(11);
    G64_BAR(8); G64_A(12);
    tail();
    G64_BAR(8 + NTAIL); G64_A(13); G64_W(14, 8);
    G64_BAR(10 + NTAIL); G64_A(14); G64_W(15, 9);
    G64_BAR(12 + NTAIL); G64_A(15);
    G64_BAR(12);
    G64_BAR(6);
    WAITVL_BAR(0);
#undef G64_BAR
#undef G64_W
#undef G64_A
}
template <int WB, int AB, class Start, class Mid, class Late, class St>
__device__ __forceinline__ void g64_consume(f32x16& acc, const Ctx& cx, int lane, Start start, Mid mid, Late late, St stamp) {
    const int lo = (lane >> 5) * 256 + (lane & 15) * 16;
    const lds_ptr pa = cx.lds + AB + (cx.w & 3) * 4096 + ((lane >> 4) & 1) * 2048 + lo, pw = cx.lds + (cx.w >> 2) * 2048 + ((lane >> 4) & 1) * 1024 + lo;
    acc = (f32x16){};
    bf16x8 aA[4], wA[4], aB[4], wB[4];
    start();
    BARRIER(); SBAR();
    stamp();
#pragma unroll
    for (int k = 0; k < 4; ++k) { wA[k] = ldsr128(pw + WB + (k >> 1) * 4096 + (k & 1) * 512); aA[k] = ldsr128(pa + k * 512); }
    SBAR();
    int sa = 16384, sw = WB + 8192;
#define G64_STEP(AC, WC, AN, WN, DO_NEXT, HOOK) do { \
        if (DO_NEXT) BARRIER(); SBAR(); HOOK; \
        const lds_ptr pwn = pw + sw, pan = pa + sa; \
        MFMA32_ACC(acc, WC[0], AC[0]); SBAR(); \
        if (DO_NEXT) { WN[0] = ldsr128(pwn); AN[0] = ldsr128(pan); WN[1] = ldsr128(pwn + 512); } SBAR(); \
        MFMA32_ACC(acc, WC[1], AC[1]); SBAR(); \
        if (DO_NEXT) { AN[1] = ldsr128(pan + 512); WN[2] = ldsr128(pwn + 4096); AN[2] = ldsr128(pan + 1024); } SBAR(); \
        MFMA32_ACC(acc, WC[2], AC[2]); SBAR(); \
        if (DO_NEXT) { WN[3] = ldsr128(pwn + 4608); AN[3] = ldsr128(pan + 1536); } SBAR(); \
        MFMA32_ACC(acc, WC[3], AC[3]); SBAR(); \
        sa = sa == 65536 ? 0 : sa + 16384; sw = sw == (WB + 9 * 8192) % 163840 ? WB : (sw == 155648 ? 0 : sw + 8192); } while (0)
#define G64_PAIR() do { G64_STEP(aA, wA, aB, wB, true, (void)0); G64_STEP(aB, wB, aA, wA, true, (void)0); } while (0)
#pragma unroll 1
    for (int t = 0; t < 4; t += 2) G64_PAIR();
    mid(4);
#pragma unroll 1
    for (int t = 4; t < 8; t += 2) G64_PAIR();
    mid(8);
    G64_PAIR();
    mid(10);
    G64_PAIR();
    G64_STEP(aA, wA, aB, wB, true, (void)0);
    sw = (WB + 8 * 8192) % 163840;
    G64_STEP(aB, wB, aA, wA, true, (void)0);
    G64_STEP(aA, wA, aB, wB, true, late());
    G64_STEP(aB, wB, aA, wA, false, (void)0);
    asm volatile("s_nop 7\n\ts_nop 7" : "+v"(acc));
    SBAR();
#undef G64_PAIR
#undef G64_STEP
}

__device__ __forceinline__ void attn_pre(const Ctx& cx) { int rb, hp; tile128x64(cx.bid, rb, hp); g64_pre<0, 6>(cx, cx.ws + WS_WQ + (size_t)hp * 131072, lane_id()); }
template <class Next>
__device__ __forceinline__ void attn_produce(const Ctx& cx, const u16* __restrict__ wo, const u16* __restrict__ wg, const u16* __restrict__ wu, const u16* __restrict__ wd, const u16* __restrict__ c, const u16* __restrict__ wmod, Next next_pre) {
    char* ws = cx.ws; const int pw = cx.w, lane = lane_id();
    int rb, hp; tile128x64(cx.bid, rb, hp);
    const char* kvg = ws + WS_KV + pw * 8192;
    const unsigned voff = lane * 16, kvd = cx.lds0 + pw * 8192;
#define KV_STAGE(st, so) do { _Pragma("unroll") for (int i_ = 0; i_ < 8; ++i_) dmac(kvg + (st) * 32768 + i_ * 1024, voff, kvd + (so) + i_ * 1024); } while (0)
    expect(ws, F_H1 + rb * 16, lane < 16 ? lane : -1, cx.tag);
    g64_produce<0, 81920, 6, 8>(cx, ws + WS_H1 + (size_t)rb * 8 * 32768, ws + WS_WQ + (size_t)hp * 131072, lane,
                    [&]() { BARRIER(); if (pw == 0) signal(ws, C_KV + (cx.bid & 15), lane); },
                    [&]() { }, []() {},
                    [&]() { KV_STAGE(0, 32768); });
    BARRIER();
    KV_STAGE(1, 65536);
    const int ao = (pw & 1) * 16384 + (pw >> 1) * 131072;
    const lds_ptr area = cx.lds + ao; const unsigned area0 = cx.lds0 + ao;
    const int k0 = (cx.bid >> 5) * 128 + pw * 32, cola = 1024 + (cx.bid & 31) * 64, colb = (cx.bid & 15) * 64 + ((cx.bid >> 4) & 1) * 3072;
    const int gw = cx.bid * 4 + pw, isgu = gw < 896, mat = gw >= 448, k8 = (gw - mat * 448) / 56, cg = gw - mat * 448 - k8 * 56;
    u32x4 cw;
    LD16(cw, c, (unsigned)(k0 + (lane >> 4) * 8) * 2u);
    pack_load((const char*)wmod + (size_t)k0 * 12288 + (2048 + cola) * 2, 12288, area0, lane);
    pack_load((const char*)wmod + (size_t)k0 * 12288 + (2048 + colb) * 2, 12288, area0 + 4096, lane);
    auto unit_load = [&](int j) {
        const unsigned dst = area0 + 8192 + (j & 1) * 4096;
        if (!isgu) { const int u = (gw - 896) * 4 + (j & 3); pack_load((const char*)wo + (size_t)(u >> 4) * 65536 + (u & 15) * 128, 2048, dst, lane); }
        else if (j < 4) pack_load((const char*)(mat ? wu : wg) + (size_t)(k8 * 4 + j) * 32 * 7168 + cg * 128, 7168, dst, lane);
        else { const int u = (j - 4) * 896 + gw; pack_load((const char*)wd + (size_t)(u >> 4) * 65536 + (u & 15) * 128, 2048, dst, lane); }
    };
    unsigned guo[4];
#pragma unroll
    for (int nf = 0; nf < 4; ++nf) {
        const int cj = cg * 8 + 2 * nf + ((lane >> 3) & 1), ct = cj / 14, r = cj - ct * 14, wn = r >= 7, c = r - wn * 7;
        const int fr = c < 6 ? mat * 3 + (c >> 1) : 6, hf = c < 6 ? (c & 1) : mat;
        guo[nf] = (unsigned)((ct * 32 * 14 + wn * 7 + fr) * 1024 + (lane >> 4) * 256 + (hf * 8 + (lane & 7)) * 16);
        PIN(guo[nf]);
    }
    auto unit_store = [&](int j) {
        const lds_ptr src = area + 8192 + (j & 1) * 4096;
        bf16x8 f[4];
#pragma unroll
        for (int nf = 0; nf < 4; ++nf) f[nf] = pack_frag(src, nf, lane);
        if (!isgu || j >= 4) {
            const int u = isgu ? (j - 4) * 896 + gw : (gw - 896) * 4 + (j & 3);
            const char* d = isgu ? ws + WS_WD + (size_t)((((u & 15) >> 1) * 112 + (u >> 4)) * 2 + (u & 1)) * 4096 : (j < 4 ? ws + WS_WO + (size_t)((u & 15) * 32 + (u >> 4)) * 4096 : ws + WS_UT + 458752 + (gw & 15) * 4096);
#pragma unroll
            for (int nf = 0; nf < 4; ++nf) st16(d, voff + nf * 1024, f[nf]);
        } else {
#pragma unroll
            for (int nf = 0; nf < 4; ++nf) st16(ws + WS_WGU + (size_t)(k8 * 4 + j) * 14336, guo[nf], f[nf]);
        }
    };
    unit_load(0);
    WAITV_BAR(13);
    KV_STAGE(2, 98304);
    WAIT_V(8);
    { const bf16x8 sc = silu_frag(cw); mod_unit(area, sc, area, lane); mod_unit(area + 4096, sc, area + 4096, lane); }
    unit_store(0); unit_load(1);
    WAITVL_BAR(8);
    KV_STAGE(3, 32768);
    if (pw < 2) {
        const LAS float* r = (const LAS float*)(cx.lds + pw * 4096) + lane;
        st4(ws + WS_MP2, (unsigned)((cx.bid >> 5) * 4096 + (pw ? colb : cola) + lane) * 4u, (r[0] + r[4096]) + (r[32768] + r[36864]));
    }
    WAIT_V(8); unit_store(1); unit_load(2);
    WAITVL_BAR(8);
    if (pw == 0) signal(ws, C_SIDE + (cx.bid & 15), lane);
    KV_STAGE(4, 65536); WAIT_V(8); unit_store(2); unit_load(3);
    WAITV_BAR(8);
    await<8>(ws, C_SIDE, 16, 16, lane);
    KV_STAGE(5, 98304);
    f32x4 vp[8];
    {
        const unsigned vo = (unsigned)((lane < 32 ? 2048 : 1024) + (k8 & 7) * 128 + (lane & 31) * 4) * 4u;
#pragma unroll
        for (int i = 0; i < 8; ++i) LD16C(vp[i], ws + WS_MP2, vo + i * 16384);
    }
    unit_store(3); unit_load(4);
    WAITV_BAR(16);
    KV_STAGE(6, 32768);
    WAIT_V(8);
    bf16x8 fr[4][4], vf[4];
#pragma unroll
    for (int j = 0; j < 4; ++j)
#pragma unroll
        for (int nf = 0; nf < 4; ++nf) LD16C(fr[j][nf], ws + WS_WGU + (size_t)((k8 & 7) * 4 + j) * 14336, guo[nf]);
    {
        f32x4 v = rbf4(sum8(vp));
        if (lane < 32) v = rbf4(v + 1.f);
        *(LAS u32x2*)(area + lane * 8) = (u32x2){cvtpk(v[0], v[1]), cvtpk(v[2], v[3])};
#pragma unroll
        for (int j = 0; j < 4; ++j) { vf[j] = ldsr128(area + (lane & 1) * 256 + j * 64 + (lane >> 4) * 16); PIN(vf[j]); }
    }
    unit_store(4); unit_load(5);
    WAITV_BAR(24);
    if (pw == 0) signal(ws, C_EARLY + (cx.bid & 15), lane);
    KV_STAGE(7, 65536);
    WAIT_V(8);
    {
        f32x4 ut[4] = {ZERO4, ZERO4, ZERO4, ZERO4};
#pragma unroll
        for (int j = 0; j < 4; ++j)
#pragma unroll
            for (int nf = 0; nf < 4; ++nf) { PIN(fr[j][nf]); ut[nf] = MFMA16(fr[j][nf], vf[j], ut[nf]); }
        unit_store(5);
        asm volatile("s_nop 7" : "+v"(ut[0]), "+v"(ut[1]), "+v"(ut[2]), "+v"(ut[3]));
        if (isgu && (lane & 15) < 2) {
#pragma unroll
            for (int nf = 0; nf < 4; ++nf) st16(ws + WS_UT, (unsigned)((k8 * 4 + mat * 2 + (lane & 15)) * F + cg * 64 + nf * 16 + (lane >> 4) * 4) * 4u, ut[nf]);
        }
    }
    {
        const u32x4 e = {~0u, ~0u, ~0u, ~0u};
#pragma unroll
        for (int i = 0; i < 4; ++i) st16(ws + WS_XCH + (size_t)gw * 4096, voff + i * 1024, e);
    }
    WAITVL_BAR(8);
#undef KV_STAGE
    __builtin_amdgcn_s_setprio(3);
    await<16>(ws, C_EARLY, 16, 16, lane);
    next_pre();
    BARRIER();
    if (pw == 0) { raise(ws, F_O + rb * 16 + hp, cx.tag, lane); signal(ws, C_SIDE + (cx.bid & 15), lane); }
}
__device__ __forceinline__ void attn_consume(const Ctx& cx) {
    char* ws = cx.ws; const lds_ptr lds = cx.lds; const int wid = cx.w, lane = lane_id();
    f32x16 qa; f32x2 kn2, vm2; float knm, vmm;
    unsigned cv;
    int rb, hp; tile128x64(cx.bid, rb, hp);
    g64_consume<0, 81920>(qa, cx, lane, []() {}, [&](int t) {
            {
                if (t == 4) asm volatile("s_nop 4\n\tglobal_load_dword %0, %1, %2 sc1" : "=v"(cv) : "v"(wid == 0 ? (lane & 15) * 256 : 0), "s"(ws + WS_CNT) : "memory");
                if (t == 8) { asm volatile("s_waitcnt vmcnt(0)" : "+v"(cv) :: "memory"); if (wid == 0 && !__all(cv >= 16u)) await<6>(ws, C_KV, 16, 16, lane); }
            }
            if (t == 10) asm volatile("s_nop 4\n\tglobal_load_dwordx2 %0, %2, %3 sc1\n\tglobal_load_dwordx2 %1, %2, %3 offset:512 sc1" : "=&v"(kn2), "=&v"(vm2) : "v"(lane * 8), "s"(ws + WS_KN) : "memory"); },
        [&]() { asm volatile("s_waitcnt vmcnt(0)" : "+v"(kn2), "+v"(vm2) :: "memory"); knm = wmax(fmaxf(kn2[0], kn2[1])); vmm = wmax(fmaxf(vm2[0], vm2[1])); PIN(knm); PIN(vmm); SBAR(); },
        [&]() { });
    BAR_L();
    const lds_ptr pl = lds + lane * 16;
    bf16x8 k0 = ldsr128(pl + 32768), k1 = ldsr128(pl + 32768 + 1024), v0 = ldsr128(pl + 32768 + 2048);
    constexpr float QS = 0.17677669529663687f * 1.4426950408889634f;
    u32x4 qw0, qw1;
#pragma unroll
    for (int j = 0; j < 4; ++j) { qw0[j] = cvtpk(qa[2 * j] * QS, qa[2 * j + 1] * QS); qw1[j] = cvtpk(qa[8 + 2 * j] * QS, qa[8 + 2 * j + 1] * QS); }
    const bf16x8 q0 = __builtin_bit_cast(bf16x8, qw0), q1 = __builtin_bit_cast(bf16x8, qw1);
    f32x16 s0 = MFMA32(k0, q0, ((f32x16){})); s0 = MFMA32(k1, q1, s0);
    PIN(s0);
    float qn = 0.f;
#pragma unroll
    for (int j = 0; j < 4; ++j) { qn += bflo(qw0[j]) * bflo(qw0[j]) + bfhi(qw0[j]) * bfhi(qw0[j]) + bflo(qw1[j]) * bflo(qw1[j]) + bfhi(qw1[j]) * bfhi(qw1[j]); }
    qn = half_sum(qn);
    const bool fast = __all((qn * knm <= 88.f * 88.f) && (vmm <= 16777216.f) && (vmm >= 9.5367431640625e-7f));
    f32x16 o = {};
    float linv;
#define EX(x) __builtin_amdgcn_exp2f(x)
#define BF8(x) __builtin_bit_cast(bf16x8, x)
    if (fast) {
        const short ov = ((lane & 15) == ((lane >> 4) & 1)) ? (short)0x3f80 : (short)0;
        const bf16x8 sel = {ov, ov, ov, ov, ov, ov, ov, ov};
        f32x4 ls = ZERO4;
        const f32x16 zero = {};
        f32x16 sA, sB;
        bf16x8 v1 = v0;
        u32x4 p0, p1 = {0u, 0u, 0u, 0u};
        sA = s0;
        k0 = ldsr128(pl + 32768 + 4096); k1 = ldsr128(pl + 32768 + 4096 + 1024);
#define ATT_TILE(SC, SN, PK, PV0, PV1) do { \
            SN = MFMA32(k0, q0, zero); PIN(SN); SBAR(); k0 = ldsr128(PK); \
            SC[0] = EX(SC[0]); SC[1] = EX(SC[1]); SC[2] = EX(SC[2]); SC[3] = EX(SC[3]); PIN(SC); SBAR(); \
            o = MFMA32(v1, BF8(p1), o); PIN(o); SBAR(); v1 = ldsr128(PV1); \
            SC[4] = EX(SC[4]); SC[5] = EX(SC[5]); SC[6] = EX(SC[6]); PIN(SC); SBAR(); \
            ls = MFMA16(sel, BF8(p1), ls); PIN(ls); SBAR(); SC[7] = EX(SC[7]); p0[0] = cvtpk(SC[0], SC[1]); p0[1] = cvtpk(SC[2], SC[3]); PIN(SC); PIN(p0); SBAR(); \
            SN = MFMA32(k1, q1, SN); PIN(SN); SBAR(); k1 = ldsr128((PK) + 1024); \
            p0[2] = cvtpk(SC[4], SC[5]); p0[3] = cvtpk(SC[6], SC[7]); PIN(p0); \
            SC[8] = EX(SC[8]); SC[9] = EX(SC[9]); SC[10] = EX(SC[10]); PIN(SC); SBAR(); \
            o = MFMA32(v0, BF8(p0), o); PIN(o); SBAR(); v0 = ldsr128(PV0); \
            SC[11] = EX(SC[11]); SC[12] = EX(SC[12]); SC[13] = EX(SC[13]); PIN(SC); SBAR(); \
            ls = MFMA16(sel, BF8(p0), ls); PIN(ls); SBAR(); \
            SC[14] = EX(SC[14]); SC[15] = EX(SC[15]); \
            p1[0] = cvtpk(SC[8], SC[9]); p1[1] = cvtpk(SC[10], SC[11]); p1[2] = cvtpk(SC[12], SC[13]); p1[3] = cvtpk(SC[14], SC[15]); PIN(p1); SBAR(); } while (0)
        int so = 32768;
#pragma unroll 1
        for (int st = 0; st < 8; ++st) {
            const int sn = so == 98304 ? 32768 : so + 32768;
            const lds_ptr pc = pl + so, pn = pl + sn;
            __builtin_amdgcn_s_setprio(1);
            ATT_TILE(sA, sB, pc + 2 * 4096, pc + 1 * 4096 + 2048, pc + 0 * 4096 + 3072);
            ATT_TILE(sB, sA, pc + 3 * 4096, pc + 2 * 4096 + 2048, pc + 1 * 4096 + 3072);
            ATT_TILE(sA, sB, pc + 4 * 4096, pc + 3 * 4096 + 2048, pc + 2 * 4096 + 3072);
            ATT_TILE(sB, sA, pc + 5 * 4096, pc + 4 * 4096 + 2048, pc + 3 * 4096 + 3072);
            __builtin_amdgcn_s_setprio(0);
            ATT_TILE(sA, sB, pc + 6 * 4096, pc + 5 * 4096 + 2048, pc + 4 * 4096 + 3072);
            ATT_TILE(sB, sA, pc + 7 * 4096, pc + 6 * 4096 + 2048, pc + 5 * 4096 + 3072);
            if (st < 7) BAR_L();
            if (st < 7) __builtin_amdgcn_s_setprio(2);
            SBAR();
            ATT_TILE(sA, sB, pn, pc + 7 * 4096 + 2048, pc + 6 * 4096 + 3072);
            ATT_TILE(sB, sA, pn + 4096, pn + 2048, pc + 7 * 4096 + 3072);
            so = sn;
        }
        o = MFMA32(v1, BF8(p1), o);
        ls = MFMA16(sel, BF8(p1), ls);
#undef ATT_TILE
        const float la = __shfl(ls[0], lane & 15), lb = __shfl(ls[1], lane & 15);
        linv = 1.f / ((lane & 16) ? lb : la);
    } else {
        float m = -INFINITY, l = 0.f;
        const f32x16 zero = {};
        int so = 32768;
#pragma unroll 1
        for (int st = 0; st < 8; ++st) {
            const lds_ptr pb = pl + so;
#pragma unroll 1
            for (int t = 0; t < 8; ++t) {
                if (t == 6 && st < 7) BAR_L();
                k0 = ldsr128(pb + t * 4096); k1 = ldsr128(pb + t * 4096 + 1024); v0 = ldsr128(pb + t * 4096 + 2048); const bf16x8 v1 = ldsr128(pb + t * 4096 + 3072);
                f32x16 s = MFMA32(k0, q0, zero);
                s = MFMA32(k1, q1, s);
                float tm = s[0];
#pragma unroll
                for (int j = 1; j < 16; ++j) tm = fmaxf(tm, s[j]);
                tm = fmaxf(tm, __shfl_xor(tm, 32));
                const float mn = fmaxf(m, tm), al = EX(m - mn);
                m = mn; l *= al;
#pragma unroll
                for (int j = 0; j < 16; ++j) o[j] *= al;
                u32x4 p0, p1;
#pragma unroll
                for (int j = 0; j < 4; ++j) {
                    p0[j] = cvtpk(EX(s[2 * j] - mn), EX(s[2 * j + 1] - mn));
                    p1[j] = cvtpk(EX(s[8 + 2 * j] - mn), EX(s[8 + 2 * j + 1] - mn));
                    l += (bflo(p0[j]) + bfhi(p0[j])) + (bflo(p1[j]) + bfhi(p1[j]));
                }
                o = MFMA32(v0, BF8(p0), o);
                o = MFMA32(v1, BF8(p1), o);
            }
            so = so == 98304 ? 32768 : so + 32768;
        }
        linv = 1.f / half_sum(l);
    }
#undef EX
#undef BF8
    {
        const char* ob = ws + WS_O + ((size_t)(rb * 8 + (wid & 3) * 2) * 128 + (hp * 2 + (wid >> 2)) * 4) * 256;
        const unsigned vo = (unsigned)(((lane >> 4) & 1) * 32768 + (lane >> 5) * 256 + (lane & 15) * 16);
#pragma unroll
        for (int gp = 0; gp < 2; ++gp) {
            const auto r0 = __builtin_amdgcn_permlane32_swap(cvtpk(o[8 * gp] * linv, o[8 * gp + 1] * linv), cvtpk(o[8 * gp + 4] * linv, o[8 * gp + 5] * linv), false, false);
            const auto r1 = __builtin_amdgcn_permlane32_swap(cvtpk(o[8 * gp + 2] * linv, o[8 * gp + 3] * linv), cvtpk(o[8 * gp + 6] * linv, o[8 * gp + 7] * linv), false, false);
            st16(ob, vo + gp * 512, ((u32x4){r0[0], r1[0], r0[1], r1[1]}));
        }
    }
    WAITVL_BAR(0);
}

constexpr int O_STAT = 159744;
struct OPre { f32x4 p[8]; };
__device__ __forceinline__ void oproj_pre(const Ctx& cx, OPre& o) {
    const int lane = lane_id(); int rb, ct; tile128x64(cx.bid, rb, ct);
    const unsigned vo = (unsigned)(((lane >> 4) & 1) * 2048 + ct * 64 + (lane & 15) * 4) * 4u;
#pragma unroll
    for (int i = 0; i < 8; ++i) LD16C(o.p[i], cx.ws + WS_MP2, vo + i * 16384);
    g64_pre<98304, 10>(cx, cx.ws + WS_WO + (size_t)ct * 131072, lane);
}
template <class Next>
__device__ __forceinline__ void oproj_produce(const Ctx& cx, OPre& o, const u16* __restrict__ x, Next next_pre) {
    char* ws = cx.ws; const int pw = cx.w, lane = lane_id();
    int rb, ct; tile128x64(cx.bid, rb, ct);
    if (pw == 0) expect(ws, F_O + rb * 16, lane < 5 ? lane : -1, cx.tag);
    INPUT_BARRIER();
    f32x4 tv;
    g64_produce<98304, 16384, 10, 4>(cx, ws + WS_O + (size_t)rb * 8 * 32768, ws + WS_WO + (size_t)ct * 131072, lane, []() {},
        [&]() { tv = rbf4(sum8(o.p)); if (lane & 16) tv = rbf4(tv + 1.f); PIN(tv); },
        [&]() { if (pw == 0) expect(ws, F_O + rb * 16, lane < 16 ? lane : -1, cx.tag); },
        [&]() {
            const unsigned voffX = (unsigned)((rb * 128 + pw * 32 + (lane >> 3)) * 2048 + ct * 128 + (((lane & 7) ^ ((lane >> 3) & 7)) << 4));
#pragma unroll

            for (int i = 0; i < 4; ++i) dmant((const char*)x + i * 8 * 2048, voffX,
 cx.lds0 + 131072 + pw * 4096 + i * 1024);
            if (pw == 0 && lane < 32) *(LAS f32x4*)(cx.lds + 147456 + lane * 16) = tv; });
    BARRIER();
    if (pw == 0 && lane == 0) *(LAS unsigned*)cx.lds = 0u;
    next_pre();
    BAR_L();
}
__device__ __forceinline__ void oproj_consume(const Ctx& cx) {
    char* ws = cx.ws; const lds_ptr lds = cx.lds; const int wid = cx.w, lane = lane_id();
    f32x16 acc;
    int rb, ct; tile128x64(cx.bid, rb, ct);
    g64_consume<98304, 16384>(acc, cx, lane, [&]() { INPUT_BARRIER(); }, [](int) {}, []() {}, [&]() { });
    BAR_L();
    const int tb = wid & 3, nh = wid >> 2, h = lane >> 5, rl = tb * 32 + (lane & 31);
    const unsigned so = (unsigned)(((lane >> 4) & 1) * 32768 + h * 256 + (lane & 15) * 16);
    u32x4 x1v[2]; float v[16];
#pragma unroll
    for (int gp = 0; gp < 2; ++gp) {
        unsigned u[2][2], y[2][2];
#pragma unroll
        for (int k = 0; k < 2; ++k) {
            const int G = 2 * gp + k;
            const f32x4 ga = *(const LAS f32x4*)(lds + 147456 + (nh * 8 + G * 2 + h) * 16), cb = *(const LAS f32x4*)(lds + 147456 + 256 + (nh * 8 + G * 2 + h) * 16);
            const u32x2 xv = *(const LAS u32x2*)(lds + 131072 + rl * 128 + (((nh * 4 + G) ^ (rl & 7)) << 4) + 8 * h);
            u[k][0] = cvtpk(bflo(xv[0]) + ga[0] * acc[4 * G], bfhi(xv[0]) + ga[1] * acc[4 * G + 1]);
            u[k][1] = cvtpk(bflo(xv[1]) + ga[2] * acc[4 * G + 2], bfhi(xv[1]) + ga[3] * acc[4 * G + 3]);
            v[4 * G] = bflo(u[k][0]); v[4 * G + 1] = bfhi(u[k][0]); v[4 * G + 2] = bflo(u[k][1]); v[4 * G + 3] = bfhi(u[k][1]);
            y[k][0] = cvtpk(v[4 * G] * cb[0], v[4 * G + 1] * cb[1]); y[k][1] = cvtpk(v[4 * G + 2] * cb[2], v[4 * G + 3] * cb[3]);
        }
        st16(ws + WS_Y + ((size_t)(rb * 8 + tb * 2) * 128 + ct * 8 + nh * 4) * 256, so + gp * 512, swap32(y[0][0], y[0][1], y[1][0], y[1][1]));
        x1v[gp] = swap32(u[0][0], u[0][1], u[1][0], u[1][1]);
    }
    BAR_L();
    {
        float sm = 0.f;
#pragma unroll
        for (int j = 0; j < 16; ++j) sm += v[j];
        const float mean = half_sum(sm) * (1.f / 32.f);
        float q = 0.f;
#pragma unroll
        for (int j = 0; j < 16; ++j) q += (v[j] - mean) * (v[j] - mean);
        q = half_sum(q);
        if (h == 0) *(LAS f32x2*)(lds + O_STAT + (rl * 2 + nh) * 8) = (f32x2){mean, q};
    }
    drain_raise(ws, lds, F_Y + rb * 16 + ct, cx.tag, lane);
#pragma unroll
    for (int gp = 0; gp < 2; ++gp) st16(ws + WS_X1 + ((size_t)(rb * 8 + tb * 2) * 128 + ct * 8 + nh * 4) * 256, so + gp * 512, x1v[gp]);
}

constexpr int GSL = 30720, G_ROW = 5 * GSL + 1024, G_COL = G_ROW + 2048;
__device__ __forceinline__ void tile_glu(int bid, int& rp, int& ct) { const int xcd = bid & 7, idx = bid >> 3; rp = (xcd & 1) * 4 + (idx & 3); ct = (xcd >> 1) * 8 + (idx >> 2); }
#define GLU_W(t, so) do { const char* w_ = Wb + (size_t)(t) * 14336; const unsigned d2_ = pw < 3 ? dW + (so) + 2048 : cx.lds0 + 5 * GSL; \
        dmac(w_, voff, dW + (so)); dmac(w_ + 1024, voff, dW + (so) + 1024); dmac(w_, voff2, d2_); dmac(w_ + 1024, voff2, pw < 3 ? d2_ + 1024 : d2_); } while (0)
__device__ __forceinline__ void glu_pre(const Ctx& cx) {
    const int pw = cx.w, lane = lane_id(); int rp, ct; tile_glu(cx.bid, rp, ct);
    const char* Wb = cx.ws + WS_WGU + (size_t)ct * 32 * 14336 + pw * 4096;
    const unsigned voff = lane * 16, dW = cx.lds0 + 16384 + pw * 4096;
#pragma unroll
    for (int t = 0; t < 4; ++t) {
        dmac(Wb + t * 14336, voff, dW + t * GSL); dmac(Wb + t * 14336 + 1024, voff, dW + t * GSL + 1024);
        if (pw < 3) { dmac(Wb + t * 14336 + 2048, voff, dW + t * GSL + 2048); dmac(Wb + t * 14336 + 3072, voff, dW + t * GSL + 3072); }
    }
}
template <class Next>
__device__ __forceinline__ void glu_produce(const Ctx& cx, Next next_pre) {
    char* ws = cx.ws; const int pw = cx.w, lane = lane_id(); int rp, ct; tile_glu(cx.bid, rp, ct);
    const char* Ab = ws + WS_Y + (size_t)(rp * 16 + 4 * pw) * 32768;
    const char* Wb = ws + WS_WGU + (size_t)ct * 32 * 14336 + pw * 4096;
    const unsigned voff = lane * 16, voff2 = pw < 3 ? lane * 16 + 2048 : 0u, dA = cx.lds0 + pw * 4096, dW = cx.lds0 + 16384 + pw * 4096;
#define GLU_A(t, so) do { const char* a_ = Ab + (size_t)(t) * 1024; dmac(a_, voff, dA + (so)); dmac(a_ + 32768, voff, dA + (so) + 1024); dmac(a_ + 65536, voff, dA + (so) + 2048); dmac(a_ + 98304, voff, dA + (so) + 3072); } while (0)
    if (pw == 0) expect(ws, F_Y + rp * 32, lane < 32 && (lane & 15) < 2 ? lane : -1, cx.tag);
    INPUT_BARRIER();
    GLU_A(0, 0); GLU_A(1, GSL);
    WAITV_BAR(4);
    GLU_A(2, 2 * GSL); GLU_A(3, 3 * GSL);
    WAITV_BAR(8);
    if (pw == 0) expect(ws, F_Y + rp * 32, lane < 32 ? lane : -1, cx.tag);
    WAITV_BAR(4); GLU_W(4, 4 * GSL); GLU_A(4, 4 * GSL); GLU_W(5, 0); GLU_A(5, 0);
    int sf = GSL;
#define GLU_NEXT(t, VM) do { WAITV_BAR(VM); GLU_W((t) + 4, sf); GLU_A((t) + 4, sf); sf = sf == 4 * GSL ? 0 : sf + GSL; } while (0)
    GLU_NEXT(2, 16); GLU_NEXT(3, 16);
    if (pw == 0) { int rbo, cto; tile128x64(cx.bid, rbo, cto); signal(ws, C_ST + (rbo >> 1), lane); }
    GLU_NEXT(4, 16); GLU_NEXT(5, 16);
    {
        unsigned cv;
        int rbd, ctd, khd; tile_down(cx.bid, rbd, ctd, khd);
        asm volatile("s_nop 4\n\tglobal_load_dword %0, %1, %2 sc1" : "=v"(cv) : "v"((lane < 16 ? C_SIDE + lane : C_ST + (lane == 16 ? rp : rbd >> 1)) * 256), "s"(ws + WS_CNT) : "memory");
        GLU_NEXT(6, 17); GLU_NEXT(7, 17); GLU_NEXT(8, 17); GLU_NEXT(9, 16);
        PIN(cv);
        if (!__all(cv >= 32u)) { await<4>(ws, C_SIDE, 16, 32, lane); await<4>(ws, C_ST + rp, 1, 32, lane); await<4>(ws, C_ST + (rbd >> 1), 1, 32, lane); }
    }
    {
        f32x2 sp[16]; f32x4 cp[8];
        const char* sb_ = ws + WS_ST2 + (size_t)(rp * 256 + pw * 64) * 8;
#pragma unroll
        for (int i = 0; i < 8; ++i) LD8C(sp[i], sb_, (unsigned)(lane * 8 + i * 16384));
        GLU_NEXT(10, 24);
#pragma unroll
        for (int i = 8; i < 16; ++i) LD8C(sp[i], sb_, (unsigned)(lane * 8 + i * 16384));
        GLU_NEXT(11, 32);
        int i4 = pw * 64 + lane; i4 = i4 < 112 ? i4 : 0;
        const unsigned vo = (unsigned)((i4 / 28) * F + ct * 112 + (i4 % 28) * 4) * 4u;
#pragma unroll
        for (int i = 0; i < 8; ++i) LD16C(cp[i], ws + WS_UT, vo + i * (4 * F * 4));
        GLU_NEXT(12, 40); GLU_NEXT(13, 32); GLU_NEXT(14, 24); GLU_NEXT(15, 16);
        __builtin_amdgcn_s_setprio(0);
        float mean = 0.f, q = 0.f;
#pragma unroll
        for (int i = 0; i < 16; ++i) { PIN(sp[i]); mean += sp[i][0]; q += sp[i][1]; }
        mean *= 1.f / 16.f; PIN(mean); PIN(q);
        GLU_NEXT(16, 16);
        float dq = 0.f;
#pragma unroll
        for (int i = 0; i < 16; ++i) dq += (sp[i][0] - mean) * (sp[i][0] - mean);
        PIN(dq);
        GLU_NEXT(17, 16);
        const float rstd = 1.f / sqrtf((q + 64.f * dq) * (1.f / 1024.f) + 1e-5f);
        *(LAS f32x2*)(cx.lds + G_ROW + (pw * 64 + lane) * 8) = (f32x2){rstd, -mean * rstd};
        GLU_NEXT(18, 16);
        const f32x4 c = sum8(cp);
        if (pw * 64 + lane < 112) *(LAS f32x4*)(cx.lds + G_COL + (pw * 64 + lane) * 16) = c;
        __builtin_amdgcn_s_setprio(3);
        GLU_NEXT(19, 16);
    }
#pragma unroll 1
    for (int t = 20; t < 28; ++t) GLU_NEXT(t, 16);
    WAITV_BAR(16); WAITV_BAR(8); WAITVL_BAR(0);
#undef GLU_NEXT
#undef GLU_A
    if (pw == 0 && lane == 0) *(LAS unsigned*)(cx.lds + G_COL + 1920) = 0u;
    BARRIER();
    next_pre();
    BAR_L();
}
#undef GLU_W
__device__ __forceinline__ void glu_consume(const Ctx& cx) {
    char* ws = cx.ws; const lds_ptr lds = cx.lds; const int wm = cx.w & 3, wn = cx.w >> 2;
    f32x4 acc[4][7];
    {
        const int lane = lane_id();
        const lds_ptr pa = lds + wm * 4096 + lane * 16, pw = lds + 16384 + wn * 7168 + lane * 16;
        bf16x8 af[4], wf[4];
#pragma unroll
        for (int n = 0; n < 7; ++n)
#pragma unroll
            for (int m = 0; m < 4; ++m) acc[m][n] = ZERO4;
        INPUT_BARRIER();
        if (cx.w == 1 || cx.w == 2) {
            int rbo, cto; tile128x64(cx.bid, rbo, cto);
            const int rl = (cx.w - 1) * 64 + lane;
            const f32x4 s = *(const LAS f32x4*)(lds + O_STAT + rl * 16);
            const float dm = s[0] - s[2];
            st8(ws + WS_ST2 + (size_t)(cto * 2048 + rbo * 128) * 8, (unsigned)rl * 8u, ((f32x2){0.5f * (s[0] + s[2]), s[1] + s[3] + 16.f * dm * dm}));
        }
        BARRIER();
#pragma unroll
        for (int m = 0; m < 4; ++m) { af[m] = ldsr128(pa + m * 1024); wf[m] = ldsr128(pw + m * 1024); }
        int sc = 0, sn = GSL;
#define LAD(p) __builtin_amdgcn_s_setprio(p)
#define GW(U, n) wf[(7 * (U) + (n)) & 3]
#define GLU_STEP(U, DO_NEXT) do { \
            if ((U) == 3) WAIT_V(0);   if (DO_NEXT) BARRIER(); LAD(1); SBAR(); \
            const lds_ptr pwc = pw + sc, pwn = pw + sn, pan = pa + sn; \
            _Pragma("unroll") for (int m = 0; m < 4; ++m) { MFMA16_ACC(acc[m][0], GW(U, 0), af[m]); MFMA16_ACC(acc[m][1], GW(U, 1), af[m]); } \
            SBAR(); GW(U, 4) = ldsr128(pwc + 4096); GW(U, 5) = ldsr128(pwc + 5120); SBAR(); \
            _Pragma("unroll") for (int m = 0; m < 4; ++m) MFMA16_ACC(acc[m][2], GW(U, 2), af[m]); \
            SBAR(); GW(U, 6) = ldsr128(pwc + 6144); SBAR(); \
            _Pragma("unroll") for (int m = 0; m < 4; ++m) MFMA16_ACC(acc[m][3], GW(U, 3), af[m]); \
            SBAR(); if (DO_NEXT) GW(U, 7) = ldsr128(pwn); SBAR(); \
            _Pragma("unroll") for (int m = 0; m < 4; ++m) MFMA16_ACC(acc[m][4], GW(U, 4), af[m]); \
            SBAR(); if (DO_NEXT) GW(U, 8) = ldsr128(pwn + 1024); SBAR(); \
            _Pragma("unroll") for (int m = 0; m < 4; ++m) { MFMA16_ACC(acc[m][5], GW(U, 5), af[m]); MFMA16_ACC(acc[m][6], GW(U, 6), af[m]); \
                SBAR(); if (DO_NEXT) af[m] = ldsr128(pan + m * 1024); if (m == 1) LAD(0); SBAR(); } \
            if (DO_NEXT) { GW(U, 9) = ldsr128(pwn + 2048); GW(U, 10) = ldsr128(pwn + 3072); } SBAR(); \
            sc = sn; sn = sn == 4 * GSL ? 0 : sn + GSL; } while (0)
#pragma unroll 1
        for (int t = 0; t < 28; t += 4) { GLU_STEP(0, true); GLU_STEP(1, true); GLU_STEP(2, true); GLU_STEP(3, true); }
        GLU_STEP(0, true); GLU_STEP(1, true); GLU_STEP(2, true); GLU_STEP(3, false);
#undef GLU_STEP
#undef GW
    }
    asm volatile("s_barrier\n\ts_nop 7\n\ts_nop 7" : "+v"(acc[0][6]), "+v"(acc[1][6]), "+v"(acc[2][6]), "+v"(acc[3][6]), "+v"(acc[0][5]), "+v"(acc[1][5]), "+v"(acc[2][5]), "+v"(acc[3][5]) :: "memory");
    SBAR();
    const int lane = lane_id(), g = lane >> 4, gc = ((g & 1) << 1) | (g >> 1);
    int rp, ct; tile_glu(cx.bid, rp, ct);
    const char* hb = ws + WS_HH + ((size_t)(rp * 16 + wm * 4) * (F / 8) + ct * 14 + wn * 7) * 256;
    const unsigned vo = (lane & 15) * 16;
    constexpr unsigned RB = (F / 8) * 256;
    f32x2 rw[4];
#pragma unroll
    for (int m = 0; m < 4; ++m) rw[m] = *(const LAS f32x2*)(lds + G_ROW + (wm * 64 + m * 16 + (lane & 15)) * 8);
#define GLU_OUT(m, f, ug, tg, uu, tu, d0, d1) do { const f32x4 gt = acc[m][f] * rw[m][0] + (ug * rw[m][1] + tg), up = acc[m][3 + (f)] * rw[m][0] + (uu * rw[m][1] + tu); \
        d0 = cvtpk(fmaxf(gt[0], 0.f) * up[0], fmaxf(gt[1], 0.f) * up[1]); d1 = cvtpk(fmaxf(gt[2], 0.f) * up[2], fmaxf(gt[3], 0.f) * up[3]); } while (0)
    {
        const lds_ptr pc = lds + G_COL + (wn * 14 + g) * 16;
        f32x4 ct_[3][4];
#pragma unroll
        for (int f = 0; f < 2; ++f)
#pragma unroll
            for (int k = 0; k < 4; ++k) ct_[f][k] = *(const LAS f32x4*)(pc + f * 64 + k * 448);
#pragma unroll
        for (int m = 0; m < 4; ++m) {
            unsigned a0, a1, b0, b1;
            GLU_OUT(m, 0, ct_[0][0], ct_[0][1], ct_[0][2], ct_[0][3], a0, a1); GLU_OUT(m, 1, ct_[1][0], ct_[1][1], ct_[1][2], ct_[1][3], b0, b1);
            st16(hb, vo + m * RB + gc * 256, swap16(a0, a1, b0, b1));
            if (m == 1) { _Pragma("unroll") for (int k = 0; k < 4; ++k) ct_[2][k] = *(const LAS f32x4*)(pc + 128 + k * 448); }
            SBAR();
        }
#pragma unroll
        for (int mp = 0; mp < 2; ++mp) {
            unsigned a0, a1, b0, b1;
            GLU_OUT(2 * mp, 2, ct_[2][0], ct_[2][1], ct_[2][2], ct_[2][3], a0, a1); GLU_OUT(2 * mp + 1, 2, ct_[2][0], ct_[2][1], ct_[2][2], ct_[2][3], b0, b1);
            st16(hb, vo + (2 * mp + (g & 1)) * RB + (4 + (g >> 1)) * 256, swap16(a0, a1, b0, b1));
            SBAR();
        }
    }
#undef GLU_OUT
    {
        const lds_ptr pc = lds + G_COL + (g >> 1) * 896 + (wn * 14 + 12 + (g & 1)) * 16;
        const f32x4 uc = *(const LAS f32x4*)pc, tc = *(const LAS f32x4*)(pc + 448);
        unsigned ux[2][2];
#pragma unroll
        for (int mp = 0; mp < 2; ++mp) {
            const f32x4 a = acc[2 * mp][6] * rw[2 * mp][0] + (uc * rw[2 * mp][1] + tc), b = acc[2 * mp + 1][6] * rw[2 * mp + 1][0] + (uc * rw[2 * mp + 1][1] + tc);
            float hv[4];
#pragma unroll
            for (int r = 0; r < 4; ++r) {
                const auto x = __builtin_amdgcn_permlane32_swap(__float_as_uint(a[r]), __float_as_uint(b[r]), false, false);
                hv[r] = fmaxf(__uint_as_float(x[0]), 0.f) * __uint_as_float(x[1]);
            }
            ux[mp][0] = cvtpk(hv[0], hv[1]); ux[mp][1] = cvtpk(hv[2], hv[3]);
        }
        st16(hb, vo + gc * RB + 6 * 256, swap16(ux[0][0], ux[0][1], ux[1][0], ux[1][1]));
    }
    BARRIER();
    drain_raise(ws, lds + G_COL + 1920, F_HH + rp * 32 + ct, cx.tag, lane);
}

#define DN_W(t, so) do { const char* w_ = Wb + (size_t)(t) * 16384; dmac(w_, voff, dW + (so)); dmac(w_ + 1024, voff, dW + (so) + 1024); dmac(w_ + 2048, voff, dW + (so) + 2048); dmac(w_ + 3072, voff, dW + (so) + 3072); } while (0)
__device__ __forceinline__ void down_pre(const Ctx& cx, OPre& o) {
    const int pw = cx.w, lane = lane_id(); int rb, ct, kh; tile_down(cx.bid, rb, ct, kh);
    const unsigned vo = (unsigned)(3072 + ct * 128 + (lane & 31) * 4) * 4u;
#pragma unroll
    for (int i = 0; i < 8; ++i) LD16C(o.p[i], cx.ws + WS_MP2, vo + i * 16384);
    const char* Wb = cx.ws + WS_WD + (size_t)(ct * 112 + kh * 56) * 8192 + pw * 4096;
    const unsigned voff = lane * 16, dW = cx.lds0 + 16384 + pw * 4096;
#pragma unroll
    for (int t = 0; t < 4; ++t) DN_W(t, t * 32768);
}
__device__ __forceinline__ void down_produce(const Ctx& cx, OPre& o) {
    char* ws = cx.ws; const int pw = cx.w, lane = lane_id(); int rb, ct, kh; tile_down(cx.bid, rb, ct, kh);
    const char* Ab = ws + WS_HH + ((size_t)(rb * 8 + 2 * pw) * (F / 8) + kh * 224) * 256;
    const char* Wb = ws + WS_WD + (size_t)(ct * 112 + kh * 56) * 8192 + pw * 4096;
    const unsigned voff = lane * 16, dA = cx.lds0 + pw * 4096, dW = cx.lds0 + 16384 + pw * 4096;
#define DN_A(t, so) do { const char* a_ = Ab + (size_t)(t) * 2048; dmac(a_, voff, dA + (so)); dmac(a_ + 1024, voff, dA + (so) + 1024); \
        dmac(a_ + (F / 8) * 256, voff, dA + (so) + 2048); dmac(a_ + (F / 8) * 256 + 1024, voff, dA + (so) + 3072); } while (0)
    if (pw == 0) expect(ws, F_HH + (rb >> 1) * 32 + kh * 16, lane < 3 ? lane : -1, cx.tag);
    INPUT_BARRIER();
    DN_A(0, 0); DN_A(1, 32768);
    WAITV_BAR(4);
    DN_A(2, 65536); DN_A(3, 98304);
    WAITV_BAR(8);
    DN_W(4, 131072);
    if (pw == 0) expect(ws, F_HH + (rb >> 1) * 32 + kh * 16, lane < 16 ? lane : -1, cx.tag);
    f32x4 tv = rbf4(sum8(o.p)); PIN(tv);
    WAITV_BAR(8); DN_A(4, 131072); DN_W(5, 0); DN_A(5, 0);
    int sf = 32768;
#pragma unroll 1
    for (int t = 2; t < 24; ++t) {
        WAITV_BAR(16);
        DN_W(t + 4, sf); DN_A(t + 4, sf);
        sf = sf == 131072 ? 0 : sf + 32768;
    }
    WAITV_BAR(16);
    {
        const char* xb = ws + WS_X1 + ((size_t)(rb * 8 + (pw >> 1) * 4 + kh * 2 + (pw & 1)) * 128 + ct * 16) * 256;
#pragma unroll
        for (int i = 0; i < 4; ++i) dmac(xb + i * 1024, voff, cx.lds0 + 98304 + pw * 4096 + i * 1024);
        if (pw == 0 && lane < 32) *(LAS f32x4*)(cx.lds + 114688 + lane * 16) = tv;
    }
    WAITV_BAR(12); WAITV_BAR(4); WAITVL_BAR(0);
#undef DN_A
}
#undef DN_W
__device__ __forceinline__ void down_consume(const Ctx& cx, u16* __restrict__ out) {
    char* ws = cx.ws; const lds_ptr lds = cx.lds; const int wid = cx.w, lane = lane_id(), wm = wid >> 2, wn = wid & 3, g = lane >> 4;
    const lds_ptr pa = lds + wm * 8192 + lane * 16, pw = lds + 16384 + wn * 2048 + lane * 16;
    f32x4 acc[4][2];
#pragma unroll
    for (int m = 0; m < 4; ++m) { acc[m][0] = ZERO4; acc[m][1] = ZERO4; }
    bf16x8 aA[8], wA[4], aB[8], wB[4];
    INPUT_BARRIER();
    BARRIER(); SBAR();
#pragma unroll
    for (int ks = 0; ks < 2; ++ks) {
        wA[2 * ks] = ldsr128(pw + ks * 8192); wA[2 * ks + 1] = ldsr128(pw + ks * 8192 + 1024);
#pragma unroll
        for (int m = 0; m < 4; ++m) aA[4 * ks + m] = ldsr128(pa + m * 2048 + ks * 1024);
    }
    SBAR();
    int sn = 32768;
#define DN_STEP(AC, WC, AN, WN, DO_NEXT) do { \
        if (DO_NEXT) BARRIER(); LAD(1); SBAR(); \
        const lds_ptr pan = pa + sn, pwn = pw + sn; \
        _Pragma("unroll") for (int i = 0; i < 8; ++i) { const int ks = i >> 2, m = i & 3; \
            MFMA16_ACC(acc[m][0], WC[2 * ks], AC[i]); MFMA16_ACC(acc[m][1], WC[2 * ks + 1], AC[i]); SBAR(); \
            if (DO_NEXT) { if (m < 2) WN[2 * ks + m] = ldsr128(pwn + ks * 8192 + m * 1024); AN[i] = ldsr128(pan + m * 2048 + ks * 1024); } \
            if (i == 5) LAD(0); \
            SBAR(); } \
        sn = sn == 131072 ? 0 : sn + 32768; } while (0)
#pragma unroll 1
    for (int t = 0; t < 26; t += 2) {
        DN_STEP(aA, wA, aB, wB, true); DN_STEP(aB, wB, aA, wA, true);
    }
    DN_STEP(aA, wA, aB, wB, true); DN_STEP(aB, wB, aA, wA, false);
#undef DN_STEP
    asm volatile("s_nop 7\n\ts_nop 7" : "+v"(acc[0][0]), "+v"(acc[0][1]), "+v"(acc[1][0]), "+v"(acc[1][1]), "+v"(acc[2][0]), "+v"(acc[2][1]), "+v"(acc[3][0]), "+v"(acc[3][1]));
    SBAR();
    int rb, ct, kh; tile_down(cx.bid, rb, ct, kh);
    f32x4 keep[2][2];
#pragma unroll
    for (int mm = 0; mm < 2; ++mm) {
        const f32x4 g0 = kh ? acc[mm][0] : acc[2 + mm][0], g1 = kh ? acc[mm][1] : acc[2 + mm][1];
        st16(ws + WS_XCH + ((size_t)cx.bid * 8 + wid) * 2048, lane * 16 + mm * 1024, ((u32x4){cvtpk(g0[0], g0[1]), cvtpk(g0[2], g0[3]), cvtpk(g1[0], g1[1]), cvtpk(g1[2], g1[3])}));
        keep[mm][0] = kh ? acc[2 + mm][0] : acc[mm][0]; keep[mm][1] = kh ? acc[2 + mm][1] : acc[mm][1];
    }
    BARRIER();
    u32x2 xr[2][2]; f32x4 gb[2];
#pragma unroll
    for (int nf = 0; nf < 2; ++nf) {
        gb[nf] = *(const LAS f32x4*)(lds + 114688 + (wn * 8 + nf * 4 + g) * 16);
#pragma unroll
        for (int mm = 0; mm < 2; ++mm) xr[mm][nf] = *(const LAS u32x2*)(lds + 98304 + ((wm * 2 + mm) * 16 + wn * 4 + nf * 2 + (g >> 1)) * 256 + (lane & 15) * 16 + 8 * (g & 1));
    }
    u32x4 got[2];
    {
        const char* xi = ws + WS_XCH + ((size_t)(cx.bid ^ 1) * 8 + wid) * 2048;
        for (unsigned spins = 0; spins < (1u << 15); ++spins) {
            asm volatile("s_nop 4\n\tglobal_load_dwordx4 %0, %2, %3 sc1\n\tglobal_load_dwordx4 %1, %2, %3 offset:1024 sc1\n\ts_waitcnt vmcnt(0)" : "=&v"(got[0]), "=&v"(got[1]) : "v"(lane * 16), "s"(xi) : "memory");
            const bool ok = got[0][0] != ~0u && got[0][1] != ~0u && got[0][2] != ~0u && got[0][3] != ~0u && got[1][0] != ~0u && got[1][1] != ~0u && got[1][2] != ~0u && got[1][3] != ~0u;
            if (__all(ok)) break;
            __builtin_amdgcn_s_sleep(2);
        }
    }
#pragma unroll
    for (int mm = 0; mm < 2; ++mm) {
        unsigned u[2][2];
#pragma unroll
        for (int nf = 0; nf < 2; ++nf) {
            const f32x4 gg = gb[nf], k = keep[mm][nf];
            u[nf][0] = cvtpk(bflo(xr[mm][nf][0]) + gg[0] * (k[0] + bflo(got[mm][2 * nf])), bfhi(xr[mm][nf][0]) + gg[1] * (k[1] + bfhi(got[mm][2 * nf])));
            u[nf][1] = cvtpk(bflo(xr[mm][nf][1]) + gg[2] * (k[2] + bflo(got[mm][2 * nf + 1])), bfhi(xr[mm][nf][1]) + gg[3] * (k[3] + bfhi(got[mm][2 * nf + 1])));
        }
        st16o(out + (size_t)(rb * 128 + wm * 64 + (2 * kh + mm) * 16) * D + ct * 128 + wn * 32, (unsigned)((lane & 15) * 2048 + (((g & 1) << 1) | (g >> 1)) * 16), swap16(u[0][0], u[0][1], u[1][0], u[1][1]));
    }
}

#define CTX_INIT extern __shared__ __attribute__((aligned(16))) char smem[]; \
    const int wid_ = uni(threadIdx.x >> 6); const bool prod = wid_ >= 8; \
    Ctx cx = {ws, (lds_ptr)smem, uni((unsigned)(uintptr_t)(lds_ptr)smem), (int)blockIdx.x, prod ? wid_ - 8 : wid_, 0ull}; \
    if (prod) __builtin_amdgcn_s_setprio(3)
__global__ __launch_bounds__(768, 3) void k_main(const u16* __restrict__ x, const u16* __restrict__ wq, const u16* __restrict__ wk, const u16* __restrict__ wv, const u16* __restrict__ wo,
                                                 const u16* __restrict__ wg, const u16* __restrict__ wu, const u16* __restrict__ wd, const u16* __restrict__ c, const u16* __restrict__ wmod,
                                                 char* __restrict__ ws, u16* __restrict__ out) {
    CTX_INIT;
    if (prod) {
        OPre o;
        ln1kv_produce(cx, wq, wk, wv, [&]() { attn_pre(cx); });
        attn_produce(cx, wo, wg, wu, wd, c, wmod, [&]() { oproj_pre(cx, o); });
        oproj_produce(cx, o, x, [&]() { glu_pre(cx); });
        glu_produce(cx, [&]() { down_pre(cx, o); });
        down_produce(cx, o);
    } else {
        ln1kv_consume(cx, x, c, wmod);
        attn_consume(cx);
        oproj_consume(cx);
        glu_consume(cx);
        down_consume(cx, out);
    }
}

extern "C" void kernel_launch(void* const* d_in, const int* in_sizes, int n_in, void* d_out, int out_size, void* d_ws, size_t ws_size, hipStream_t stream) {
    constexpr int LDS = 163840;
    (void)hipFuncSetAttribute((const void*)k_main, hipFuncAttributeMaxDynamicSharedMemorySize, LDS);
    k_main<<<256, 768, LDS, stream>>>((const u16*)d_in[0], (const u16*)d_in[1], (const u16*)d_in[2], (const u16*)d_in[3], (const u16*)d_in[4], (const u16*)d_in[6], (const u16*)d_in[7],
                                      (const u16*)d_in[8], (const u16*)d_in[9], (const u16*)d_in[10], (char*)d_ws, (u16*)d_out);
}
```

```cpp
#include <hip/hip_runtime.h>
#include <hip/hip_bf16.h>

#define LAS __attribute__((address_space(3)))
#define GAS __attribute__((address_space(1)))
typedef unsigned short u16;
typedef short bf16x8 __attribute__((ext_vector_type(8)));
typedef short s16x4 __attribute__((ext_vector_type(4)));
typedef float f32x2 __attribute__((ext_vector_type(2)));
typedef float f32x4 __attribute__((ext_vector_type(4)));
typedef float f32x16 __attribute__((ext_vector_type(16)));
typedef unsigned u32x4 __attribute__((ext_vector_type(4)));
typedef unsigned u32x2 __attribute__((ext_vector_type(2)));
typedef __bf16 bf16x2v __attribute__((ext_vector_type(2)));
typedef LAS char* lds_ptr;

constexpr int D = 1024, F = 3584;
constexpr size_t WS_CNT = 0;
constexpr int C_KV = 0, C_SIDE = 16, C_ST = 32, C_EARLY = 40, C_NUM = 56;
constexpr size_t WS_FLG = 1u << 20;
constexpr int F_H1 = 0, F_O = 256, F_Y = 512, F_HH = 768, F_WQ = 1024;
constexpr size_t WS_EPOCH = 98304 + 4096;
constexpr size_t WS_MP1 = 32768;
constexpr size_t WS_MP2 = 786432;
constexpr size_t WS_KN = 98304;
constexpr size_t WS_KV = 262144;
constexpr size_t WS_ST2 = 524288;
constexpr size_t WS_UT = (1u << 20) + 262144;
constexpr size_t WS_H1 = 2u << 20;
constexpr size_t WS_O = WS_H1 + (4u << 20);
constexpr size_t WS_X1 = WS_O + (4u << 20);
constexpr size_t WS_Y = WS_X1 + (4u << 20);
constexpr size_t WS_HH = WS_Y + (4u << 20);
constexpr size_t WS_XCH = WS_HH + (16u << 20);
constexpr size_t WS_WQ = WS_XCH + (4u << 20);
constexpr size_t WS_WO = WS_WQ + (2u << 20);
constexpr size_t WS_WGU = WS_WO + (2u << 20);
constexpr size_t WS_WD = WS_WGU + (16u << 20);

__device__ __forceinline__ float bf2f(u16 b) { return __uint_as_float(((unsigned)b) << 16); }
__device__ __forceinline__ float bflo(unsigned w) { return __uint_as_float(w << 16); }
__device__ __forceinline__ float bfhi(unsigned w) { return __uint_as_float(w & 0xffff0000u); }
__device__ __forceinline__ unsigned cvtpk(float lo, float hi) { return __builtin_bit_cast(unsigned, __builtin_convertvector((f32x2){lo, hi}, bf16x2v)); }
__device__ __forceinline__ float rbf(float x) { return bflo(cvtpk(x, x)); }
__device__ __forceinline__ f32x4 rbf4(f32x4 s) { return (f32x4){rbf(s[0]), rbf(s[1]), rbf(s[2]), rbf(s[3])}; }

__device__ __forceinline__ void dma16(const void* gbase, unsigned voff, unsigned ldsaddr) {
    asm volatile("s_mov_b32 m0, %0\n\ts_nop 3\n\tglobal_load_lds_dwordx4 %1, %2" ::"s"(ldsaddr), "v"(voff), "s"(gbase) : "memory");
}
__device__ __forceinline__ void dmant(const void* gbase, unsigned voff, unsigned ldsaddr) {
    asm volatile("s_mov_b32 m0, %0\n\ts_nop 3\n\tglobal_load_lds_dwordx4 %1, %2 nt" ::"s"(ldsaddr), "v"(voff), "s"(gbase) : "memory");
}
__device__ __forceinline__ void dmac(const void* gbase, unsigned voff, unsigned ldsaddr) {
    asm volatile("s_mov_b32 m0, %0\n\ts_nop 3\n\tglobal_load_lds_dwordx4 %1, %2 sc1" ::"s"(ldsaddr), "v"(voff), "s"(gbase) : "memory");
}
#define LD16(dst, base, voff) asm volatile("s_nop 4\n\tglobal_load_dwordx4 %0, %1, %2" : "=v"(dst) : "v"(voff), "s"(base) : "memory")
#define LD16C(dst, base, voff) asm volatile("s_nop 4\n\tglobal_load_dwordx4 %0, %1, %2 sc1" : "=v"(dst) : "v"(voff), "s"(base) : "memory")
#define LD8C(dst, base, voff) asm volatile("s_nop 4\n\tglobal_load_dwordx2 %0, %1, %2 sc1" : "=v"(dst) : "v"(voff), "s"(base) : "memory")
#define TOUCH(reg, base, voff) asm volatile("s_nop 4\n\tglobal_load_dword %0, %1, %2 sc1" : "+v"(reg) : "v"(voff), "s"(base) : "memory")
template <class T> __device__ __forceinline__ void st16(const void* base, unsigned voff, T v) {
    asm volatile("s_nop 4\n\tglobal_store_dwordx4 %0, %1, %2 sc1\n\ts_nop 1" ::"v"(voff), "v"(v), "s"(base) : "memory");
}
template <class T> __device__ __forceinline__ void st16o(const void* base, unsigned voff, T v) {
    asm volatile("s_nop 4\n\tglobal_store_dwordx4 %0, %1, %2 sc1 nt\n\ts_nop 1" ::"v"(voff), "v"(v), "s"(base) : "memory");
}
template <class T> __device__ __forceinline__ void st8(const void* base, unsigned voff, T v) { asm volatile("s_nop 4\n\tglobal_store_dwordx2 %0, %1, %2 sc1" ::"v"(voff), "v"(v), "s"(base) : "memory"); }
__device__ __forceinline__ void st4(const void* base, unsigned voff, float v) { asm volatile("s_nop 4\n\tglobal_store_dword %0, %1, %2 sc1" ::"v"(voff), "v"(v), "s"(base) : "memory"); }
#define WAIT_V(n) asm volatile("s_waitcnt vmcnt(" #n ")" ::: "memory")
#define BARRIER() asm volatile("s_barrier" ::: "memory")
#define BAR_L() asm volatile("s_waitcnt lgkmcnt(0)\n\ts_barrier" ::: "memory")
#define WAITV_BAR(n) asm volatile("s_waitcnt vmcnt(" #n ")\n\ts_barrier" ::: "memory")
#define WAITVL_BAR(n) asm volatile("s_waitcnt vmcnt(" #n ") lgkmcnt(0)\n\ts_barrier" ::: "memory")
#define PIN(x) asm volatile("" : "+v"(x))
#define SBAR() __builtin_amdgcn_sched_barrier(0)
#define MFMA32(a, b, c) __builtin_amdgcn_mfma_f32_32x32x16_bf16(a, b, c, 0, 0, 0)
#define MFMA16(a, b, c) __builtin_amdgcn_mfma_f32_16x16x32_bf16(a, b, c, 0, 0, 0)
#define MFMA16_ACC(c, a, b) asm volatile("v_mfma_f32_16x16x32_bf16 %0, %1, %2, %0" : "+v"(c) : "v"(a), "v"(b))
#define MFMA32_ACC(c, a, b) asm volatile("v_mfma_f32_32x32x16_bf16 %0, %1, %2, %0" : "+v"(c) : "v"(a), "v"(b))
#define ZERO4 ((f32x4){0.f, 0.f, 0.f, 0.f})
__device__ __forceinline__ bf16x8 ldsr128(lds_ptr p) { return *(const LAS bf16x8*)p; }
__device__ __forceinline__ s16x4 ldstr(lds_ptr p) { return __builtin_amdgcn_ds_read_tr16_b64_v4i16((LAS s16x4*)p); }
__device__ __forceinline__ bf16x8 ldstr2(lds_ptr p, int d) { s16x4 a = ldstr(p), b = ldstr(p + d); return (bf16x8){a[0], a[1], a[2], a[3], b[0], b[1], b[2], b[3]}; }
__device__ __forceinline__ unsigned uni(unsigned x) { return (unsigned)__builtin_amdgcn_readfirstlane((int)x); }
__device__ __forceinline__ int lane_id() { int l = __builtin_amdgcn_mbcnt_hi(~0u, __builtin_amdgcn_mbcnt_lo(~0u, 0u)); PIN(l); return l; }
__device__ __forceinline__ u32x4 swap16(unsigned a0, unsigned a1, unsigned b0, unsigned b1) {
    const auto r0 = __builtin_amdgcn_permlane16_swap(a0, b0, false, false), r1 = __builtin_amdgcn_permlane16_swap(a1, b1, false, false);
    return (u32x4){r0[0], r1[0], r0[1], r1[1]};
}
__device__ __forceinline__ u32x4 swap32(unsigned a0, unsigned a1, unsigned b0, unsigned b1) {
    const auto r0 = __builtin_amdgcn_permlane32_swap(a0, b0, false, false), r1 = __builtin_amdgcn_permlane32_swap(a1, b1, false, false);
    return (u32x4){r0[0], r1[0], r0[1], r1[1]};
}


template <int CTRL> __device__ __forceinline__ float dppf(float v) { return __builtin_bit_cast(float, __builtin_amdgcn_mov_dpp(__builtin_bit_cast(int, v), CTRL, 0xf, 0xf, true)); }
__device__ __forceinline__ float half_sum(float v) { const auto r = __builtin_amdgcn_permlane32_swap(__float_as_uint(v), __float_as_uint(v), false, false); return __uint_as_float(r[0]) + __uint_as_float(r[1]); }
__device__ __forceinline__ float rows_sum(float v) {
    const auto r = __builtin_amdgcn_permlane16_swap(__float_as_uint(v), __float_as_uint(v), false, false); return half_sum(__uint_as_float(r[0]) + __uint_as_float(r[1]));
}
__device__ __forceinline__ float rows_max(float v) {
    auto r = __builtin_amdgcn_permlane16_swap(__float_as_uint(v), __float_as_uint(v), false, false); v = fmaxf(__uint_as_float(r[0]), __uint_as_float(r[1]));
    r = __builtin_amdgcn_permlane32_swap(__float_as_uint(v), __float_as_uint(v), false, false); return fmaxf(__uint_as_float(r[0]), __uint_as_float(r[1]));
}
__device__ __forceinline__ float row16_sum(float v) { v += dppf<0xB1>(v); v += dppf<0x4E>(v); v += dppf<0x141>(v); v += dppf<0x140>(v); return v; }
__device__ __forceinline__ float row16_max(float v) { v = fmaxf(v, dppf<0xB1>(v)); v = fmaxf(v, dppf<0x4E>(v)); v = fmaxf(v, dppf<0x141>(v)); v = fmaxf(v, dppf<0x140>(v)); return v; }
__device__ __forceinline__ float wsum(float v) { return rows_sum(row16_sum(v)); }
__device__ __forceinline__ float wmax(float v) { return rows_max(row16_max(v)); }
__device__ __forceinline__ f32x4 sum8(f32x4 (&p)[8]) {
    asm volatile("" : "+v"(p[0]), "+v"(p[1]), "+v"(p[2]), "+v"(p[3]), "+v"(p[4]), "+v"(p[5]), "+v"(p[6]), "+v"(p[7]));
    return ((p[0] + p[1]) + (p[2] + p[3])) + ((p[4] + p[5]) + (p[6] + p[7]));
}

__device__ __forceinline__ void signal(char* ws, int idx, int) {
    unsigned long long sv;
    asm volatile("s_mov_b64 %0, exec\n\ts_mov_b64 exec, 1\n\ts_nop 2\n\tglobal_atomic_add %1, %2, %3\n\ts_mov_b64 exec, %0" : "=&s"(sv) : "v"(idx * 256), "v"(1u), "s"(ws + WS_CNT) : "memory");
}
template <int SLEEP>
__device__ __forceinline__ void await(char* ws, int idx, int n, unsigned need, int lane) {
    {
        GAS unsigned* p = (GAS unsigned*)(ws + WS_CNT) + (idx + (lane < n ? lane : 0)) * 64;
        for (unsigned spins = 0; spins < (1u << 15); ++spins) {
            if (__all(__hip_atomic_load(p, __ATOMIC_RELAXED, __HIP_MEMORY_SCOPE_AGENT) >= need)) break;
            __builtin_amdgcn_s_sleep(SLEEP);
        }
        asm volatile("s_waitcnt vmcnt(0)" ::: "memory");
    }
}
__device__ __forceinline__ void raise(char* ws, int idx, unsigned long long tag, int) {
    unsigned long long sv;
    asm volatile("s_mov_b64 %0, exec\n\ts_mov_b64 exec, 1\n\ts_nop 2\n\tglobal_store_dwordx2 %1, %2, %3 sc1\n\ts_mov_b64 exec, %0" : "=&s"(sv) : "v"(idx * 128), "v"(tag), "s"(ws + WS_FLG) : "memory");
}
__device__ __forceinline__ void expect(char* ws, int idx0, int off, unsigned long long tag) {
    GAS unsigned long long* p = (GAS unsigned long long*)(ws + WS_FLG) + (idx0 + (off < 0 ? 0 : off)) * 16;
    for (unsigned spins = 0; spins < (1u << 15); ++spins) {
        if (__all(off < 0 || __hip_atomic_load(p, __ATOMIC_RELAXED, __HIP_MEMORY_SCOPE_AGENT) == tag)) break;
        __builtin_amdgcn_s_sleep(2);
    }
    asm volatile("s_waitcnt vmcnt(0)" ::: "memory");
}
__device__ __forceinline__ void drain_raise(char* ws, lds_ptr word, int idx, unsigned long long tag, int lane) {
    asm volatile("s_waitcnt vmcnt(0)" ::: "memory");
    unsigned old = 0u;
    if (lane == 0) old = __hip_atomic_fetch_add((LAS unsigned*)word, 1u, __ATOMIC_RELAXED, __HIP_MEMORY_SCOPE_WORKGROUP);
    if (uni(old) == 7u) raise(ws, idx, tag, lane);
}
#define INPUT_BARRIER() BAR_L()

__device__ __forceinline__ void pack_load(const char* src  , unsigned pitch, unsigned ldsaddr, int lane) {
    const unsigned vo = (unsigned)(lane >> 3) * pitch, sw = (lane & 7) ^ (((lane >> 4) & 1) << 1);
#pragma unroll
    for (int i = 0; i < 4; ++i) dmant(src + (size_t)i * 8 * pitch, vo + ((sw ^ ((i & 1) << 2)) << 4), ldsaddr + i * 1024);
}
__device__ __forceinline__ bf16x8 pack_frag(lds_ptr area, int nf, int lane) {
    const int g = lane >> 4, q = (lane & 15) >> 2;
    return ldstr2(area + (8 * g + q) * 128 + ((nf ^ (((q >> 1) & 1) | ((g & 1) << 1))) << 5) + (lane & 3) * 8, 512);
}
__device__ __forceinline__ void mod_unit(lds_ptr area, bf16x8 sc, lds_ptr red, int lane) {
    f32x4 d[4];
#pragma unroll
    for (int nf = 0; nf < 4; ++nf) d[nf] = MFMA16(pack_frag(area, nf, lane), sc, ZERO4);
    if ((lane & 15) == 0) {
#pragma unroll
        for (int nf = 0; nf < 4; ++nf) *(LAS f32x4*)(red + nf * 64 + (lane >> 4) * 16) = d[nf];
    }
}

__device__ __forceinline__ float silu_bf(float cv) { return rbf(cv / (1.f + __expf(-cv))); }
__device__ __forceinline__ bf16x8 silu_frag(u32x4 cw) {
    PIN(cw);
    u32x4 r;
#pragma unroll
    for (int j = 0; j < 4; ++j) r[j] = cvtpk(silu_bf(bflo(cw[j])), silu_bf(bfhi(cw[j])));
    return __builtin_bit_cast(bf16x8, r);
}

__device__ __forceinline__ void tile128x64(int bid, int& rb, int& ct) { const int xcd = bid & 7, idx = bid >> 3; rb = (xcd >> 1) * 4 + (idx & 3); ct = (xcd & 1) * 8 + (idx >> 2); }
__device__ __forceinline__ unsigned long long epoch_load(char* ws) { return __hip_atomic_load((GAS unsigned long long*)(ws + WS_EPOCH), __ATOMIC_RELAXED, __HIP_MEMORY_SCOPE_AGENT); }
__device__ __forceinline__ unsigned long long epoch_tag(unsigned long long e) { return ((((unsigned long long)uni((unsigned)(e >> 32)) << 32) | uni((unsigned)e)) + 1ull) << 24; }
__device__ __forceinline__ void tile_down(int bid, int& rb, int& ct, int& kh) { const int xcd = bid & 7, idx = bid >> 3; kh = xcd & 1; rb = (xcd >> 1) * 4 + (idx & 3); ct = idx >> 2; }
struct Ctx { char* ws; lds_ptr lds; unsigned lds0; int bid, w; unsigned long long tag;
};

__device__ __forceinline__ void ln1kv_consume(Ctx& cx, const u16* __restrict__ x, const u16* __restrict__ c, const u16* __restrict__ wmod) {
    char* ws = cx.ws; const lds_ptr lds = cx.lds; const int wid = cx.w, lane = lane_id(), tid = wid * 64 + lane;
    const int rb = (cx.bid >> 4) * 8 + (cx.bid & 7), isv = (cx.bid >> 3) & 1;
    const int idx = cx.bid >> 3, cs = ((((idx & 3) << 3) | (cx.bid & 7)) << 1) | ((idx >> 2) & 1), kq = idx >> 3, kr = kq * 256 + (tid >> 2);
    const u32x4 w0 = __builtin_nontemporal_load((const u32x4*)(wmod + (size_t)kr * 6144 + cs * 32 + (tid & 3) * 8)), w1 = __builtin_nontemporal_load((const u32x4*)(wmod + (size_t)(kr + 128) * 6144 + cs * 32 + (tid & 3) * 8));
    const u16 c0 = c[kr], c1 = c[kr + 128];
    const unsigned long long ep = epoch_load(ws);
    u32x4 xv[2][2];
    {
        const float s0 = silu_bf(bf2f(c0)), s1 = silu_bf(bf2f(c1));
        float a[8];
#pragma unroll
        for (int j = 0; j < 4; ++j) { a[2 * j] = s0 * bflo(w0[j]) + s1 * bflo(w1[j]); a[2 * j + 1] = s0 * bfhi(w0[j]) + s1 * bfhi(w1[j]); }
#pragma unroll
        for (int j = 0; j < 8; ++j) PIN(a[j]);
        asm volatile("" ::: "memory");
#pragma unroll
        for (int rr = 0; rr < 2; ++rr)
#pragma unroll
            for (int hh = 0; hh < 2; ++hh) xv[rr][hh] = __builtin_nontemporal_load((const u32x4*)(x + (size_t)(rb * 16 + 2 * wid + rr) * D + hh * 512 + lane * 8));
        SBAR();
#pragma unroll
        for (int j = 0; j < 8; ++j) { a[j] += dppf<0x124>(a[j]); a[j] += dppf<0x128>(a[j]); a[j] = rows_sum(a[j]); }
        cx.tag = epoch_tag(ep);
        asm volatile("" : "+s"(cx.tag));
        if (lane < 4) { *(LAS f32x4*)(lds + 114688 + wid * 128 + lane * 32) = (f32x4){a[0], a[1], a[2], a[3]}; *(LAS f32x4*)(lds + 114688 + wid * 128 + lane * 32 + 16) = (f32x4){a[4], a[5], a[6], a[7]}; }
    }
    BAR_L();
    const unsigned long long tag = cx.tag;
    if (wid == 0 && lane < 32) {
        const LAS float* r = (const LAS float*)(lds + 114688) + lane;
        const unsigned pv = __float_as_uint(((r[0] + r[32]) + (r[64] + r[96])) + ((r[128] + r[160]) + (r[192] + r[224])));
        __hip_atomic_store((GAS unsigned long long*)(ws + WS_MP1) + (kq * 2048 + ((cs * 32 + 1024) & 2047) + lane), tag | ((pv + 0x80u) >> 8), __ATOMIC_RELAXED, __HIP_MEMORY_SCOPE_AGENT);
    }
    float xn[2][16];
#pragma unroll
    for (int rr = 0; rr < 2; ++rr) {
        float* v = xn[rr];
#pragma unroll
        for (int hh = 0; hh < 2; ++hh)
#pragma unroll
            for (int j = 0; j < 4; ++j) { v[hh * 8 + 2 * j] = bflo(xv[rr][hh][j]); v[hh * 8 + 2 * j + 1] = bfhi(xv[rr][hh][j]); }
        float sm = 0.f;
#pragma unroll
        for (int j = 0; j < 16; ++j) sm += v[j];
        const float mean = wsum(sm) * (1.f / 1024.f);
        float q = 0.f;
#pragma unroll
        for (int j = 0; j < 16; ++j) { v[j] -= mean; q += v[j] * v[j]; }
        const float rstd = 1.f / sqrtf(wsum(q) * (1.f / 1024.f) + 1e-5f);
#pragma unroll
        for (int j = 0; j < 16; ++j) { v[j] *= rstd; PIN(v[j]); }
    }
    LAS float* tab = (LAS float*)(lds + 98304);
    {
        u32x4 g[4][2];
        const unsigned tl = (unsigned)tag, th = (unsigned)(tag >> 32);
        __builtin_amdgcn_s_sleep(18);
        for (unsigned spins = 0; spins < (1u << 15); ++spins) {
            asm volatile("s_nop 4\n\tglobal_load_dwordx4 %0, %8, %12 sc1\n\tglobal_load_dwordx4 %1, %8, %13 sc1\n\tglobal_load_dwordx4 %2, %9, %12 sc1\n\tglobal_load_dwordx4 %3, %9, %13 sc1\n\t"
                         "global_load_dwordx4 %4, %10, %12 sc1\n\tglobal_load_dwordx4 %5, %10, %13 sc1\n\tglobal_load_dwordx4 %6, %11, %12 sc1\n\tglobal_load_dwordx4 %7, %11, %13 sc1\n\ts_waitcnt vmcnt(0)"
                         : "=&v"(g[0][0]), "=&v"(g[0][1]), "=&v"(g[1][0]), "=&v"(g[1][1]), "=&v"(g[2][0]), "=&v"(g[2][1]), "=&v"(g[3][0]), "=&v"(g[3][1])
                         : "v"(tid * 16), "v"(tid * 16 + 16384), "v"(tid * 16 + 32768), "v"(tid * 16 + 49152), "s"(ws + WS_MP1), "s"(ws + WS_MP1 + 8192) : "memory");
            unsigned bad = 0u;
#pragma unroll
            for (int k = 0; k < 4; ++k)
                bad |= ((g[k][0][0] ^ tl) | (g[k][0][2] ^ tl) | (g[k][1][0] ^ tl) | (g[k][1][2] ^ tl)) >> 24 | (g[k][0][1] ^ th) | (g[k][0][3] ^ th) | (g[k][1][1] ^ th) | (g[k][1][3] ^ th);
            if (__all(bad == 0u)) break;
            __builtin_amdgcn_s_sleep(8);
        }
        f32x4 v;
#pragma unroll
        for (int j = 0; j < 4; ++j)
            v[j] = (__uint_as_float(g[0][j >> 1][(j & 1) * 2] << 8) + __uint_as_float(g[1][j >> 1][(j & 1) * 2] << 8)) + (__uint_as_float(g[2][j >> 1][(j & 1) * 2] << 8) + __uint_as_float(g[3][j >> 1][(j & 1) * 2] << 8));
        v = rbf4(v);
        *(LAS f32x2*)(tab + tid * 2) = (f32x2){rbf(1.f + v[0]), rbf(1.f + v[1])};
        *(LAS f32x2*)(tab + 1024 + tid * 2) = (f32x2){v[2], v[3]};
    }
    BAR_L();
    if (cx.bid == 0 && tid == 0) __hip_atomic_store((GAS unsigned long long*)(ws + WS_EPOCH), tag >> 24, __ATOMIC_RELAXED, __HIP_MEMORY_SCOPE_AGENT);
    const lds_ptr img = lds + 65536;
#pragma unroll
    for (int hh = 0; hh < 2; ++hh) {
        const int c = hh * 64 + lane;
        const f32x4 g0 = *(const LAS f32x4*)(tab + c * 8), g1 = *(const LAS f32x4*)(tab + c * 8 + 4);
        const f32x4 b0 = *(const LAS f32x4*)(tab + 1024 + c * 8), b1 = *(const LAS f32x4*)(tab + 1024 + c * 8 + 4);
#pragma unroll
        for (int rr = 0; rr < 2; ++rr) {
            float h[8];
#pragma unroll
            for (int j = 0; j < 4; ++j) { h[j] = xn[rr][hh * 8 + j] * g0[j] + b0[j]; h[4 + j] = xn[rr][hh * 8 + 4 + j] * g1[j] + b1[j]; }
            *(LAS u32x4*)(img + c * 256 + (((2 * wid + rr) ^ (c & 15)) << 4)) = (u32x4){cvtpk(h[0], h[1]), cvtpk(h[2], h[3]), cvtpk(h[4], h[5]), cvtpk(h[6], h[7])};
        }
    }
    BAR_L();
    f32x4 acc[2] = {ZERO4, ZERO4};
    const int g = lane >> 4, q = (lane & 15) >> 2, p = lane & 3;
    bf16x8 hf[4], wf[4][2];
#pragma unroll
    for (int i = 0; i < 4; ++i) { const int cc = 4 * (wid * 4 + i) + g; hf[i] = ldsr128(img + cc * 256 + (((lane & 15) ^ (cc & 15)) << 4)); }
    const char* hb = ws + WS_H1 + ((size_t)rb * 128 + 16 * wid) * 256;
    if (isv) { st16(hb, lane * 16 + 1024, hf[1]); st16(hb, lane * 16 + 3072, hf[3]); } else { st16(hb, lane * 16, hf[0]); st16(hb, lane * 16 + 2048, hf[2]); }
#pragma unroll
    for (int i = 0; i < 4; ++i)
#pragma unroll
        for (int nf = 0; nf < 2; ++nf) wf[i][nf] = ldstr2(lds + (32 * (wid * 4 + i) + 8 * g + q) * 64 + ((nf ^ (g & 1)) << 5) + 8 * p, 256);
    LAS float* red = (LAS float*)(lds + 147456);
    if (isv) {
#pragma unroll
        for (int i = 0; i < 4; ++i) { acc[0] = MFMA16(hf[i], wf[i][0], acc[0]); acc[1] = MFMA16(hf[i], wf[i][1], acc[1]); }
#pragma unroll
        for (int nf = 0; nf < 2; ++nf) *(LAS f32x4*)(red + wid * 512 + (nf * 16 + (lane & 15)) * 16 + 4 * g) = acc[nf];
    } else {
#pragma unroll
        for (int i = 0; i < 4; ++i) { acc[0] = MFMA16(wf[i][0], hf[i], acc[0]); acc[1] = MFMA16(wf[i][1], hf[i], acc[1]); }
#pragma unroll
        for (int nf = 0; nf < 2; ++nf) *(LAS f32x4*)(red + wid * 512 + (lane & 15) * 32 + nf * 16 + 4 * g) = acc[nf];
    }
    WAITVL_BAR(0);
    if (wid == 0) {
        const int o0 = isv ? (lane & 31) * 16 + 4 * (lane >> 5) : (lane & 15) * 32 + (g >> 1) * 16 + (g & 1) * 4, o1 = o0 + 8;
        f32x4 a = ZERO4, b = ZERO4;
#pragma unroll
        for (int w = 0; w < 8; ++w) { a += *(const LAS f32x4*)(red + w * 512 + o0); b += *(const LAS f32x4*)(red + w * 512 + o1); }
        const u32x4 v = {cvtpk(a[0], a[1]), cvtpk(a[2], a[3]), cvtpk(b[0], b[1]), cvtpk(b[2], b[3])};
        st16(ws + WS_KV + (size_t)(rb >> 1) * 4096, isv ? 2048 + (rb & 1) * 1024 + lane * 16 : (g >> 1) * 1024 + ((g & 1) * 32 + (rb & 1) * 16 + (lane & 15)) * 16, v);
        float n2 = 0.f, mx = 0.f;
#pragma unroll
        for (int j = 0; j < 4; ++j) { n2 += bflo(v[j]) * bflo(v[j]) + bfhi(v[j]) * bfhi(v[j]); mx = fmaxf(mx, fmaxf(fabsf(bflo(v[j])), fabsf(bfhi(v[j])))); }
        if (!isv) mx = rows_sum(n2);
        if (!(n2 - n2 == 0.f)) mx = INFINITY;
        mx = wmax(mx);
        if (lane == 0) st4(ws + WS_KN, (unsigned)(isv * 128 + rb) * 4u, mx);
    }
    WAITV_BAR(0);
}
template <class Next>
__device__ __forceinline__ void ln1kv_produce(Ctx& cx, const u16* __restrict__ wq, const u16* __restrict__ wk, const u16* __restrict__ wv, const u16* __restrict__ c, const u16* __restrict__ wmod, Next next_pre) {
    char* ws = cx.ws; const int pw = cx.w, lane = lane_id();
    cx.tag = epoch_tag(epoch_load(ws));
    asm volatile("" : "+s"(cx.tag));
    int rbq, hp; tile128x64(cx.bid, rbq, hp);
    if (cx.bid == 0 && pw == 2) { if (lane < C_NUM) st4(ws + WS_CNT, (unsigned)lane * 256u, 0.f); WAIT_V(0); }
    BARRIER();
    if (pw < 2) pack_load((const char*)wq + (size_t)(rbq * 2 + pw) * 65536 + hp * 128, 2048, cx.lds0 + 106496 + pw * 4096, lane);
    {
        const char* w = (const char*)(((cx.bid >> 3) & 1) ? wv : wk) + pw * 16384;
        const unsigned voff = (lane >> 2) * 64 + (((lane & 3) ^ (((lane >> 5) & 1) << 1)) << 4);
#pragma unroll
        for (int i = 0; i < 16; ++i) dma16(w + i * 1024, voff, cx.lds0 + pw * 16384 + i * 1024);
    }
    const int k0 = (cx.bid >> 5) * 128 + pw * 32, col2 = 1024 + (cx.bid & 31) * 64;
    u32x4 cw;
    pack_load((const char*)wmod + (size_t)k0 * 12288 + (2048 + col2) * 2, 12288, cx.lds0 + 126976 + pw * 4096, lane);
    LD16(cw, c, (unsigned)(k0 + (lane >> 4) * 8) * 2u);
    if (pw < 2) {
        WAIT_V(21);
#pragma unroll
        for (int nf = 0; nf < 4; ++nf) st16(ws + WS_WQ + (size_t)(hp * 32 + rbq * 2 + pw) * 4096, lane * 16 + nf * 1024, pack_frag(cx.lds + 106496 + pw * 4096, nf, lane));
    }
    WAIT_V(0);
    mod_unit(cx.lds + 126976 + pw * 4096, silu_frag(cw), cx.lds + 143360 + pw * 256, lane);
    BAR_L();
    if (pw == 0) {
        raise(ws, F_WQ + hp * 16 + rbq, cx.tag, lane);
        const LAS float* r = (const LAS float*)(cx.lds + 143360) + lane;
        st4(ws + WS_MP2, (unsigned)((cx.bid >> 5) * 4096 + col2 + lane) * 4u, (r[0] + r[64]) + (r[128] + r[192]));
    }
    BARRIER();
    expect(ws, F_WQ + hp * 16, lane < 16 ? lane : -1, cx.tag);
    BARRIER();
    if (pw == 0) raise(ws, F_H1 + cx.bid, cx.tag, lane);
    next_pre();
}

template <int WB, int NPRE>
__device__ __forceinline__ void g64_pre(const Ctx& cx, const char* Wp, int lane) {
#pragma unroll
    for (int t = 0; t < NPRE; ++t) { const unsigned d = cx.lds0 + (WB + t * 8192) % 163840 + cx.w * 2048; dmac(Wp + cx.w * 2048 + t * 8192, lane * 16, d); dmac(Wp + cx.w * 2048 + t * 8192 + 1024, lane * 16, d + 1024); }
}
template <int WB, int AB, int NPRE, int NTAIL, class Issued, class First, class Rest, class Tail>
__device__ __forceinline__ void g64_produce(const Ctx& cx, const char* Ap, const char* Wp, int lane, Issued issued, First first, Rest rest, Tail tail) {
    const char* Aw = Ap + (size_t)(2 * cx.w) * 32768; const char* Ww = Wp + cx.w * 2048;
    const unsigned voff = lane * 16, dA = cx.lds0 + AB + cx.w * 4096, dW = cx.lds0 + cx.w * 2048;
#define G64_A(t) do { const char* a_ = Aw + (t) * 2048; const unsigned d_ = dA + ((t) % 5) * 16384; dmac(a_, voff, d_); dmac(a_ + 32768, voff, d_ + 2048); dmac(a_ + 1024, voff, d_ + 1024); dmac(a_ + 33792, voff, d_ + 3072); } while (0)
#define G64_W(t, slot) do { const unsigned d_ = dW + (WB + (slot) * 8192) % 163840; dmac(Ww + (t) * 8192, voff, d_); dmac(Ww + (t) * 8192 + 1024, voff, d_ + 1024); } while (0)
    G64_A(0); G64_A(1);
    issued();
    WAITV_BAR(4);
    G64_A(2); G64_A(3); G64_A(4);
    constexpr int E = (10 - NPRE) * 2;
    if (NPRE == 6) { G64_W(6, 6); G64_W(7, 7); G64_W(8, 8); G64_W(9, 9); }
    first();
#define G64_BAR(n) asm volatile("s_waitcnt vmcnt(%0)\n\ts_barrier" ::"i"(n) : "memory")
    G64_BAR(12 + E);
    rest();
    G64_BAR(8 + E); G64_A(5); G64_W(10, 0);
    G64_BAR(10 + E); G64_A(6); G64_W(11, 1);
    G64_BAR(12 + E); G64_A(7); G64_W(12, 2);
    G64_BAR(14); G64_A(8); G64_W(13, 3);
    G64_BAR(14); G64_A(9);
    G64_BAR(12); G64_A(10);
    G64_BAR(10); G64_A(11);
    G64_BAR(8); G64_A(12);
    tail();
    G64_BAR(8 + NTAIL); G64_A(13); G64_W(14, 8);
    G64_BAR(10 + NTAIL); G64_A(14); G64_W(15, 9);
    G64_BAR(12 + NTAIL); G64_A(15);
    G64_BAR(12);
    G64_BAR(6);
    WAITVL_BAR(0);
#undef G64_BAR
#undef G64_W
#undef G64_A
}
template <int WB, int AB, class Start, class Mid, class Late, class St>
__device__ __forceinline__ void g64_consume(f32x16& acc, const Ctx& cx, int lane, Start start, Mid mid, Late late, St stamp) {
    const int lo = (lane >> 5) * 256 + (lane & 15) * 16;
    const lds_ptr pa = cx.lds + AB + (cx.w & 3) * 4096 + ((lane >> 4) & 1) * 2048 + lo, pw = cx.lds + (cx.w >> 2) * 2048 + ((lane >> 4) & 1) * 1024 + lo;
    acc = (f32x16){};
    bf16x8 aA[4], wA[4], aB[4], wB[4];
    start();
    BARRIER(); SBAR();
    stamp();
#pragma unroll
    for (int k = 0; k < 4; ++k) { wA[k] = ldsr128(pw + WB + (k >> 1) * 4096 + (k & 1) * 512); aA[k] = ldsr128(pa + k * 512); }
    SBAR();
    int sa = 16384, sw = WB + 8192;
#define G64_STEP(AC, WC, AN, WN, DO_NEXT, HOOK) do { \
        if (DO_NEXT) BARRIER(); SBAR(); HOOK; \
        const lds_ptr pwn = pw + sw, pan = pa + sa; \
        MFMA32_ACC(acc, WC[0], AC[0]); SBAR(); \
        if (DO_NEXT) { WN[0] = ldsr128(pwn); AN[0] = ldsr128(pan); WN[1] = ldsr128(pwn + 512); } SBAR(); \
        MFMA32_ACC(acc, WC[1], AC[1]); SBAR(); \
        if (DO_NEXT) { AN[1] = ldsr128(pan + 512); WN[2] = ldsr128(pwn + 4096); AN[2] = ldsr128(pan + 1024); } SBAR(); \
        MFMA32_ACC(acc, WC[2], AC[2]); SBAR(); \
        if (DO_NEXT) { WN[3] = ldsr128(pwn + 4608); AN[3] = ldsr128(pan + 1536); } SBAR(); \
        MFMA32_ACC(acc, WC[3], AC[3]); SBAR(); \
        sa = sa == 65536 ? 0 : sa + 16384; sw = sw == (WB + 9 * 8192) % 163840 ? WB : (sw == 155648 ? 0 : sw + 8192); } while (0)
#define G64_PAIR() do { G64_STEP(aA, wA, aB, wB, true, (void)0); G64_STEP(aB, wB, aA, wA, true, (void)0); } while (0)
#pragma unroll 1
    for (int t = 0; t < 4; t += 2) G64_PAIR();
    mid(4);
#pragma unroll 1
    for (int t = 4; t < 8; t += 2) G64_PAIR();
    mid(8);
    G64_PAIR();
    mid(10);
    G64_PAIR();
    G64_STEP(aA, wA, aB, wB, true, (void)0);
    sw = (WB + 8 * 8192) % 163840;
    G64_STEP(aB, wB, aA, wA, true, (void)0);
    G64_STEP(aA, wA, aB, wB, true, late());
    G64_STEP(aB, wB, aA, wA, false, (void)0);
    asm volatile("s_nop 7\n\ts_nop 7" : "+v"(acc));
    SBAR();
#undef G64_PAIR
#undef G64_STEP
}

__device__ __forceinline__ void attn_pre(const Ctx& cx) { int rb, hp; tile128x64(cx.bid, rb, hp); g64_pre<0, 6>(cx, cx.ws + WS_WQ + (size_t)hp * 131072, lane_id()); }
template <class Next>
__device__ __forceinline__ void attn_produce(const Ctx& cx, const u16* __restrict__ wo, const u16* __restrict__ wg, const u16* __restrict__ wu, const u16* __restrict__ wd, const u16* __restrict__ c, const u16* __restrict__ wmod, Next next_pre) {
    char* ws = cx.ws; const int pw = cx.w, lane = lane_id();
    int rb, hp; tile128x64(cx.bid, rb, hp);
    const char* kvg = ws + WS_KV + pw * 8192;
    const unsigned voff = lane * 16, kvd = cx.lds0 + pw * 8192;
#define KV_STAGE(st, so) do { _Pragma("unroll") for (int i_ = 0; i_ < 8; ++i_) dmac(kvg + (st) * 32768 + i_ * 1024, voff, kvd + (so) + i_ * 1024); } while (0)
    expect(ws, F_H1 + rb * 16, lane < 16 ? lane : -1, cx.tag);
    if (pw == 0) signal(ws, C_SIDE + (cx.bid & 15), lane);
    g64_produce<0, 81920, 6, 8>(cx, ws + WS_H1 + (size_t)rb * 8 * 32768, ws + WS_WQ + (size_t)hp * 131072, lane,
                    [&]() { BARRIER(); if (pw == 0) signal(ws, C_KV + (cx.bid & 15), lane); },
                    [&]() { }, []() {},
                    [&]() { KV_STAGE(0, 32768); });
    BARRIER();
    KV_STAGE(1, 65536);
    const int ao = (pw & 1) * 16384 + (pw >> 1) * 131072;
    const lds_ptr area = cx.lds + ao; const unsigned area0 = cx.lds0 + ao;
    const int k0 = (cx.bid >> 5) * 128 + pw * 32, col2 = (cx.bid & 15) * 64 + ((cx.bid >> 4) & 1) * 3072;
    const int gw = cx.bid * 4 + pw, isgu = gw < 896, mat = gw >= 448, k8 = (gw - mat * 448) / 56, cg = gw - mat * 448 - k8 * 56;
    u32x4 cw; f32x4 vp[8];
    pack_load((const char*)wmod + (size_t)k0 * 12288 + (2048 + col2) * 2, 12288, area0, lane);
    LD16(cw, c, (unsigned)(k0 + (lane >> 4) * 8) * 2u);
    auto unit_load = [&](int j) {
        const unsigned dst = area0 + 8192 + (j & 1) * 4096;
        if (!isgu) { const int u = (gw - 896) * 4 + (j & 3); pack_load((const char*)wo + (size_t)(u >> 4) * 65536 + (u & 15) * 128, 2048, dst, lane); }
        else if (j < 4) pack_load((const char*)(mat ? wu : wg) + (size_t)(k8 * 4 + j) * 32 * 7168 + cg * 128, 7168, dst, lane);
        else { const int u = (j - 4) * 896 + gw; pack_load((const char*)wd + (size_t)(u >> 4) * 65536 + (u & 15) * 128, 2048, dst, lane); }
    };
    f32x4 ut[4] = {ZERO4, ZERO4, ZERO4, ZERO4};
    unsigned guo[4];
#pragma unroll
    for (int nf = 0; nf < 4; ++nf) {
        const int cj = cg * 8 + 2 * nf + ((lane >> 3) & 1), ct = cj / 14, r = cj - ct * 14, wn = r >= 7, c = r - wn * 7;
        const int fr = c < 6 ? mat * 3 + (c >> 1) : 6, hf = c < 6 ? (c & 1) : mat;
        guo[nf] = (unsigned)((ct * 32 * 14 + wn * 7 + fr) * 1024 + (lane >> 4) * 256 + (hf * 8 + (lane & 7)) * 16);
        PIN(guo[nf]);
    }
    auto unit_store = [&](int j) {
        const lds_ptr src = area + 8192 + (j & 1) * 4096;
        bf16x8 f[4];
#pragma unroll
        for (int nf = 0; nf < 4; ++nf) f[nf] = pack_frag(src, nf, lane);
        if (!isgu || j >= 4) {
            const int u = isgu ? (j - 4) * 896 + gw : (gw - 896) * 4 + (j & 3);
            const char* d = isgu ? ws + WS_WD + (size_t)((((u & 15) >> 1) * 112 + (u >> 4)) * 2 + (u & 1)) * 4096 : (j < 4 ? ws + WS_WO + (size_t)((u & 15) * 32 + (u >> 4)) * 4096 : ws + WS_UT + 458752 + (gw & 15) * 4096);
#pragma unroll
            for (int nf = 0; nf < 4; ++nf) st16(d, voff + nf * 1024, f[nf]);
        } else {
            bf16x8 vf = ldsr128(area + 4096 + (lane & 1) * 256 + j * 64 + (lane >> 4) * 16);
#pragma unroll
            for (int nf = 0; nf < 4; ++nf) {
                st16(ws + WS_WGU + (size_t)(k8 * 4 + j) * 14336, guo[nf], f[nf]);
                ut[nf] = MFMA16(f[nf], vf, ut[nf]);
            }
        }
    };
    {
        const unsigned vo = (unsigned)((lane < 32 ? 2048 : 1024) + (k8 & 7) * 128 + (lane & 31) * 4) * 4u;
#pragma unroll
        for (int i = 0; i < 8; ++i) LD16C(vp[i], ws + WS_MP2, vo + i * 16384);
    }
    unit_load(0);
    WAITV_BAR(17);
    KV_STAGE(2, 98304);
    WAIT_V(8);
    mod_unit(area, silu_frag(cw), area + 5120, lane);
    {
        f32x4 v = rbf4(sum8(vp));
        if (lane < 32) v = rbf4(v + 1.f);
        *(LAS u32x2*)(area + 4096 + lane * 8) = (u32x2){cvtpk(v[0], v[1]), cvtpk(v[2], v[3])};
    }
    unit_store(0); unit_load(1);
    WAITVL_BAR(8);
    KV_STAGE(3, 32768);
    if (pw == 0) {
        const LAS float* r = (const LAS float*)(cx.lds + 5120) + lane;
        st4(ws + WS_MP2, (unsigned)((cx.bid >> 5) * 4096 + col2 + lane) * 4u, (r[0] + r[4096]) + (r[32768] + r[36864]));
    }
    WAIT_V(8); unit_store(1); unit_load(2);
    WAITVL_BAR(8); KV_STAGE(4, 65536); WAIT_V(8); unit_store(2); unit_load(3);
    WAITVL_BAR(8); KV_STAGE(5, 98304); WAIT_V(8); unit_store(3); unit_load(4);
    if (isgu && (lane & 15) < 2) {
#pragma unroll
        for (int nf = 0; nf < 4; ++nf) st16(ws + WS_UT, (unsigned)((k8 * 4 + mat * 2 + (lane & 15)) * F + cg * 64 + nf * 16 + (lane >> 4) * 4) * 4u, ut[nf]);
    }
    WAITVL_BAR(8); KV_STAGE(6, 32768); WAIT_V(8); unit_store(4); unit_load(5);
    WAITVL_BAR(8);
    if (pw == 0) signal(ws, C_EARLY + (cx.bid & 15), lane);
    KV_STAGE(7, 65536); WAIT_V(8); unit_store(5);
    {
        const u32x4 e = {~0u, ~0u, ~0u, ~0u};
#pragma unroll
        for (int i = 0; i < 4; ++i) st16(ws + WS_XCH + (size_t)gw * 4096, voff + i * 1024, e);
    }
    WAITVL_BAR(0);
    if (pw == 0) signal(ws, C_SIDE + (cx.bid & 15), lane);
#undef KV_STAGE
    __builtin_amdgcn_s_setprio(3);
    await<16>(ws, C_EARLY, 16, 16, lane);
    next_pre();
    BARRIER();
    if (pw == 0) raise(ws, F_O + rb * 16 + hp, cx.tag, lane);
}
__device__ __forceinline__ void attn_consume(const Ctx& cx) {
    char* ws = cx.ws; const lds_ptr lds = cx.lds; const int wid = cx.w, lane = lane_id();
    f32x16 qa; f32x2 kn2, vm2; float knm, vmm;
    unsigned cv;
    int rb, hp; tile128x64(cx.bid, rb, hp);
    g64_consume<0, 81920>(qa, cx, lane, []() {}, [&](int t) {
            {
                if (t == 4) asm volatile("s_nop 4\n\tglobal_load_dword %0, %1, %2 sc1" : "=v"(cv) : "v"(wid == 0 ? (lane & 31) * 256 : 0), "s"(ws + WS_CNT) : "memory");
                if (t == 8) { asm volatile("s_waitcnt vmcnt(0)" : "+v"(cv) :: "memory"); if (wid == 0 && !__all(cv >= 16u)) await<6>(ws, C_KV, 32, 16, lane); }
            }
            if (t == 10) asm volatile("s_nop 4\n\tglobal_load_dwordx2 %0, %2, %3 sc1\n\tglobal_load_dwordx2 %1, %2, %3 offset:512 sc1" : "=&v"(kn2), "=&v"(vm2) : "v"(lane * 8), "s"(ws + WS_KN) : "memory"); },
        [&]() { asm volatile("s_waitcnt vmcnt(0)" : "+v"(kn2), "+v"(vm2) :: "memory"); knm = wmax(fmaxf(kn2[0], kn2[1])); vmm = wmax(fmaxf(vm2[0], vm2[1])); PIN(knm); PIN(vmm); SBAR(); },
        [&]() { });
    BAR_L();
    const lds_ptr pl = lds + lane * 16;
    bf16x8 k0 = ldsr128(pl + 32768), k1 = ldsr128(pl + 32768 + 1024), v0 = ldsr128(pl + 32768 + 2048);
    constexpr float QS = 0.17677669529663687f * 1.4426950408889634f;
    u32x4 qw0, qw1;
#pragma unroll
    for (int j = 0; j < 4; ++j) { qw0[j] = cvtpk(qa[2 * j] * QS, qa[2 * j + 1] * QS); qw1[j] = cvtpk(qa[8 + 2 * j] * QS, qa[8 + 2 * j + 1] * QS); }
    const bf16x8 q0 = __builtin_bit_cast(bf16x8, qw0), q1 = __builtin_bit_cast(bf16x8, qw1);
    f32x16 s0 = MFMA32(k0, q0, ((f32x16){})); s0 = MFMA32(k1, q1, s0);
    PIN(s0);
    float qn = 0.f;
#pragma unroll
    for (int j = 0; j < 4; ++j) { qn += bflo(qw0[j]) * bflo(qw0[j]) + bfhi(qw0[j]) * bfhi(qw0[j]) + bflo(qw1[j]) * bflo(qw1[j]) + bfhi(qw1[j]) * bfhi(qw1[j]); }
    qn = half_sum(qn);
    const bool fast = __all((qn * knm <= 88.f * 88.f) && (vmm <= 16777216.f) && (vmm >= 9.5367431640625e-7f));
    f32x16 o = {};
    float linv;
#define EX(x) __builtin_amdgcn_exp2f(x)
#define BF8(x) __builtin_bit_cast(bf16x8, x)
    if (fast) {
        const short ov = ((lane & 15) == ((lane >> 4) & 1)) ? (short)0x3f80 : (short)0;
        const bf16x8 sel = {ov, ov, ov, ov, ov, ov, ov, ov};
        f32x4 ls = ZERO4;
        const f32x16 zero = {};
        f32x16 sA, sB;
        bf16x8 v1 = v0;
        u32x4 p0, p1 = {0u, 0u, 0u, 0u};
        sA = s0;
        k0 = ldsr128(pl + 32768 + 4096); k1 = ldsr128(pl + 32768 + 4096 + 1024);
#define ATT_TILE(SC, SN, PK, PV0, PV1) do { \
            SN = MFMA32(k0, q0, zero); PIN(SN); SBAR(); k0 = ldsr128(PK); \
            SC[0] = EX(SC[0]); SC[1] = EX(SC[1]); SC[2] = EX(SC[2]); SC[3] = EX(SC[3]); PIN(SC); SBAR(); \
            o = MFMA32(v1, BF8(p1), o); PIN(o); SBAR(); v1 = ldsr128(PV1); \
            SC[4] = EX(SC[4]); SC[5] = EX(SC[5]); SC[6] = EX(SC[6]); PIN(SC); SBAR(); \
            ls = MFMA16(sel, BF8(p1), ls); PIN(ls); SBAR(); SC[7] = EX(SC[7]); p0[0] = cvtpk(SC[0], SC[1]); p0[1] = cvtpk(SC[2], SC[3]); PIN(SC); PIN(p0); SBAR(); \
            SN = MFMA32(k1, q1, SN); PIN(SN); SBAR(); k1 = ldsr128((PK) + 1024); \
            p0[2] = cvtpk(SC[4], SC[5]); p0[3] = cvtpk(SC[6], SC[7]); PIN(p0); \
            SC[8] = EX(SC[8]); SC[9] = EX(SC[9]); SC[10] = EX(SC[10]); PIN(SC); SBAR(); \
            o = MFMA32(v0, BF8(p0), o); PIN(o); SBAR(); v0 = ldsr128(PV0); \
            SC[11] = EX(SC[11]); SC[12] = EX(SC[12]); SC[13] = EX(SC[13]); PIN(SC); SBAR(); \
            ls = MFMA16(sel, BF8(p0), ls); PIN(ls); SBAR(); \
            SC[14] = EX(SC[14]); SC[15] = EX(SC[15]); \
            p1[0] = cvtpk(SC[8], SC[9]); p1[1] = cvtpk(SC[10], SC[11]); p1[2] = cvtpk(SC[12], SC[13]); p1[3] = cvtpk(SC[14], SC[15]); PIN(p1); SBAR(); } while (0)
        int so = 32768;
#pragma unroll 1
        for (int st = 0; st < 8; ++st) {
            const int sn = so == 98304 ? 32768 : so + 32768;
            const lds_ptr pc = pl + so, pn = pl + sn;
            __builtin_amdgcn_s_setprio(1);
            ATT_TILE(sA, sB, pc + 2 * 4096, pc + 1 * 4096 + 2048, pc + 0 * 4096 + 3072);
            ATT_TILE(sB, sA, pc + 3 * 4096, pc + 2 * 4096 + 2048, pc + 1 * 4096 + 3072);
            ATT_TILE(sA, sB, pc + 4 * 4096, pc + 3 * 4096 + 2048, pc + 2 * 4096 + 3072);
            ATT_TILE(sB, sA, pc + 5 * 4096, pc + 4 * 4096 + 2048, pc + 3 * 4096 + 3072);
            __builtin_amdgcn_s_setprio(0);
            ATT_TILE(sA, sB, pc + 6 * 4096, pc + 5 * 4096 + 2048, pc + 4 * 4096 + 3072);
            ATT_TILE(sB, sA, pc + 7 * 4096, pc + 6 * 4096 + 2048, pc + 5 * 4096 + 3072);
            if (st < 7) BAR_L();
            if (st < 7) __builtin_amdgcn_s_setprio(2);
            SBAR();
            ATT_TILE(sA, sB, pn, pc + 7 * 4096 + 2048, pc + 6 * 4096 + 3072);
            ATT_TILE(sB, sA, pn + 4096, pn + 2048, pc + 7 * 4096 + 3072);
            so = sn;
        }
        o = MFMA32(v1, BF8(p1), o);
        ls = MFMA16(sel, BF8(p1), ls);
#undef ATT_TILE
        const float la = __shfl(ls[0], lane & 15), lb = __shfl(ls[1], lane & 15);
        linv = 1.f / ((lane & 16) ? lb : la);
    } else {
        float m = -INFINITY, l = 0.f;
        const f32x16 zero = {};
        int so = 32768;
#pragma unroll 1
        for (int st = 0; st < 8; ++st) {
            const lds_ptr pb = pl + so;
#pragma unroll 1
            for (int t = 0; t < 8; ++t) {
                if (t == 6 && st < 7) BAR_L();
                k0 = ldsr128(pb + t * 4096); k1 = ldsr128(pb + t * 4096 + 1024); v0 = ldsr128(pb + t * 4096 + 2048); const bf16x8 v1 = ldsr128(pb + t * 4096 + 3072);
                f32x16 s = MFMA32(k0, q0, zero);
                s = MFMA32(k1, q1, s);
                float tm = s[0];
#pragma unroll
                for (int j = 1; j < 16; ++j) tm = fmaxf(tm, s[j]);
                tm = fmaxf(tm, __shfl_xor(tm, 32));
                const float mn = fmaxf(m, tm), al = EX(m - mn);
                m = mn; l *= al;
#pragma unroll
                for (int j = 0; j < 16; ++j) o[j] *= al;
                u32x4 p0, p1;
#pragma unroll
                for (int j = 0; j < 4; ++j) {
                    p0[j] = cvtpk(EX(s[2 * j] - mn), EX(s[2 * j + 1] - mn));
                    p1[j] = cvtpk(EX(s[8 + 2 * j] - mn), EX(s[8 + 2 * j + 1] - mn));
                    l += (bflo(p0[j]) + bfhi(p0[j])) + (bflo(p1[j]) + bfhi(p1[j]));
                }
                o = MFMA32(v0, BF8(p0), o);
                o = MFMA32(v1, BF8(p1), o);
            }
            so = so == 98304 ? 32768 : so + 32768;
        }
        linv = 1.f / half_sum(l);
    }
#undef EX
#undef BF8
    {
        const char* ob = ws + WS_O + ((size_t)(rb * 8 + (wid & 3) * 2) * 128 + (hp * 2 + (wid >> 2)) * 4) * 256;
        const unsigned vo = (unsigned)(((lane >> 4) & 1) * 32768 + (lane >> 5) * 256 + (lane & 15) * 16);
#pragma unroll
        for (int gp = 0; gp < 2; ++gp) {
            const auto r0 = __builtin_amdgcn_permlane32_swap(cvtpk(o[8 * gp] * linv, o[8 * gp + 1] * linv), cvtpk(o[8 * gp + 4] * linv, o[8 * gp + 5] * linv), false, false);
            const auto r1 = __builtin_amdgcn_permlane32_swap(cvtpk(o[8 * gp + 2] * linv, o[8 * gp + 3] * linv), cvtpk(o[8 * gp + 6] * linv, o[8 * gp + 7] * linv), false, false);
            st16(ob, vo + gp * 512, ((u32x4){r0[0], r1[0], r0[1], r1[1]}));
        }
    }
    WAITVL_BAR(0);
}

constexpr int O_STAT = 159744;
struct OPre { f32x4 p[8]; };
__device__ __forceinline__ void oproj_pre(const Ctx& cx, OPre& o) {
    const int lane = lane_id(); int rb, ct; tile128x64(cx.bid, rb, ct);
    const unsigned vo = (unsigned)(((lane >> 4) & 1) * 2048 + ct * 64 + (lane & 15) * 4) * 4u;
#pragma unroll
    for (int i = 0; i < 8; ++i) LD16C(o.p[i], cx.ws + WS_MP2, vo + i * 16384);
    g64_pre<98304, 10>(cx, cx.ws + WS_WO + (size_t)ct * 131072, lane);
}
template <class Next>
__device__ __forceinline__ void oproj_produce(const Ctx& cx, OPre& o, const u16* __restrict__ x, Next next_pre) {
    char* ws = cx.ws; const int pw = cx.w, lane = lane_id();
    int rb, ct; tile128x64(cx.bid, rb, ct);
    if (pw == 0) expect(ws, F_O + rb * 16, lane < 5 ? lane : -1, cx.tag);
    INPUT_BARRIER();
    f32x4 tv;
    g64_produce<98304, 16384, 10, 4>(cx, ws + WS_O + (size_t)rb * 8 * 32768, ws + WS_WO + (size_t)ct * 131072, lane, []() {},
        [&]() { tv = rbf4(sum8(o.p)); if (lane & 16) tv = rbf4(tv + 1.f); PIN(tv); },
        [&]() { if (pw == 0) expect(ws, F_O + rb * 16, lane < 16 ? lane : -1, cx.tag); },
        [&]() {
            const unsigned voffX = (unsigned)((rb * 128 + pw * 32 + (lane >> 3)) * 2048 + ct * 128 + (((lane & 7) ^ ((lane >> 3) & 7)) << 4));
#pragma unroll

            for (int i = 0; i < 4; ++i) dmant((const char*)x + i * 8 * 2048, voffX,
 cx.lds0 + 131072 + pw * 4096 + i * 1024);
            if (pw == 0 && lane < 32) *(LAS f32x4*)(cx.lds + 147456 + lane * 16) = tv; });
    BARRIER();
    if (pw == 0 && lane == 0) *(LAS unsigned*)cx.lds = 0u;
    next_pre();
    BAR_L();
}
__device__ __forceinline__ void oproj_consume(const Ctx& cx) {
    char* ws = cx.ws; const lds_ptr lds = cx.lds; const int wid = cx.w, lane = lane_id();
    f32x16 acc;
    int rb, ct; tile128x64(cx.bid, rb, ct);
    g64_consume<98304, 16384>(acc, cx, lane, [&]() { INPUT_BARRIER(); }, [](int) {}, []() {}, [&]() { });
    BAR_L();
    const int tb = wid & 3, nh = wid >> 2, h = lane >> 5, rl = tb * 32 + (lane & 31);
    const unsigned so = (unsigned)(((lane >> 4) & 1) * 32768 + h * 256 + (lane & 15) * 16);
    u32x4 x1v[2]; float v[16];
#pragma unroll
    for (int gp = 0; gp < 2; ++gp) {
        unsigned u[2][2], y[2][2];
#pragma unroll
        for (int k = 0; k < 2; ++k) {
            const int G = 2 * gp + k;
            const f32x4 ga = *(const LAS f32x4*)(lds + 147456 + (nh * 8 + G * 2 + h) * 16), cb = *(const LAS f32x4*)(lds + 147456 + 256 + (nh * 8 + G * 2 + h) * 16);
            const u32x2 xv = *(const LAS u32x2*)(lds + 131072 + rl * 128 + (((nh * 4 + G) ^ (rl & 7)) << 4) + 8 * h);
            u[k][0] = cvtpk(bflo(xv[0]) + ga[0] * acc[4 * G], bfhi(xv[0]) + ga[1] * acc[4 * G + 1]);
            u[k][1] = cvtpk(bflo(xv[1]) + ga[2] * acc[4 * G + 2], bfhi(xv[1]) + ga[3] * acc[4 * G + 3]);
            v[4 * G] = bflo(u[k][0]); v[4 * G + 1] = bfhi(u[k][0]); v[4 * G + 2] = bflo(u[k][1]); v[4 * G + 3] = bfhi(u[k][1]);
            y[k][0] = cvtpk(v[4 * G] * cb[0], v[4 * G + 1] * cb[1]); y[k][1] = cvtpk(v[4 * G + 2] * cb[2], v[4 * G + 3] * cb[3]);
        }
        st16(ws + WS_Y + ((size_t)(rb * 8 + tb * 2) * 128 + ct * 8 + nh * 4) * 256, so + gp * 512, swap32(y[0][0], y[0][1], y[1][0], y[1][1]));
        x1v[gp] = swap32(u[0][0], u[0][1], u[1][0], u[1][1]);
    }
    BAR_L();
    {
        float sm = 0.f;
#pragma unroll
        for (int j = 0; j < 16; ++j) sm += v[j];
        const float mean = half_sum(sm) * (1.f / 32.f);
        float q = 0.f;
#pragma unroll
        for (int j = 0; j < 16; ++j) q += (v[j] - mean) * (v[j] - mean);
        q = half_sum(q);
        if (h == 0) *(LAS f32x2*)(lds + O_STAT + (rl * 2 + nh) * 8) = (f32x2){mean, q};
    }
    drain_raise(ws, lds, F_Y + rb * 16 + ct, cx.tag, lane);
#pragma unroll
    for (int gp = 0; gp < 2; ++gp) st16(ws + WS_X1 + ((size_t)(rb * 8 + tb * 2) * 128 + ct * 8 + nh * 4) * 256, so + gp * 512, x1v[gp]);
}

constexpr int GSL = 30720, G_ROW = 5 * GSL + 1024, G_COL = G_ROW + 2048;
__device__ __forceinline__ void tile_glu(int bid, int& rp, int& ct) { const int xcd = bid & 7, idx = bid >> 3; rp = (xcd & 1) * 4 + (idx & 3); ct = (xcd >> 1) * 8 + (idx >> 2); }
#define GLU_W(t, so) do { const char* w_ = Wb + (size_t)(t) * 14336; const unsigned d2_ = pw < 3 ? dW + (so) + 2048 : cx.lds0 + 5 * GSL; \
        dmac(w_, voff, dW + (so)); dmac(w_ + 1024, voff, dW + (so) + 1024); dmac(w_, voff2, d2_); dmac(w_ + 1024, voff2, pw < 3 ? d2_ + 1024 : d2_); } while (0)
__device__ __forceinline__ void glu_pre(const Ctx& cx) {
    const int pw = cx.w, lane = lane_id(); int rp, ct; tile_glu(cx.bid, rp, ct);
    const char* Wb = cx.ws + WS_WGU + (size_t)ct * 32 * 14336 + pw * 4096;
    const unsigned voff = lane * 16, dW = cx.lds0 + 16384 + pw * 4096;
#pragma unroll
    for (int t = 0; t < 4; ++t) {
        dmac(Wb + t * 14336, voff, dW + t * GSL); dmac(Wb + t * 14336 + 1024, voff, dW + t * GSL + 1024);
        if (pw < 3) { dmac(Wb + t * 14336 + 2048, voff, dW + t * GSL + 2048); dmac(Wb + t * 14336 + 3072, voff, dW + t * GSL + 3072); }
    }
}
template <class Next>
__device__ __forceinline__ void glu_produce(const Ctx& cx, Next next_pre) {
    char* ws = cx.ws; const int pw = cx.w, lane = lane_id(); int rp, ct; tile_glu(cx.bid, rp, ct);
    const char* Ab = ws + WS_Y + (size_t)(rp * 16 + 4 * pw) * 32768;
    const char* Wb = ws + WS_WGU + (size_t)ct * 32 * 14336 + pw * 4096;
    const unsigned voff = lane * 16, voff2 = pw < 3 ? lane * 16 + 2048 : 0u, dA = cx.lds0 + pw * 4096, dW = cx.lds0 + 16384 + pw * 4096;
#define GLU_A(t, so) do { const char* a_ = Ab + (size_t)(t) * 1024; dmac(a_, voff, dA + (so)); dmac(a_ + 32768, voff, dA + (so) + 1024); dmac(a_ + 65536, voff, dA + (so) + 2048); dmac(a_ + 98304, voff, dA + (so) + 3072); } while (0)
    if (pw == 0) expect(ws, F_Y + rp * 32, lane < 32 && (lane & 15) < 2 ? lane : -1, cx.tag);
    INPUT_BARRIER();
    GLU_A(0, 0); GLU_A(1, GSL);
    WAITV_BAR(4);
    GLU_A(2, 2 * GSL); GLU_A(3, 3 * GSL);
    WAITV_BAR(8);
    if (pw == 0) expect(ws, F_Y + rp * 32, lane < 32 ? lane : -1, cx.tag);
    WAITV_BAR(4); GLU_W(4, 4 * GSL); GLU_A(4, 4 * GSL); GLU_W(5, 0); GLU_A(5, 0);
    int sf = GSL;
#define GLU_NEXT(t, VM) do { WAITV_BAR(VM); GLU_W((t) + 4, sf); GLU_A((t) + 4, sf); sf = sf == 4 * GSL ? 0 : sf + GSL; } while (0)
    GLU_NEXT(2, 16); GLU_NEXT(3, 16);
    if (pw == 0) { int rbo, cto; tile128x64(cx.bid, rbo, cto); signal(ws, C_ST + (rbo >> 1), lane); }
    GLU_NEXT(4, 16); GLU_NEXT(5, 16);
    {
        unsigned cv;
        asm volatile("s_nop 4\n\tglobal_load_dword %0, %1, %2 sc1" : "=v"(cv) : "v"((C_ST + rp) * 256), "s"(ws + WS_CNT) : "memory");
        GLU_NEXT(6, 17); GLU_NEXT(7, 17); GLU_NEXT(8, 17); GLU_NEXT(9, 16);
        PIN(cv);
        if (!__all(cv >= 32u)) await<4>(ws, C_ST + rp, 1, 32, lane);
    }
    {
        f32x2 sp[16]; f32x4 cp[8];
        const char* sb_ = ws + WS_ST2 + (size_t)(rp * 256 + pw * 64) * 8;
#pragma unroll
        for (int i = 0; i < 8; ++i) LD8C(sp[i], sb_, (unsigned)(lane * 8 + i * 16384));
        GLU_NEXT(10, 24);
#pragma unroll
        for (int i = 8; i < 16; ++i) LD8C(sp[i], sb_, (unsigned)(lane * 8 + i * 16384));
        GLU_NEXT(11, 32);
        int i4 = pw * 64 + lane; i4 = i4 < 112 ? i4 : 0;
        const unsigned vo = (unsigned)((i4 / 28) * F + ct * 112 + (i4 % 28) * 4) * 4u;
#pragma unroll
        for (int i = 0; i < 8; ++i) LD16C(cp[i], ws + WS_UT, vo + i * (4 * F * 4));
        GLU_NEXT(12, 40); GLU_NEXT(13, 32); GLU_NEXT(14, 24); GLU_NEXT(15, 16);
        __builtin_amdgcn_s_setprio(0);
        float mean = 0.f, q = 0.f;
#pragma unroll
        for (int i = 0; i < 16; ++i) { PIN(sp[i]); mean += sp[i][0]; q += sp[i][1]; }
        mean *= 1.f / 16.f; PIN(mean); PIN(q);
        GLU_NEXT(16, 16);
        float dq = 0.f;
#pragma unroll
        for (int i = 0; i < 16; ++i) dq += (sp[i][0] - mean) * (sp[i][0] - mean);
        PIN(dq);
        GLU_NEXT(17, 16);
        const float rstd = 1.f / sqrtf((q + 64.f * dq) * (1.f / 1024.f) + 1e-5f);
        *(LAS f32x2*)(cx.lds + G_ROW + (pw * 64 + lane) * 8) = (f32x2){rstd, -mean * rstd};
        GLU_NEXT(18, 16);
        const f32x4 c = sum8(cp);
        if (pw * 64 + lane < 112) *(LAS f32x4*)(cx.lds + G_COL + (pw * 64 + lane) * 16) = c;
        __builtin_amdgcn_s_setprio(3);
        GLU_NEXT(19, 16);
    }
    {
        unsigned cv;
        int rbd, ctd, khd; tile_down(cx.bid, rbd, ctd, khd);
        asm volatile("s_nop 4\n\tglobal_load_dword %0, %1, %2 sc1" : "=v"(cv) : "v"((lane < 16 ? C_SIDE + lane : C_ST + (rbd >> 1)) * 256), "s"(ws + WS_CNT) : "memory");
        GLU_NEXT(20, 17); GLU_NEXT(21, 17); GLU_NEXT(22, 17); GLU_NEXT(23, 16);
        PIN(cv);
        if (!__all(cv >= 32u)) { await<4>(ws, C_SIDE, 16, 32, lane); await<4>(ws, C_ST + (rbd >> 1), 1, 32, lane); }
    }
#pragma unroll 1
    for (int t = 24; t < 28; ++t) GLU_NEXT(t, 16);
    WAITV_BAR(16); WAITV_BAR(8); WAITVL_BAR(0);
#undef GLU_NEXT
#undef GLU_A
    if (pw == 0 && lane == 0) *(LAS unsigned*)(cx.lds + G_COL + 1920) = 0u;
    BARRIER();
    next_pre();
    BAR_L();
}
#undef GLU_W
__device__ __forceinline__ void glu_consume(const Ctx& cx) {
    char* ws = cx.ws; const lds_ptr lds = cx.lds; const int wm = cx.w & 3, wn = cx.w >> 2;
    f32x4 acc[4][7];
    {
        const int lane = lane_id();
        const lds_ptr pa = lds + wm * 4096 + lane * 16, pw = lds + 16384 + wn * 7168 + lane * 16;
        bf16x8 af[4], wf[4];
#pragma unroll
        for (int n = 0; n < 7; ++n)
#pragma unroll
            for (int m = 0; m < 4; ++m) acc[m][n] = ZERO4;
        INPUT_BARRIER();
        if (cx.w == 1 || cx.w == 2) {
            int rbo, cto; tile128x64(cx.bid, rbo, cto);
            const int rl = (cx.w - 1) * 64 + lane;
            const f32x4 s = *(const LAS f32x4*)(lds + O_STAT + rl * 16);
            const float dm = s[0] - s[2];
            st8(ws + WS_ST2 + (size_t)(cto * 2048 + rbo * 128) * 8, (unsigned)rl * 8u, ((f32x2){0.5f * (s[0] + s[2]), s[1] + s[3] + 16.f * dm * dm}));
        }
        BARRIER();
#pragma unroll
        for (int m = 0; m < 4; ++m) { af[m] = ldsr128(pa + m * 1024); wf[m] = ldsr128(pw + m * 1024); }
        int sc = 0, sn = GSL;
#define LAD(p) __builtin_amdgcn_s_setprio(p)
#define GW(U, n) wf[(7 * (U) + (n)) & 3]
#define GLU_STEP(U, DO_NEXT) do { \
            if ((U) == 3) WAIT_V(0);   if (DO_NEXT) BARRIER(); LAD(1); SBAR(); \
            const lds_ptr pwc = pw + sc, pwn = pw + sn, pan = pa + sn; \
            _Pragma("unroll") for (int m = 0; m < 4; ++m) { MFMA16_ACC(acc[m][0], GW(U, 0), af[m]); MFMA16_ACC(acc[m][1], GW(U, 1), af[m]); } \
            SBAR(); GW(U, 4) = ldsr128(pwc + 4096); GW(U, 5) = ldsr128(pwc + 5120); SBAR(); \
            _Pragma("unroll") for (int m = 0; m < 4; ++m) MFMA16_ACC(acc[m][2], GW(U, 2), af[m]); \
            SBAR(); GW(U, 6) = ldsr128(pwc + 6144); SBAR(); \
            _Pragma("unroll") for (int m = 0; m < 4; ++m) MFMA16_ACC(acc[m][3], GW(U, 3), af[m]); \
            SBAR(); if (DO_NEXT) GW(U, 7) = ldsr128(pwn); SBAR(); \
            _Pragma("unroll") for (int m = 0; m < 4; ++m) MFMA16_ACC(acc[m][4], GW(U, 4), af[m]); \
            SBAR(); if (DO_NEXT) GW(U, 8) = ldsr128(pwn + 1024); SBAR(); \
            _Pragma("unroll") for (int m = 0; m < 4; ++m) { MFMA16_ACC(acc[m][5], GW(U, 5), af[m]); MFMA16_ACC(acc[m][6], GW(U, 6), af[m]); \
                SBAR(); if (DO_NEXT) af[m] = ldsr128(pan + m * 1024); if (m == 1) LAD(0); SBAR(); } \
            if (DO_NEXT) { GW(U, 9) = ldsr128(pwn + 2048); GW(U, 10) = ldsr128(pwn + 3072); } SBAR(); \
            sc = sn; sn = sn == 4 * GSL ? 0 : sn + GSL; } while (0)
#pragma unroll 1
        for (int t = 0; t < 28; t += 4) { GLU_STEP(0, true); GLU_STEP(1, true); GLU_STEP(2, true); GLU_STEP(3, true); }
        GLU_STEP(0, true); GLU_STEP(1, true); GLU_STEP(2, true); GLU_STEP(3, false);
#undef GLU_STEP
#undef GW
    }
    asm volatile("s_barrier\n\ts_nop 7\n\ts_nop 7" : "+v"(acc[0][6]), "+v"(acc[1][6]), "+v"(acc[2][6]), "+v"(acc[3][6]), "+v"(acc[0][5]), "+v"(acc[1][5]), "+v"(acc[2][5]), "+v"(acc[3][5]) :: "memory");
    SBAR();
    const int lane = lane_id(), g = lane >> 4, gc = ((g & 1) << 1) | (g >> 1);
    int rp, ct; tile_glu(cx.bid, rp, ct);
    const char* hb = ws + WS_HH + ((size_t)(rp * 16 + wm * 4) * (F / 8) + ct * 14 + wn * 7) * 256;
    const unsigned vo = (lane & 15) * 16;
    constexpr unsigned RB = (F / 8) * 256;
    f32x2 rw[4];
#pragma unroll
    for (int m = 0; m < 4; ++m) rw[m] = *(const LAS f32x2*)(lds + G_ROW + (wm * 64 + m * 16 + (lane & 15)) * 8);
#define GLU_OUT(m, f, ug, tg, uu, tu, d0, d1) do { const f32x4 gt = acc[m][f] * rw[m][0] + (ug * rw[m][1] + tg), up = acc[m][3 + (f)] * rw[m][0] + (uu * rw[m][1] + tu); \
        d0 = cvtpk(fmaxf(gt[0], 0.f) * up[0], fmaxf(gt[1], 0.f) * up[1]); d1 = cvtpk(fmaxf(gt[2], 0.f) * up[2], fmaxf(gt[3], 0.f) * up[3]); } while (0)
    {
        const lds_ptr pc = lds + G_COL + (wn * 14 + g) * 16;
        f32x4 ct_[3][4];
#pragma unroll
        for (int f = 0; f < 2; ++f)
#pragma unroll
            for (int k = 0; k < 4; ++k) ct_[f][k] = *(const LAS f32x4*)(pc + f * 64 + k * 448);
#pragma unroll
        for (int m = 0; m < 4; ++m) {
            unsigned a0, a1, b0, b1;
            GLU_OUT(m, 0, ct_[0][0], ct_[0][1], ct_[0][2], ct_[0][3], a0, a1); GLU_OUT(m, 1, ct_[1][0], ct_[1][1], ct_[1][2], ct_[1][3], b0, b1);
            st16(hb, vo + m * RB + gc * 256, swap16(a0, a1, b0, b1));
            if (m == 1) { _Pragma("unroll") for (int k = 0; k < 4; ++k) ct_[2][k] = *(const LAS f32x4*)(pc + 128 + k * 448); }
            SBAR();
        }
#pragma unroll
        for (int mp = 0; mp < 2; ++mp) {
            unsigned a0, a1, b0, b1;
            GLU_OUT(2 * mp, 2, ct_[2][0], ct_[2][1], ct_[2][2], ct_[2][3], a0, a1); GLU_OUT(2 * mp + 1, 2, ct_[2][0], ct_[2][1], ct_[2][2], ct_[2][3], b0, b1);
            st16(hb, vo + (2 * mp + (g & 1)) * RB + (4 + (g >> 1)) * 256, swap16(a0, a1, b0, b1));
            SBAR();
        }
    }
#undef GLU_OUT
    {
        const lds_ptr pc = lds + G_COL + (g >> 1) * 896 + (wn * 14 + 12 + (g & 1)) * 16;
        const f32x4 uc = *(const LAS f32x4*)pc, tc = *(const LAS f32x4*)(pc + 448);
        unsigned ux[2][2];
#pragma unroll
        for (int mp = 0; mp < 2; ++mp) {
            const f32x4 a = acc[2 * mp][6] * rw[2 * mp][0] + (uc * rw[2 * mp][1] + tc), b = acc[2 * mp + 1][6] * rw[2 * mp + 1][0] + (uc * rw[2 * mp + 1][1] + tc);
            float hv[4];
#pragma unroll
            for (int r = 0; r < 4; ++r) {
                const auto x = __builtin_amdgcn_permlane32_swap(__float_as_uint(a[r]), __float_as_uint(b[r]), false, false);
                hv[r] = fmaxf(__uint_as_float(x[0]), 0.f) * __uint_as_float(x[1]);
            }
            ux[mp][0] = cvtpk(hv[0], hv[1]); ux[mp][1] = cvtpk(hv[2], hv[3]);
        }
        st16(hb, vo + gc * RB + 6 * 256, swap16(ux[0][0], ux[0][1], ux[1][0], ux[1][1]));
    }
    BARRIER();
    drain_raise(ws, lds + G_COL + 1920, F_HH + rp * 32 + ct, cx.tag, lane);
}

#define DN_W(t, so) do { const char* w_ = Wb + (size_t)(t) * 16384; dmac(w_, voff, dW + (so)); dmac(w_ + 1024, voff, dW + (so) + 1024); dmac(w_ + 2048, voff, dW + (so) + 2048); dmac(w_ + 3072, voff, dW + (so) + 3072); } while (0)
__device__ __forceinline__ void down_pre(const Ctx& cx, OPre& o) {
    const int pw = cx.w, lane = lane_id(); int rb, ct, kh; tile_down(cx.bid, rb, ct, kh);
    const unsigned vo = (unsigned)(3072 + ct * 128 + (lane & 31) * 4) * 4u;
#pragma unroll
    for (int i = 0; i < 8; ++i) LD16C(o.p[i], cx.ws + WS_MP2, vo + i * 16384);
    const char* Wb = cx.ws + WS_WD + (size_t)(ct * 112 + kh * 56) * 8192 + pw * 4096;
    const unsigned voff = lane * 16, dW = cx.lds0 + 16384 + pw * 4096;
#pragma unroll
    for (int t = 0; t < 4; ++t) DN_W(t, t * 32768);
}
__device__ __forceinline__ void down_produce(const Ctx& cx, OPre& o) {
    char* ws = cx.ws; const int pw = cx.w, lane = lane_id(); int rb, ct, kh; tile_down(cx.bid, rb, ct, kh);
    const char* Ab = ws + WS_HH + ((size_t)(rb * 8 + 2 * pw) * (F / 8) + kh * 224) * 256;
    const char* Wb = ws + WS_WD + (size_t)(ct * 112 + kh * 56) * 8192 + pw * 4096;
    const unsigned voff = lane * 16, dA = cx.lds0 + pw * 4096, dW = cx.lds0 + 16384 + pw * 4096;
#define DN_A(t, so) do { const char* a_ = Ab + (size_t)(t) * 2048; dmac(a_, voff, dA + (so)); dmac(a_ + 1024, voff, dA + (so) + 1024); \
        dmac(a_ + (F / 8) * 256, voff, dA + (so) + 2048); dmac(a_ + (F / 8) * 256 + 1024, voff, dA + (so) + 3072); } while (0)
    if (pw == 0) expect(ws, F_HH + (rb >> 1) * 32 + kh * 16, lane < 3 ? lane : -1, cx.tag);
    INPUT_BARRIER();
    DN_A(0, 0); DN_A(1, 32768);
    WAITV_BAR(4);
    DN_A(2, 65536); DN_A(3, 98304);
    WAITV_BAR(8);
    DN_W(4, 131072);
    if (pw == 0) expect(ws, F_HH + (rb >> 1) * 32 + kh * 16, lane < 16 ? lane : -1, cx.tag);
    f32x4 tv = rbf4(sum8(o.p)); PIN(tv);
    WAITV_BAR(8); DN_A(4, 131072); DN_W(5, 0); DN_A(5, 0);
    int sf = 32768;
#pragma unroll 1
    for (int t = 2; t < 24; ++t) {
        WAITV_BAR(16);
        DN_W(t + 4, sf); DN_A(t + 4, sf);
        sf = sf == 131072 ? 0 : sf + 32768;
    }
    WAITV_BAR(16);
    {
        const char* xb = ws + WS_X1 + ((size_t)(rb * 8 + (pw >> 1) * 4 + kh * 2 + (pw & 1)) * 128 + ct * 16) * 256;
#pragma unroll
        for (int i = 0; i < 4; ++i) dmac(xb + i * 1024, voff, cx.lds0 + 98304 + pw * 4096 + i * 1024);
        if (pw == 0 && lane < 32) *(LAS f32x4*)(cx.lds + 114688 + lane * 16) = tv;
    }
    WAITV_BAR(12); WAITV_BAR(4); WAITVL_BAR(0);
#undef DN_A
}
#undef DN_W
__device__ __forceinline__ void down_consume(const Ctx& cx, u16* __restrict__ out) {
    char* ws = cx.ws; const lds_ptr lds = cx.lds; const int wid = cx.w, lane = lane_id(), wm = wid >> 2, wn = wid & 3, g = lane >> 4;
    const lds_ptr pa = lds + wm * 8192 + lane * 16, pw = lds + 16384 + wn * 2048 + lane * 16;
    f32x4 acc[4][2];
#pragma unroll
    for (int m = 0; m < 4; ++m) { acc[m][0] = ZERO4; acc[m][1] = ZERO4; }
    bf16x8 aA[8], wA[4], aB[8], wB[4];
    INPUT_BARRIER();
    BARRIER(); SBAR();
#pragma unroll
    for (int ks = 0; ks < 2; ++ks) {
        wA[2 * ks] = ldsr128(pw + ks * 8192); wA[2 * ks + 1] = ldsr128(pw + ks * 8192 + 1024);
#pragma unroll
        for (int m = 0; m < 4; ++m) aA[4 * ks + m] = ldsr128(pa + m * 2048 + ks * 1024);
    }
    SBAR();
    int sn = 32768;
#define DN_STEP(AC, WC, AN, WN, DO_NEXT) do { \
        if (DO_NEXT) BARRIER(); LAD(1); SBAR(); \
        const lds_ptr pan = pa + sn, pwn = pw + sn; \
        _Pragma("unroll") for (int i = 0; i < 8; ++i) { const int ks = i >> 2, m = i & 3; \
            MFMA16_ACC(acc[m][0], WC[2 * ks], AC[i]); MFMA16_ACC(acc[m][1], WC[2 * ks + 1], AC[i]); SBAR(); \
            if (DO_NEXT) { if (m < 2) WN[2 * ks + m] = ldsr128(pwn + ks * 8192 + m * 1024); AN[i] = ldsr128(pan + m * 2048 + ks * 1024); } \
            if (i == 5) LAD(0); \
            SBAR(); } \
        sn = sn == 131072 ? 0 : sn + 32768; } while (0)
#pragma unroll 1
    for (int t = 0; t < 26; t += 2) {
        DN_STEP(aA, wA, aB, wB, true); DN_STEP(aB, wB, aA, wA, true);
    }
    DN_STEP(aA, wA, aB, wB, true); DN_STEP(aB, wB, aA, wA, false);
#undef DN_STEP
    asm volatile("s_nop 7\n\ts_nop 7" : "+v"(acc[0][0]), "+v"(acc[0][1]), "+v"(acc[1][0]), "+v"(acc[1][1]), "+v"(acc[2][0]), "+v"(acc[2][1]), "+v"(acc[3][0]), "+v"(acc[3][1]));
    SBAR();
    int rb, ct, kh; tile_down(cx.bid, rb, ct, kh);
    f32x4 keep[2][2];
#pragma unroll
    for (int mm = 0; mm < 2; ++mm) {
        const f32x4 g0 = kh ? acc[mm][0] : acc[2 + mm][0], g1 = kh ? acc[mm][1] : acc[2 + mm][1];
        st16(ws + WS_XCH + ((size_t)cx.bid * 8 + wid) * 2048, lane * 16 + mm * 1024, ((u32x4){cvtpk(g0[0], g0[1]), cvtpk(g0[2], g0[3]), cvtpk(g1[0], g1[1]), cvtpk(g1[2], g1[3])}));
        keep[mm][0] = kh ? acc[2 + mm][0] : acc[mm][0]; keep[mm][1] = kh ? acc[2 + mm][1] : acc[mm][1];
    }
    BARRIER();
    u32x2 xr[2][2]; f32x4 gb[2];
#pragma unroll
    for (int nf = 0; nf < 2; ++nf) {
        gb[nf] = *(const LAS f32x4*)(lds + 114688 + (wn * 8 + nf * 4 + g) * 16);
#pragma unroll
        for (int mm = 0; mm < 2; ++mm) xr[mm][nf] = *(const LAS u32x2*)(lds + 98304 + ((wm * 2 + mm) * 16 + wn * 4 + nf * 2 + (g >> 1)) * 256 + (lane & 15) * 16 + 8 * (g & 1));
    }
    u32x4 got[2];
    {
        const char* xi = ws + WS_XCH + ((size_t)(cx.bid ^ 1) * 8 + wid) * 2048;
        for (unsigned spins = 0; spins < (1u << 15); ++spins) {
            asm volatile("s_nop 4\n\tglobal_load_dwordx4 %0, %2, %3 sc1\n\tglobal_load_dwordx4 %1, %2, %3 offset:1024 sc1\n\ts_waitcnt vmcnt(0)" : "=&v"(got[0]), "=&v"(got[1]) : "v"(lane * 16), "s"(xi) : "memory");
            const bool ok = got[0][0] != ~0u && got[0][1] != ~0u && got[0][2] != ~0u && got[0][3] != ~0u && got[1][0] != ~0u && got[1][1] != ~0u && got[1][2] != ~0u && got[1][3] != ~0u;
            if (__all(ok)) break;
            __builtin_amdgcn_s_sleep(2);
        }
    }
#pragma unroll
    for (int mm = 0; mm < 2; ++mm) {
        unsigned u[2][2];
#pragma unroll
        for (int nf = 0; nf < 2; ++nf) {
            const f32x4 gg = gb[nf], k = keep[mm][nf];
            u[nf][0] = cvtpk(bflo(xr[mm][nf][0]) + gg[0] * (k[0] + bflo(got[mm][2 * nf])), bfhi(xr[mm][nf][0]) + gg[1] * (k[1] + bfhi(got[mm][2 * nf])));
            u[nf][1] = cvtpk(bflo(xr[mm][nf][1]) + gg[2] * (k[2] + bflo(got[mm][2 * nf + 1])), bfhi(xr[mm][nf][1]) + gg[3] * (k[3] + bfhi(got[mm][2 * nf + 1])));
        }
        st16o(out + (size_t)(rb * 128 + wm * 64 + (2 * kh + mm) * 16) * D + ct * 128 + wn * 32, (unsigned)((lane & 15) * 2048 + (((g & 1) << 1) | (g >> 1)) * 16), swap16(u[0][0], u[0][1], u[1][0], u[1][1]));
    }
}

#define CTX_INIT extern __shared__ __attribute__((aligned(16))) char smem[]; \
    const int wid_ = uni(threadIdx.x >> 6); const bool prod = wid_ >= 8; \
    Ctx cx = {ws, (lds_ptr)smem, uni((unsigned)(uintptr_t)(lds_ptr)smem), (int)blockIdx.x, prod ? wid_ - 8 : wid_, 0ull}; \
    if (prod) __builtin_amdgcn_s_setprio(3)
__global__ __launch_bounds__(768, 3) void k_main(const u16* __restrict__ x, const u16* __restrict__ wq, const u16* __restrict__ wk, const u16* __restrict__ wv, const u16* __restrict__ wo,
                                                 const u16* __restrict__ wg, const u16* __restrict__ wu, const u16* __restrict__ wd, const u16* __restrict__ c, const u16* __restrict__ wmod,
                                                 char* __restrict__ ws, u16* __restrict__ out) {
    CTX_INIT;
    if (prod) {
        OPre o;
        ln1kv_produce(cx, wq, wk, wv, c, wmod, [&]() { attn_pre(cx); });
        attn_produce(cx, wo, wg, wu, wd, c, wmod, [&]() { oproj_pre(cx, o); });
        oproj_produce(cx, o, x, [&]() { glu_pre(cx); });
        glu_produce(cx, [&]() { down_pre(cx, o); });
        down_produce(cx, o);
    } else {
        ln1kv_consume(cx, x, c, wmod);
        attn_consume(cx);
        oproj_consume(cx);
        glu_consume(cx);
        down_consume(cx, out);
    }
}

extern "C" void kernel_launch(void* const* d_in, const int* in_sizes, int n_in, void* d_out, int out_size, void* d_ws, size_t ws_size, hipStream_t stream) {
    constexpr int LDS = 163840;
    (void)hipFuncSetAttribute((const void*)k_main, hipFuncAttributeMaxDynamicSharedMemorySize, LDS);
    k_main<<<256, 768, LDS, stream>>>((const u16*)d_in[0], (const u16*)d_in[1], (const u16*)d_in[2], (const u16*)d_in[3], (const u16*)d_in[4], (const u16*)d_in[6], (const u16*)d_in[7],
                                      (const u16*)d_in[8], (const u16*)d_in[9], (const u16*)d_in[10], (char*)d_ws, (u16*)d_out);
}
```
